```python
import torch

def _softmax(x, axis=-1):
    return torch.softmax(x, dim=axis)

def _gelu(x):
    return torch.nn.functional.gelu(x, approximate='tanh')

def _silu(x):
    return torch.nn.functional.silu(x)

def _sigmoid(x):
    return torch.sigmoid(x)

def _layernorm(x, axis=-1, eps=1e-05):
    m = x.mean(dim=axis, keepdim=True)
    v = x.var(dim=axis, keepdim=True, unbiased=False)
    return (x - m) / torch.sqrt(v + eps)

def _scatter_add(tgt, idx, src, axis=0):
    return tgt.clone().index_add_(axis, idx, src)

def _segment_sum(src, seg, n):
    out = torch.zeros((n,) + src.shape[1:], dtype=src.dtype)
    return out.index_add_(0, seg, src)

def _pad(x, pw):
    flat = tuple((v for pair in reversed(pw) for v in pair))
    return torch.nn.functional.pad(x, flat)
kernel_name = 'parallel_sum__memory_aug_recurrent_mem_tr__moe_arch_mod__prompt'

def setup_inputs():

    _w = lambda a: (lambda t: t.to(torch.bfloat16) if t.is_floating_point() else t)(torch.as_tensor(a))

    def _A_setup():
        B, S, Dm = (1, 2048, 640)
        H, Dh = (32, 128)
        M, seg = (8, 32)
        seg = min(seg, S)
        S = S // seg * seg
        return {'x': _w((lambda *s: torch.randn(*s, dtype=torch.bfloat16) * 0.05)(B, S, Dm)), 'mem0': _w((lambda *s: torch.randn(*s, dtype=torch.bfloat16) * 1.6)(B, M, Dm)), 'Wq': _w((lambda *s: torch.randn(*s, dtype=torch.bfloat16) * 0.8)(Dm, H, Dh) * Dm ** (-0.5)), 'Wk': _w((lambda *s: torch.randn(*s, dtype=torch.bfloat16) * 0.8)(Dm, H, Dh) * Dm ** (-0.5)), 'Wv': _w((lambda *s: torch.randn(*s, dtype=torch.bfloat16) * 1.6)(Dm, H, Dh) * Dm ** (-0.5)), 'Wo': _w((lambda *s: torch.randn(*s, dtype=torch.bfloat16) * 1.6)(H, Dh, Dm) * (H * Dh) ** (-0.5)), 'seg_len': seg}
    _A_in = _A_setup()

    def _B_setup():
        B, S, D, F, C = (1, 2048, 640, 1728, 8)
        return {'x': _w((lambda *s: torch.randn(*s, dtype=torch.bfloat16) * 0.05)(B, S, D)), 'w_r': _w((lambda *s: torch.randn(*s, dtype=torch.bfloat16) * 0.05)(D)), 'w_gate': _w((lambda *s: torch.randn(*s, dtype=torch.bfloat16) * 0.05)(D, F)), 'w_up': _w((lambda *s: torch.randn(*s, dtype=torch.bfloat16) * 0.05)(D, F)), 'w_down': _w((lambda *s: torch.randn(*s, dtype=torch.bfloat16) * 0.05)(F, D))}
    _B_in = _B_setup()
    return {'A_x': _A_in['x'], 'A_mem0': _A_in['mem0'], 'A_Wq': _A_in['Wq'], 'A_Wk': _A_in['Wk'], 'A_Wv': _A_in['Wv'], 'A_Wo': _A_in['Wo'], 'A_seg_len': _A_in['seg_len'], 'B_x': _B_in['x'], 'B_w_r': _B_in['w_r'], 'B_w_gate': _B_in['w_gate'], 'B_w_up': _B_in['w_up'], 'B_w_down': _B_in['w_down']}

def reference(A_x, A_mem0, A_Wq, A_Wk, A_Wv, A_Wo, A_seg_len, B_x, B_w_r, B_w_gate, B_w_up, B_w_down):

    def _A_ref(x, mem0, Wq, Wk, Wv, Wo, seg_len):
        B, S, Dm = x.shape
        H, Dh = (Wq.shape[1], Wq.shape[2])
        M = mem0.shape[1]
        scale = Dh ** (-0.5)
        n_seg = S // seg_len
        L = M + seg_len
        idx = torch.arange(L)
        mask = (idx[:, None] < M) | (idx[:, None] >= idx[None, :])
        mem = mem0
        outs = []
        for s in range(n_seg):
            xs = x[:, s * seg_len:(s + 1) * seg_len, :].clone()
            h = torch.concatenate([mem, xs], axis=1)
            q = torch.einsum('btd,dhe->bhte', h, Wq)
            k = torch.einsum('btd,dhe->bhte', h, Wk)
            v = torch.einsum('btd,dhe->bhte', h, Wv)
            scores = torch.einsum('bhqe,bhke->bhqk', q, k) * scale
            scores = torch.where(mask[None, None], scores, -1000000000.0)
            p = _softmax(scores, axis=-1)
            o = torch.einsum('bhqk,bhke->bhqe', p, v)
            o = torch.einsum('bhte,hed->btd', o, Wo)
            mem = o[:, :M, :].clone()
            outs.append(o[:, M:, :])
        return torch.concatenate(outs, axis=1)

    def _B_ref(x, w_r, w_gate, w_up, w_down):
        B, S, D = x.shape
        C = 8
        r = x @ w_r
        idx = torch.argsort(-r, axis=-1)[:, :C]
        idx_e = (lambda x, n, axis: torch.repeat_interleave(x, n, dim=axis))(idx.reshape(B, C, 1), D, axis=2)
        x_sel = (lambda x, i, axis: torch.take_along_dim(x, i, dim=axis))(x, idx_e, axis=1)
        g = _silu(x_sel @ w_gate)
        u = x_sel @ w_up
        y = g * u @ w_down
        r_sel = _sigmoid((lambda x, i, axis: torch.take_along_dim(x, i, dim=axis))(r, idx, axis=1))
        y = y * r_sel.reshape(B, C, 1)
        b_off = (torch.arange(B) * S).reshape(B, 1)
        flat_idx = (idx + b_off).reshape(B * C)
        zero = (x * 0.0).reshape(B * S, D)
        upd = _scatter_add(zero, flat_idx, y.reshape(B * C, D), axis=0)
        return x + upd.reshape(B, S, D)
    x = A_x
    xn = _layernorm(x, axis=-1)
    return x + _A_ref(xn, A_mem0, A_Wq, A_Wk, A_Wv, A_Wo, A_seg_len) + _B_ref(xn, B_w_r, B_w_gate, B_w_up, B_w_down)
```

```cpp
#include <hip/hip_runtime.h>
#include <hip/hip_bf16.h>
#include <stdint.h>

constexpr int S_TOK = 2048, DM = 640, NH = 32, DH = 128, HD = 4096, NMEM = 8, SEG = 32, NSEG = 64;
constexpr int FF = 1728, CSEL = 8;
constexpr int KS_DM = DM / 32;
constexpr int KS_HD = HD / 32;

typedef unsigned int u32;
typedef unsigned short u16;
typedef __attribute__((ext_vector_type(4))) u32 u32x4;
typedef __attribute__((ext_vector_type(2))) u32 u32x2;
typedef __attribute__((ext_vector_type(4))) float f32x4;
typedef __attribute__((ext_vector_type(8))) short bf16x8;
typedef __attribute__((ext_vector_type(4))) short bf16x4;
typedef __attribute__((ext_vector_type(2))) __bf16 nbf16x2;

#define SENT 0xFFFFFFFFu

__device__ __forceinline__ float bf2f(u16 v) { return __uint_as_float(((u32)v) << 16); }
__device__ __forceinline__ float bflo(u32 v) { return __uint_as_float(v << 16); }
__device__ __forceinline__ float bfhi(u32 v) { return __uint_as_float(v & 0xffff0000u); }
__device__ __forceinline__ u32 pack2(float a, float b) {
  nbf16x2 v = {(__bf16)a, (__bf16)b};
  return __builtin_bit_cast(u32, v);
}
__device__ __forceinline__ float bfr(float a) { return (float)(__bf16)a; }
__device__ __forceinline__ bf16x8 as_bf8(u32x4 v) { return __builtin_bit_cast(bf16x8, v); }
__device__ __forceinline__ bf16x4 as_bf4(u32x2 v) { return __builtin_bit_cast(bf16x4, v); }
__device__ __forceinline__ f32x4 mfma32(bf16x8 a, bf16x8 b, f32x4 c) { return __builtin_amdgcn_mfma_f32_16x16x32_bf16(a, b, c, 0, 0, 0); }
__device__ __forceinline__ f32x4 mfma16(bf16x4 a, bf16x4 b, f32x4 c) {
  u32x2 a2 = __builtin_bit_cast(u32x2, a), b2 = __builtin_bit_cast(u32x2, b);
  u32x4 a4 = {a2.x, a2.y, 0u, 0u}, b4 = {b2.x, b2.y, 0u, 0u};
  return __builtin_amdgcn_mfma_f32_16x16x32_bf16(__builtin_bit_cast(bf16x8, a4), __builtin_bit_cast(bf16x8, b4), c, 0, 0, 0);
}
__device__ __forceinline__ int p32(int T, int r) { return 8 * (r >> 2) + 4 * T + (r & 3); }

constexpr size_t SZ_XN = (size_t)S_TOK * DM * 2;
constexpr size_t OFF_XN = 0;
constexpr size_t OFF_XNF = OFF_XN + SZ_XN;
constexpr size_t OFF_R = OFF_XNF + SZ_XN;
constexpr size_t OFF_WF = OFF_R + 8192;
constexpr size_t SZ_WF = (size_t)768 * 20 * 1024;
constexpr size_t OFF_WOF = OFF_WF + SZ_WF;
constexpr size_t SZ_WOF = (size_t)40 * 128 * 1024;
constexpr size_t OFF_Q = OFF_WOF + SZ_WOF;
constexpr size_t SZ_Q = (size_t)S_TOK * HD * 2;
constexpr size_t OFF_K = OFF_Q + SZ_Q;
constexpr size_t OFF_VT = OFF_K + SZ_Q;
constexpr size_t OFF_KM = OFF_VT + SZ_Q;
constexpr size_t SZ_KM = (size_t)NSEG * NMEM * HD * 2;
constexpr size_t OFF_VMT = OFF_KM + SZ_KM;
constexpr size_t OFF_OF = OFF_VMT + SZ_KM;
constexpr size_t OFF_INBOX = OFF_OF + SZ_Q;
constexpr u32 NPROD = 128, NRED = 80;
constexpr u32 INBOX_SLOT = NRED * NPROD * 256;
constexpr size_t OFF_MEMB = OFF_INBOX + 2 * (size_t)INBOX_SLOT;
constexpr u32 MEM_STEP = NRED * 128;
constexpr size_t SZ_MEMB = (size_t)65 * MEM_STEP;
constexpr size_t OFF_HB = OFF_MEMB + ((SZ_MEMB + 255) / 256) * 256;
constexpr size_t OFF_YP = OFF_HB + 32768;
constexpr size_t OFF_SEL = OFF_YP + (size_t)8 * 8 * 640 * 4;
constexpr size_t OFF_ERR = OFF_SEL + 256;
constexpr size_t WS_NEED = OFF_ERR + 256;

constexpr int PREP_LN_BLOCKS = S_TOK / 4;
constexpr int PREP_WQKV_BLOCKS = 3 * 20 * 16;
constexpr int PREP_WO_BLOCKS = 128 * 3;
constexpr int PREP_INIT_BLOCKS = 64;
constexpr int PREP_BLOCKS = PREP_LN_BLOCKS + PREP_WQKV_BLOCKS + PREP_WO_BLOCKS + PREP_INIT_BLOCKS;

__device__ __forceinline__ void tile_weights(const u16* __restrict__ W, int N, int k0, int n0, int ncols, u16* __restrict__ dst,
                                             int nks_total, int ks, int nt0, u16* lds) {
  const int tid = threadIdx.x;
  const int chunks_per_row = ncols / 8;
  for (int i = tid; i < 32 * chunks_per_row; i += 256) {
    int row = i / chunks_per_row, ch = i % chunks_per_row;
    u32x4 v = *(const u32x4*)(W + (size_t)(k0 + row) * N + n0 + ch * 8);
    *(u32x4*)(lds + row * 264 + ch * 8) = v;
  }
  __syncthreads();
  const int ntiles = ncols / 16;
  for (int o = tid; o < ntiles * 64; o += 256) {
    int t = o >> 6, l = o & 63, r = l & 15, g = l >> 4;
    int col = 32 * (t >> 1) + p32(t & 1, r);
    u16 e[8];
#pragma unroll
    for (int j = 0; j < 8; ++j) e[j] = lds[(8 * g + j) * 264 + col];
    u32x4 v = {(u32)e[0] | ((u32)e[1] << 16), (u32)e[2] | ((u32)e[3] << 16), (u32)e[4] | ((u32)e[5] << 16), (u32)e[6] | ((u32)e[7] << 16)};
    *(u32x4*)(dst + (((size_t)(nt0 + t) * nks_total + ks) * 64 + l) * 8) = v;
  }
}

__global__ __launch_bounds__(256) void prep_kernel(const u16* __restrict__ x, const u16* __restrict__ mem0, const u16* __restrict__ Wq,
                                                   const u16* __restrict__ Wk, const u16* __restrict__ Wv, const u16* __restrict__ Wo,
                                                   const u16* __restrict__ w_r, char* __restrict__ ws) {
  __shared__ __attribute__((aligned(16))) u16 lds[32 * 264];
  const int b = blockIdx.x, tid = threadIdx.x;
  if (b < PREP_LN_BLOCKS) {
    const int w = tid >> 6, l = tid & 63;
    const int t = b * 4 + w;
    const u16* xr = x + (size_t)t * DM;
    u32x4 c0 = *(const u32x4*)(xr + l * 8);
    u32x4 c1 = {0, 0, 0, 0};
    if (l < 16) c1 = *(const u32x4*)(xr + 512 + l * 8);
    float v[16];
    v[0] = bflo(c0.x); v[1] = bfhi(c0.x); v[2] = bflo(c0.y); v[3] = bfhi(c0.y); v[4] = bflo(c0.z); v[5] = bfhi(c0.z); v[6] = bflo(c0.w); v[7] = bfhi(c0.w);
    v[8] = bflo(c1.x); v[9] = bfhi(c1.x); v[10] = bflo(c1.y); v[11] = bfhi(c1.y); v[12] = bflo(c1.z); v[13] = bfhi(c1.z); v[14] = bflo(c1.w); v[15] = bfhi(c1.w);
    float s = 0.f;
#pragma unroll
    for (int j = 0; j < 16; ++j) s += v[j];
#pragma unroll
    for (int o = 32; o > 0; o >>= 1) s += __shfl_xor(s, o);
    const float mean = s * (1.0f / DM);
    float q = 0.f;
#pragma unroll
    for (int j = 0; j < 8; ++j) { float d = v[j] - mean; q += d * d; }
    if (l < 16) {
#pragma unroll
      for (int j = 8; j < 16; ++j) { float d = v[j] - mean; q += d * d; }
    }
#pragma unroll
    for (int o = 32; o > 0; o >>= 1) q += __shfl_xor(q, o);
    const float var_b = bfr(q * (1.0f / DM));
    const float mean_b = bfr(mean);
    const float den = bfr(sqrtf(bfr(var_b + 1e-05f)));
    float y[16];
#pragma unroll
    for (int j = 0; j < 16; ++j) y[j] = bfr(bfr(v[j] - mean_b) / den);
    u32x4 w0 = *(const u32x4*)(w_r + l * 8);
    u32x4 w1 = {0, 0, 0, 0};
    if (l < 16) w1 = *(const u32x4*)(w_r + 512 + l * 8);
    float rs = y[0] * bflo(w0.x) + y[1] * bfhi(w0.x) + y[2] * bflo(w0.y) + y[3] * bfhi(w0.y) + y[4] * bflo(w0.z) + y[5] * bfhi(w0.z) + y[6] * bflo(w0.w) + y[7] * bfhi(w0.w);
    if (l < 16) rs += y[8] * bflo(w1.x) + y[9] * bfhi(w1.x) + y[10] * bflo(w1.y) + y[11] * bfhi(w1.y) + y[12] * bflo(w1.z) + y[13] * bfhi(w1.z) + y[14] * bflo(w1.w) + y[15] * bfhi(w1.w);
#pragma unroll
    for (int o = 32; o > 0; o >>= 1) rs += __shfl_xor(rs, o);
    if (l == 0) ((float*)(ws + OFF_R))[t] = bfr(rs);
    u16* xn = (u16*)(ws + OFF_XN);
    u16* xnf = (u16*)(ws + OFF_XNF);
    const int sg = t >> 5, tt = t & 31;
    const int mt = 2 * sg + ((tt >> 2) & 1), r = 4 * (tt >> 3) + (tt & 3);
    u32x4 o0 = {pack2(y[0], y[1]), pack2(y[2], y[3]), pack2(y[4], y[5]), pack2(y[6], y[7])};
    *(u32x4*)(xn + (size_t)t * DM + l * 8) = o0;
    { int c = l; *(u32x4*)(xnf + (((size_t)mt * 20 + (c >> 2)) * 64 + (c & 3) * 16 + r) * 8) = o0; }
    if (l < 16) {
      u32x4 o1 = {pack2(y[8], y[9]), pack2(y[10], y[11]), pack2(y[12], y[13]), pack2(y[14], y[15])};
      *(u32x4*)(xn + (size_t)t * DM + 512 + l * 8) = o1;
      int c = 64 + l;
      *(u32x4*)(xnf + (((size_t)mt * 20 + (c >> 2)) * 64 + (c & 3) * 16 + r) * 8) = o1;
    }
  } else if (b < PREP_LN_BLOCKS + PREP_WQKV_BLOCKS) {
    int i = b - PREP_LN_BLOCKS;
    int mat = i / 320, rem = i % 320, ks = rem / 16, nb = rem % 16;
    const u16* W = mat == 0 ? Wq : (mat == 1 ? Wk : Wv);
    tile_weights(W, HD, ks * 32, nb * 256, 256, (u16*)(ws + OFF_WF), 20, ks, mat * 256 + nb * 16, lds);
  } else if (b < PREP_LN_BLOCKS + PREP_WQKV_BLOCKS + PREP_WO_BLOCKS) {
    int i = b - PREP_LN_BLOCKS - PREP_WQKV_BLOCKS;
    int ks = i / 3, nb = i % 3;
    tile_weights(Wo, DM, ks * 32, nb * 256, nb == 2 ? 128 : 256, (u16*)(ws + OFF_WOF), 128, ks, nb * 16, lds);
  } else {
    int i = b - (PREP_LN_BLOCKS + PREP_WQKV_BLOCKS + PREP_WO_BLOCKS);
    u32x4 sv = {SENT, SENT, SENT, SENT};
    u32x4* p = (u32x4*)(ws + OFF_INBOX);
    const size_t n16 = (2 * (size_t)INBOX_SLOT) / 16;
    for (size_t j = (size_t)i * 256 + tid; j < n16; j += (size_t)PREP_INIT_BLOCKS * 256) p[j] = sv;
    u32x4* pm = (u32x4*)(ws + OFF_MEMB + MEM_STEP);
    const size_t m16 = (size_t)64 * MEM_STEP / 16;
    for (size_t j = (size_t)i * 256 + tid; j < m16; j += (size_t)PREP_INIT_BLOCKS * 256) pm[j] = sv;
    if (i == 0) {
      for (int j = tid; j < 640; j += 256) {
        int p_ = j >> 3, m = j & 7;
        u32x4 v = *(const u32x4*)(mem0 + m * DM + p_ * 8);
        if (v.x == SENT) v.x = 0xFFC0FFC0u;
        if (v.y == SENT) v.y = 0xFFC0FFC0u;
        if (v.z == SENT) v.z = 0xFFC0FFC0u;
        if (v.w == SENT) v.w = 0xFFC0FFC0u;
        *(u32x4*)(ws + OFF_MEMB + (size_t)j * 16) = v;
      }
      if (tid == 0) *(u32*)(ws + OFF_ERR) = 0;
    }
  }
}

__device__ __forceinline__ void glds16(const void* g, void* l) {
  __builtin_amdgcn_global_load_lds((const __attribute__((address_space(1))) void*)g, (__attribute__((address_space(3))) void*)l, 16, 0, 0);
}

__global__ __launch_bounds__(256, 2) void qkv_gemm_kernel(char* __restrict__ ws) {
  extern __shared__ __attribute__((aligned(16))) char smem[];
  const int tid = threadIdx.x, w = __builtin_amdgcn_readfirstlane(tid >> 6), l = tid & 63;
  const int nb = blockIdx.x % 96, mb = blockIdx.x / 96;
  const char* xnf = ws + OFF_XNF;
  const char* wf = ws + OFF_WF;
  const int mat = nb >> 5, h = nb & 31;
  const int wr = w >> 1, wc = w & 1;
  f32x4 acc[4][4];
#pragma unroll
  for (int i = 0; i < 4; ++i)
#pragma unroll
    for (int j = 0; j < 4; ++j) acc[i][j] = (f32x4){0, 0, 0, 0};

  auto stage = [&](int t, int buf) {
#pragma unroll
    for (int kk = 0; kk < 2; ++kk) {
#pragma unroll
      for (int i = 0; i < 4; ++i) {
        int f = 4 * w + i;
        int ks = 2 * t + kk;
        const char* src = (f < 8) ? xnf + (((size_t)(mb * 8 + f) * 20 + ks) * 1024) : wf + (((size_t)(nb * 8 + (f - 8)) * 20 + ks) * 1024);
        glds16(src + l * 16, smem + buf * 32768 + (kk * 16 + f) * 1024 + l * 16);
      }
    }
  };
  stage(0, 0);
  for (int t = 0; t < 10; ++t) {
    asm volatile("s_waitcnt vmcnt(0)" ::: "memory");
    __syncthreads();
    if (t + 1 < 10) stage(t + 1, (t + 1) & 1);
    const char* base = smem + (t & 1) * 32768;
#pragma unroll
    for (int kk = 0; kk < 2; ++kk) {
      bf16x8 a[4], bb[4];
#pragma unroll
      for (int i = 0; i < 4; ++i) a[i] = *(const bf16x8*)(base + (kk * 16 + 4 * wr + i) * 1024 + l * 16);
#pragma unroll
      for (int j = 0; j < 4; ++j) bb[j] = *(const bf16x8*)(base + (kk * 16 + 8 + 4 * wc + j) * 1024 + l * 16);
      if (mat < 2) {
#pragma unroll
        for (int i = 0; i < 4; ++i)
#pragma unroll
          for (int j = 0; j < 4; ++j) acc[i][j] = mfma32(bb[j], a[i], acc[i][j]);
      } else {
#pragma unroll
        for (int i = 0; i < 4; ++i)
#pragma unroll
          for (int j = 0; j < 4; ++j) acc[i][j] = mfma32(a[i], bb[j], acc[i][j]);
      }
    }
  }
  const int c = l & 15, g = l >> 4;
  if (mat < 2) {
    u16* dst = (u16*)(ws + (mat == 0 ? OFF_Q : OFF_K));
#pragma unroll
    for (int i = 0; i < 4; ++i) {
      int mt = mb * 8 + 4 * wr + i;
      int tok = 32 * (mt >> 1) + p32(mt & 1, c);
#pragma unroll
      for (int u = 0; u < 2; ++u) {
        f32x4 a0 = acc[i][2 * u], a1 = acc[i][2 * u + 1];
        u32x4 v = {pack2(a0.x, a0.y), pack2(a0.z, a0.w), pack2(a1.x, a1.y), pack2(a1.z, a1.w)};
        *(u32x4*)(dst + (size_t)tok * HD + h * 128 + 32 * (2 * wc + u) + 8 * g) = v;
      }
    }
  } else {
    u16* dst = (u16*)(ws + OFF_VT);
#pragma unroll
    for (int ip = 0; ip < 2; ++ip) {
      int sg = mb * 4 + 2 * wr + ip;
#pragma unroll
      for (int j = 0; j < 4; ++j) {
        int ntl = 4 * wc + j;
        int e = 32 * (ntl >> 1) + p32(ntl & 1, c);
        f32x4 a0 = acc[2 * ip][j], a1 = acc[2 * ip + 1][j];
        u32x4 v = {pack2(a0.x, a0.y), pack2(a0.z, a0.w), pack2(a1.x, a1.y), pack2(a1.z, a1.w)};
        *(u32x4*)(dst + (((size_t)sg * 32 + h) * 128 + e) * 32 + 8 * g) = v;
      }
    }
  }
}

constexpr u32 SPIN_CAP = 1u << 16;
__device__ __forceinline__ bool has_sent(u32x4 v) { return (v.x == SENT) | (v.y == SENT) | (v.z == SENT) | (v.w == SENT); }
__device__ __forceinline__ u32 desent(float f) { u32 b = __float_as_uint(f); return b == SENT ? 0x7FC00000u : b; }

constexpr int CH_MEMF = 0, CH_PART = 20480, CH_WV = 40960, CH_WO = 81920, CH_SRED = 122880, CH_LDS = 123904;

__global__ __launch_bounds__(256, 1) void chain_kernel(char* __restrict__ ws) {
  extern __shared__ __attribute__((aligned(16))) char smem[];
  const int c = blockIdx.x, tid = threadIdx.x, w = __builtin_amdgcn_readfirstlane(tid >> 6), l = tid & 63;
  const int h = c >> 2, gs = c & 3;
  const int m = l & 15, g = l >> 4;
  const char* wf = ws + OFF_WF;
  const char* wof = ws + OFF_WOF;
  bf16x8 wq[2][20], wk[2][20];
#pragma unroll
  for (int T = 0; T < 2; ++T)
#pragma unroll
    for (int ks = 0; ks < 20; ++ks) {
      wq[T][ks] = *(const bf16x8*)(wf + (((size_t)(2 * (4 * h + w) + T) * 20 + ks) * 64 + l) * 16);
      wk[T][ks] = *(const bf16x8*)(wf + (((size_t)(256 + 2 * (4 * h + w) + T) * 20 + ks) * 64 + l) * 16);
    }
  for (int i = tid; i < 40 * 64; i += 256) {
    int f = i >> 6, ll = i & 63;
    *(u32x4*)(smem + CH_WV + i * 16) = *(const u32x4*)(wf + (((size_t)(512 + 2 * (4 * h + gs) + f / 20) * 20 + f % 20) * 64 + ll) * 16);
    *(u32x4*)(smem + CH_WO + i * 16) = *(const u32x4*)(wof + (((size_t)f * 128 + (4 * h + gs)) * 64 + ll) * 16);
  }
  for (int i = tid; i < 1280; i += 256) *(u32x4*)(smem + CH_MEMF + i * 16) = (u32x4){0, 0, 0, 0};
  __syncthreads();

  const __amdgpu_buffer_rsrc_t rin = __builtin_amdgcn_make_buffer_rsrc(ws + OFF_INBOX, 0, 2 * INBOX_SLOT, 0x00020000);
  const __amdgpu_buffer_rsrc_t rmem = __builtin_amdgcn_make_buffer_rsrc(ws + OFF_MEMB, 0, (u32)SZ_MEMB, 0x00020000);
  const u16* Kx = (const u16*)(ws + OFF_K);
  const u16* VT = (const u16*)(ws + OFF_VT);
  u16* Km = (u16*)(ws + OFF_KM);
  u16* VmT = (u16*)(ws + OFF_VMT);
  u32* err = (u32*)(ws + OFF_ERR);
  const float scale = 0.08838834764831845f;

  for (int s = 0; s < NSEG; ++s) {
    bf16x8 kx[2], vx[2];
#pragma unroll
    for (int T = 0; T < 2; ++T) {
      kx[T] = *(const bf16x8*)(Kx + (size_t)(32 * s + p32(T, m)) * HD + h * 128 + 32 * w + 8 * g);
      vx[T] = *(const bf16x8*)(VT + (((size_t)s * 32 + h) * 128 + 32 * gs + p32(T, m)) * 32 + 8 * g);
    }
    {
      const u32 base = s * MEM_STEP + w * 2560;
      u32x4 a, b, d;
      u32 spins = 0;
      for (;;) {
        a = __builtin_amdgcn_raw_buffer_load_b128(rmem, base + l * 16, 0, 16);
        b = __builtin_amdgcn_raw_buffer_load_b128(rmem, base + 1024 + l * 16, 0, 16);
        d = __builtin_amdgcn_raw_buffer_load_b128(rmem, base + 2048 + (l & 31) * 16, 0, 16);
        bool bad = has_sent(a) | has_sent(b) | has_sent(d);
        if (!__any(bad)) break;
        if (++spins > SPIN_CAP) { if (l == 0) *err = 1; break; }
      }
      int i0 = w * 160 + l, i1 = i0 + 64, i2 = i0 + 128;
      *(u32x4*)(smem + CH_MEMF + ((i0 >> 3) * 16 + (i0 & 7)) * 16) = a;
      *(u32x4*)(smem + CH_MEMF + ((i1 >> 3) * 16 + (i1 & 7)) * 16) = b;
      if (l < 32) *(u32x4*)(smem + CH_MEMF + ((i2 >> 3) * 16 + (i2 & 7)) * 16) = d;
    }
    __syncthreads();
    f32x4 aq[2], ak[2], av[2];
#pragma unroll
    for (int T = 0; T < 2; ++T) { aq[T] = (f32x4){0, 0, 0, 0}; ak[T] = aq[T]; av[T] = aq[T]; }
#pragma unroll
    for (int ks = 0; ks < 20; ++ks) {
      bf16x8 mf = *(const bf16x8*)(smem + CH_MEMF + ks * 1024 + l * 16);
      aq[0] = mfma32(wq[0][ks], mf, aq[0]);
      aq[1] = mfma32(wq[1][ks], mf, aq[1]);
      ak[0] = mfma32(wk[0][ks], mf, ak[0]);
      ak[1] = mfma32(wk[1][ks], mf, ak[1]);
    }
#pragma unroll
    for (int i = 0; i < 5; ++i) {
      int ks = 5 * w + i;
      bf16x8 mf = *(const bf16x8*)(smem + CH_MEMF + ks * 1024 + l * 16);
      bf16x8 v0 = *(const bf16x8*)(smem + CH_WV + ks * 1024 + l * 16);
      bf16x8 v1 = *(const bf16x8*)(smem + CH_WV + (20 + ks) * 1024 + l * 16);
      av[0] = mfma32(mf, v0, av[0]);
      av[1] = mfma32(mf, v1, av[1]);
    }
    u32x4 qp = {pack2(aq[0].x, aq[0].y), pack2(aq[0].z, aq[0].w), pack2(aq[1].x, aq[1].y), pack2(aq[1].z, aq[1].w)};
    u32x4 kp = {pack2(ak[0].x, ak[0].y), pack2(ak[0].z, ak[0].w), pack2(ak[1].x, ak[1].y), pack2(ak[1].z, ak[1].w)};
    const f32x4 z4 = {0, 0, 0, 0};
    f32x4 sM = mfma32(as_bf8(kp), as_bf8(qp), z4);
    f32x4 sX0 = mfma32(kx[0], as_bf8(qp), z4);
    f32x4 sX1 = mfma32(kx[1], as_bf8(qp), z4);
    {
      char* pp = smem + CH_PART + w * 5120 + l * 16;
      *(f32x4*)(pp) = sM; *(f32x4*)(pp + 1024) = sX0; *(f32x4*)(pp + 2048) = sX1; *(f32x4*)(pp + 3072) = av[0]; *(f32x4*)(pp + 4096) = av[1];
    }
    if (w == gs && m < 8) *(u32x4*)(Km + ((size_t)s * 8 + m) * HD + h * 128 + 32 * w + 8 * g) = kp;
    __syncthreads();
    f32x4 t[5];
#pragma unroll
    for (int i = 0; i < 5; ++i) {
      const char* pp = smem + CH_PART + i * 1024 + l * 16;
      f32x4 a0 = *(const f32x4*)(pp), a1 = *(const f32x4*)(pp + 5120), a2 = *(const f32x4*)(pp + 10240), a3 = *(const f32x4*)(pp + 15360);
      t[i] = (a0 + a1) + (a2 + a3);
    }
    float sm_[4], sx[8];
#pragma unroll
    for (int i = 0; i < 4; ++i) {
      sm_[i] = bfr(bfr(t[0][i]) * scale);
      sx[i] = bfr(bfr(t[1][i]) * scale);
      sx[4 + i] = bfr(bfr(t[2][i]) * scale);
    }
    float mx = sx[0];
#pragma unroll
    for (int i = 1; i < 8; ++i) mx = fmaxf(mx, sx[i]);
    if (g < 2) {
#pragma unroll
      for (int i = 0; i < 4; ++i) mx = fmaxf(mx, sm_[i]);
    }
    mx = fmaxf(mx, __shfl_xor(mx, 16));
    mx = fmaxf(mx, __shfl_xor(mx, 32));
    float sum = 0.f;
#pragma unroll
    for (int i = 0; i < 8; ++i) { sx[i] = __expf(sx[i] - mx); sum += sx[i]; }
#pragma unroll
    for (int i = 0; i < 4; ++i) { sm_[i] = g < 2 ? __expf(sm_[i] - mx) : 0.f; sum += sm_[i]; }
    sum += __shfl_xor(sum, 16);
    sum += __shfl_xor(sum, 32);
    const float inv = 1.0f / sum;
    u32x4 px = {pack2(sx[0] * inv, sx[1] * inv), pack2(sx[2] * inv, sx[3] * inv), pack2(sx[4] * inv, sx[5] * inv), pack2(sx[6] * inv, sx[7] * inv)};
    u32x2 pm = {pack2(sm_[0] * inv, sm_[1] * inv), pack2(sm_[2] * inv, sm_[3] * inv)};
    u32x2 vm0 = {pack2(t[3].x, t[3].y), pack2(t[3].z, t[3].w)};
    u32x2 vm1 = {pack2(t[4].x, t[4].y), pack2(t[4].z, t[4].w)};
    if (w == 0 && g < 2) {
      *(u32x2*)(VmT + (((size_t)s * 32 + h) * 128 + 32 * gs + p32(0, m)) * 8 + 4 * g) = vm0;
      *(u32x2*)(VmT + (((size_t)s * 32 + h) * 128 + 32 * gs + p32(1, m)) * 8 + 4 * g) = vm1;
    }
    f32x4 o0 = mfma32(vx[0], as_bf8(px), z4);
    f32x4 o1 = mfma32(vx[1], as_bf8(px), z4);
    o0 = mfma16(as_bf4(vm0), as_bf4(pm), o0);
    o1 = mfma16(as_bf4(vm1), as_bf4(pm), o1);
    u32x4 op = {pack2(o0.x, o0.y), pack2(o0.z, o0.w), pack2(o1.x, o1.y), pack2(o1.z, o1.w)};
    const int slot = s & 1;
#pragma unroll
    for (int i = 0; i < 10; ++i) {
      int nt = 10 * w + i;
      bf16x8 wo = *(const bf16x8*)(smem + CH_WO + nt * 1024 + l * 16);
      f32x4 d = mfma32(wo, as_bf8(op), z4);
      if (m < 8) {
        u32x4 v = {desent(d.x), desent(d.y), desent(d.z), desent(d.w)};
        u32 piece = 4 * (nt >> 1) + g;
        u32 off = slot * INBOX_SLOT + (piece * NPROD + c) * 256 + m * 32 + (nt & 1) * 16;
        __builtin_amdgcn_raw_buffer_store_b128(v, rin, off, 0, 16);
      }
    }
    if (c < (int)NRED) {
      const u32 base = slot * INBOX_SLOT + c * NPROD * 256 + w * 8192 + l * 16;
      u32x4 v[8];
      u32 spins = 0;
      for (;;) {
        bool bad = false;
#pragma unroll
        for (int i = 0; i < 8; ++i) v[i] = __builtin_amdgcn_raw_buffer_load_b128(rin, base + i * 1024, 0, 16);
#pragma unroll
        for (int i = 0; i < 8; ++i) bad |= has_sent(v[i]);
        if (!__any(bad)) break;
        if (++spins > SPIN_CAP) { if (l == 0) *err = 2; break; }
      }
      f32x4 sum4 = {0, 0, 0, 0};
#pragma unroll
      for (int i = 0; i < 8; ++i) sum4 += __builtin_bit_cast(f32x4, v[i]);
#pragma unroll
      for (int j = 0; j < 4; ++j) { sum4[j] += __shfl_xor(sum4[j], 16); sum4[j] += __shfl_xor(sum4[j], 32); }
      if (l < 16) *(f32x4*)(smem + CH_SRED + (w * 16 + l) * 16) = sum4;
      const u32x4 sv = {SENT, SENT, SENT, SENT};
#pragma unroll
      for (int i = 0; i < 8; ++i) __builtin_amdgcn_raw_buffer_store_b128(sv, rin, base + i * 1024, 0, 16);
      __syncthreads();
      if (w == 0 && l < 8) {
        const f32x4* sr = (const f32x4*)(smem + CH_SRED);
        f32x4 a0 = (sr[2 * l] + sr[16 + 2 * l]) + (sr[32 + 2 * l] + sr[48 + 2 * l]);
        f32x4 a1 = (sr[2 * l + 1] + sr[16 + 2 * l + 1]) + (sr[32 + 2 * l + 1] + sr[48 + 2 * l + 1]);
        u32x4 o = {pack2(a0.x, a0.y), pack2(a0.z, a0.w), pack2(a1.x, a1.y), pack2(a1.z, a1.w)};
        if (o.x == SENT) o.x = 0xFFC0FFC0u;
        if (o.y == SENT) o.y = 0xFFC0FFC0u;
        if (o.z == SENT) o.z = 0xFFC0FFC0u;
        if (o.w == SENT) o.w = 0xFFC0FFC0u;
        __builtin_amdgcn_raw_buffer_store_b128(o, rmem, (s + 1) * MEM_STEP + c * 128 + l * 16, 0, 16);
      }
    }
  }
}

__device__ __forceinline__ void block_top8(const float* __restrict__ r, int* sidx, float* sval, float* swv, int* swi) {
  const int tid = threadIdx.x, w = tid >> 6, l = tid & 63;
  float v[8];
#pragma unroll
  for (int j = 0; j < 8; ++j) v[j] = r[tid + 256 * j];
  for (int round = 0; round < CSEL; ++round) {
    float bv = v[0]; int bi = tid;
#pragma unroll
    for (int j = 1; j < 8; ++j) if (v[j] > bv) { bv = v[j]; bi = tid + 256 * j; }
#pragma unroll
    for (int o = 32; o > 0; o >>= 1) {
      float ov = __shfl_xor(bv, o); int oi = __shfl_xor(bi, o);
      if (ov > bv || (ov == bv && oi < bi)) { bv = ov; bi = oi; }
    }
    if (l == 0) { swv[w] = bv; swi[w] = bi; }
    __syncthreads();
    float fv = swv[0]; int fi = swi[0];
#pragma unroll
    for (int k = 1; k < 4; ++k) { float ov = swv[k]; int oi = swi[k]; if (ov > fv || (ov == fv && oi < fi)) { fv = ov; fi = oi; } }
    if (tid == 0) { sidx[round] = fi; sval[round] = fv; }
#pragma unroll
    for (int j = 0; j < 8; ++j) if (tid + 256 * j == fi) v[j] = -INFINITY;
    __syncthreads();
  }
}

__global__ __launch_bounds__(256) void mod_gateup_kernel(const u16* __restrict__ wg, const u16* __restrict__ wu, char* __restrict__ ws) {
  __shared__ float xs[8][640];
  __shared__ float red[4][2][8][128];
  __shared__ int sidx[8]; __shared__ float sval[8]; __shared__ float swv[4]; __shared__ int swi[4];
  const int tid = threadIdx.x, w = tid >> 6, l = tid & 63;
  block_top8((const float*)(ws + OFF_R), sidx, sval, swv, swi);
  const u16* xn = (const u16*)(ws + OFF_XN);
  for (int i = tid; i < 8 * 640; i += 256) { int rr = i / 640, k = i % 640; xs[rr][k] = bf2f(xn[(size_t)sidx[rr] * DM + k]); }
  if (blockIdx.x == 0 && tid < 8) {
    ((int*)(ws + OFF_SEL))[tid] = sidx[tid];
    ((float*)(ws + OFF_SEL))[8 + tid] = bfr(1.0f / (1.0f + __expf(-sval[tid])));
  }
  __syncthreads();
  const int n0 = blockIdx.x * 128 + 2 * l;
  const bool ok = n0 < FF;
  float ag[8][2], au[8][2];
#pragma unroll
  for (int rr = 0; rr < 8; ++rr) { ag[rr][0] = ag[rr][1] = au[rr][0] = au[rr][1] = 0.f; }
  if (ok) {
#pragma unroll 4
    for (int k = 160 * w; k < 160 * w + 160; ++k) {
      u32 g2 = *(const u32*)(wg + (size_t)k * FF + n0);
      u32 u2 = *(const u32*)(wu + (size_t)k * FF + n0);
      float g0 = bflo(g2), g1 = bfhi(g2), u0 = bflo(u2), u1 = bfhi(u2);
#pragma unroll
      for (int rr = 0; rr < 8; ++rr) {
        float xv = xs[rr][k];
        ag[rr][0] += xv * g0; ag[rr][1] += xv * g1; au[rr][0] += xv * u0; au[rr][1] += xv * u1;
      }
    }
  }
#pragma unroll
  for (int rr = 0; rr < 8; ++rr) {
    red[w][0][rr][2 * l] = ag[rr][0]; red[w][0][rr][2 * l + 1] = ag[rr][1];
    red[w][1][rr][2 * l] = au[rr][0]; red[w][1][rr][2 * l + 1] = au[rr][1];
  }
  __syncthreads();
  u16* hb = (u16*)(ws + OFF_HB);
  for (int i = tid; i < 8 * 128; i += 256) {
    int rr = i >> 7, cc = i & 127;
    int n = blockIdx.x * 128 + cc;
    if (n < FF) {
      float gsum = bfr((red[0][0][rr][cc] + red[1][0][rr][cc]) + (red[2][0][rr][cc] + red[3][0][rr][cc]));
      float usum = bfr((red[0][1][rr][cc] + red[1][1][rr][cc]) + (red[2][1][rr][cc] + red[3][1][rr][cc]));
      float sg = bfr(gsum / (1.0f + __expf(-gsum)));
      float hv = bfr(sg * usum);
      hb[rr * FF + n] = (u16)(__float_as_uint(hv) >> 16);
    }
  }
}

__global__ __launch_bounds__(256) void mod_down_kernel(const u16* __restrict__ wd, char* __restrict__ ws) {
  __shared__ float hs[8][216];
  __shared__ float red[4][8][128];
  const int tid = threadIdx.x, w = tid >> 6, l = tid & 63;
  const int cb = blockIdx.x % 5, kb = blockIdx.x / 5;
  const u16* hb = (const u16*)(ws + OFF_HB);
  for (int i = tid; i < 8 * 216; i += 256) { int rr = i / 216, k = i % 216; hs[rr][k] = bf2f(hb[rr * FF + kb * 216 + k]); }
  __syncthreads();
  const int n0 = cb * 128 + 2 * l;
  float a[8][2];
#pragma unroll
  for (int rr = 0; rr < 8; ++rr) a[rr][0] = a[rr][1] = 0.f;
#pragma unroll 6
  for (int k = 54 * w; k < 54 * w + 54; ++k) {
    u32 d2 = *(const u32*)(wd + (size_t)(kb * 216 + k) * DM + n0);
    float d0 = bflo(d2), d1 = bfhi(d2);
#pragma unroll
    for (int rr = 0; rr < 8; ++rr) { float hv = hs[rr][k]; a[rr][0] += hv * d0; a[rr][1] += hv * d1; }
  }
#pragma unroll
  for (int rr = 0; rr < 8; ++rr) { red[w][rr][2 * l] = a[rr][0]; red[w][rr][2 * l + 1] = a[rr][1]; }
  __syncthreads();
  float* yp = (float*)(ws + OFF_YP);
  for (int i = tid; i < 8 * 128; i += 256) {
    int rr = i >> 7, cc = i & 127;
    yp[((size_t)kb * 8 + rr) * DM + cb * 128 + cc] = (red[0][rr][cc] + red[1][rr][cc]) + (red[2][rr][cc] + red[3][rr][cc]);
  }
}

__global__ __launch_bounds__(256) void attn_kernel(char* __restrict__ ws) {
  const int tid = threadIdx.x, w = tid >> 6, l = tid & 63;
  const int unit = blockIdx.x * 4 + w;
  const int s = unit >> 5, h = unit & 31;
  const int c = l & 15, g = l >> 4;
  const u16* Q = (const u16*)(ws + OFF_Q);
  const u16* Kx = (const u16*)(ws + OFF_K);
  const u16* VT = (const u16*)(ws + OFF_VT);
  const u16* Km = (const u16*)(ws + OFF_KM);
  const u16* VmT = (const u16*)(ws + OFF_VMT);
  u16* of = (u16*)(ws + OFF_OF);
  const float scale = 0.08838834764831845f;
  const f32x4 z4 = {0, 0, 0, 0};
  f32x4 sM[2], sX[2][2];
#pragma unroll
  for (int T = 0; T < 2; ++T) { sM[T] = z4; sX[0][T] = z4; sX[1][T] = z4; }
#pragma unroll
  for (int ks = 0; ks < 4; ++ks) {
    bf16x8 q[2], kx[2], km;
#pragma unroll
    for (int T = 0; T < 2; ++T) {
      q[T] = *(const bf16x8*)(Q + (size_t)(32 * s + p32(T, c)) * HD + h * 128 + 32 * ks + 8 * g);
      kx[T] = *(const bf16x8*)(Kx + (size_t)(32 * s + p32(T, c)) * HD + h * 128 + 32 * ks + 8 * g);
    }
    km = *(const bf16x8*)(Km + ((size_t)s * 8 + (c & 7)) * HD + h * 128 + 32 * ks + 8 * g);
#pragma unroll
    for (int T = 0; T < 2; ++T) {
      sM[T] = mfma32(km, q[T], sM[T]);
      sX[0][T] = mfma32(kx[0], q[T], sX[0][T]);
      sX[1][T] = mfma32(kx[1], q[T], sX[1][T]);
    }
  }
  u32x4 px[2]; u32x2 pm[2];
#pragma unroll
  for (int T = 0; T < 2; ++T) {
    const int qi = p32(T, c);
    float sm_[4], sx[8];
#pragma unroll
    for (int i = 0; i < 4; ++i) {
      sm_[i] = bfr(bfr(sM[T][i]) * scale);
      float a = bfr(bfr(sX[0][T][i]) * scale), b = bfr(bfr(sX[1][T][i]) * scale);
      sx[i] = (8 * g + i <= qi) ? a : -1e9f;
      sx[4 + i] = (8 * g + 4 + i <= qi) ? b : -1e9f;
    }
    float mx = sx[0];
#pragma unroll
    for (int i = 1; i < 8; ++i) mx = fmaxf(mx, sx[i]);
    if (g < 2) {
#pragma unroll
      for (int i = 0; i < 4; ++i) mx = fmaxf(mx, sm_[i]);
    }
    mx = fmaxf(mx, __shfl_xor(mx, 16));
    mx = fmaxf(mx, __shfl_xor(mx, 32));
    float sum = 0.f;
#pragma unroll
    for (int i = 0; i < 8; ++i) { sx[i] = __expf(sx[i] - mx); sum += sx[i]; }
#pragma unroll
    for (int i = 0; i < 4; ++i) { sm_[i] = g < 2 ? __expf(sm_[i] - mx) : 0.f; sum += sm_[i]; }
    sum += __shfl_xor(sum, 16);
    sum += __shfl_xor(sum, 32);
    const float inv = 1.0f / sum;
    px[T] = (u32x4){pack2(sx[0] * inv, sx[1] * inv), pack2(sx[2] * inv, sx[3] * inv), pack2(sx[4] * inv, sx[5] * inv), pack2(sx[6] * inv, sx[7] * inv)};
    pm[T] = (u32x2){pack2(sm_[0] * inv, sm_[1] * inv), pack2(sm_[2] * inv, sm_[3] * inv)};
  }
#pragma unroll
  for (int u = 0; u < 4; ++u) {
    f32x4 o[2][2];
#pragma unroll
    for (int E = 0; E < 2; ++E) {
      const int e = 32 * u + p32(E, c);
      bf16x8 vx = *(const bf16x8*)(VT + (((size_t)s * 32 + h) * 128 + e) * 32 + 8 * g);
      u32x2 vm = {0, 0};
      if (g < 2) vm = *(const u32x2*)(VmT + (((size_t)s * 32 + h) * 128 + e) * 8 + 4 * g);
#pragma unroll
      for (int T = 0; T < 2; ++T) {
        o[E][T] = mfma32(vx, as_bf8(px[T]), z4);
        o[E][T] = mfma16(as_bf4(vm), as_bf4(pm[T]), o[E][T]);
      }
    }
#pragma unroll
    for (int T = 0; T < 2; ++T) {
      u32x4 v = {pack2(o[0][T].x, o[0][T].y), pack2(o[0][T].z, o[0][T].w), pack2(o[1][T].x, o[1][T].y), pack2(o[1][T].z, o[1][T].w)};
      *(u32x4*)(of + (((size_t)(2 * s + T) * 128 + (4 * h + u)) * 64 + l) * 8) = v;
    }
  }
}

__global__ __launch_bounds__(256, 2) void wo_gemm_kernel(const u16* __restrict__ x, char* __restrict__ ws, u16* __restrict__ out) {
  extern __shared__ __attribute__((aligned(16))) char smem[];
  const int tid = threadIdx.x, w = __builtin_amdgcn_readfirstlane(tid >> 6), l = tid & 63;
  const int nb = blockIdx.x % 5, mb = blockIdx.x / 5;
  const char* of = ws + OFF_OF;
  const char* wof = ws + OFF_WOF;
  const int wr = w >> 1, wc = w & 1;
  f32x4 acc[2][4];
#pragma unroll
  for (int i = 0; i < 2; ++i)
#pragma unroll
    for (int j = 0; j < 4; ++j) acc[i][j] = (f32x4){0, 0, 0, 0};
  auto stage = [&](int t, int buf) {
#pragma unroll
    for (int kk = 0; kk < 2; ++kk) {
#pragma unroll
      for (int i = 0; i < 3; ++i) {
        int f = 3 * w + i;
        int ks = 2 * t + kk;
        const char* src = (f < 4) ? of + (((size_t)(mb * 4 + f) * 128 + ks) * 1024) : wof + (((size_t)(nb * 8 + (f - 4)) * 128 + ks) * 1024);
        glds16(src + l * 16, smem + buf * 24576 + (kk * 12 + f) * 1024 + l * 16);
      }
    }
  };
  stage(0, 0);
  for (int t = 0; t < 64; ++t) {
    asm volatile("s_waitcnt vmcnt(0)" ::: "memory");
    __syncthreads();
    if (t + 1 < 64) stage(t + 1, (t + 1) & 1);
    const char* base = smem + (t & 1) * 24576;
#pragma unroll
    for (int kk = 0; kk < 2; ++kk) {
      bf16x8 a[2], bb[4];
#pragma unroll
      for (int i = 0; i < 2; ++i) a[i] = *(const bf16x8*)(base + (kk * 12 + 2 * wr + i) * 1024 + l * 16);
#pragma unroll
      for (int j = 0; j < 4; ++j) bb[j] = *(const bf16x8*)(base + (kk * 12 + 4 + 4 * wc + j) * 1024 + l * 16);
#pragma unroll
      for (int i = 0; i < 2; ++i)
#pragma unroll
        for (int j = 0; j < 4; ++j) acc[i][j] = mfma32(bb[j], a[i], acc[i][j]);
    }
  }
  const int c = l & 15, g = l >> 4;
  const u16* xn = (const u16*)(ws + OFF_XN);
  const int* sel = (const int*)(ws + OFF_SEL);
  const float* rsel = (const float*)(ws + OFF_SEL) + 8;
  const float* yp = (const float*)(ws + OFF_YP);
#pragma unroll
  for (int i = 0; i < 2; ++i) {
    const int mt = mb * 4 + 2 * wr + i;
    const int tok = 32 * (mt >> 1) + p32(mt & 1, c);
    int slot = -1;
#pragma unroll
    for (int k = 0; k < 8; ++k) if (sel[k] == tok) slot = k;
#pragma unroll
    for (int u = 0; u < 2; ++u) {
      const int n0 = nb * 128 + 32 * (2 * wc + u) + 8 * g;
      f32x4 a0 = acc[i][2 * u], a1 = acc[i][2 * u + 1];
      float a[8] = {a0.x, a0.y, a0.z, a0.w, a1.x, a1.y, a1.z, a1.w};
      u32x4 xv = *(const u32x4*)(x + (size_t)tok * DM + n0);
      u32x4 nv = *(const u32x4*)(xn + (size_t)tok * DM + n0);
      float xf[8] = {bflo(xv.x), bfhi(xv.x), bflo(xv.y), bfhi(xv.y), bflo(xv.z), bfhi(xv.z), bflo(xv.w), bfhi(xv.w)};
      float nf[8] = {bflo(nv.x), bfhi(nv.x), bflo(nv.y), bfhi(nv.y), bflo(nv.z), bfhi(nv.z), bflo(nv.w), bfhi(nv.w)};
      if (slot >= 0) {
        const float rs = rsel[slot];
#pragma unroll
        for (int j = 0; j < 8; ++j) {
          float y = 0.f;
#pragma unroll
          for (int kb = 0; kb < 8; ++kb) y += yp[((size_t)kb * 8 + slot) * DM + n0 + j];
          y = bfr(bfr(y) * rs);
          nf[j] = bfr(nf[j] + y);
        }
      }
      float o[8];
#pragma unroll
      for (int j = 0; j < 8; ++j) o[j] = bfr(xf[j] + bfr(a[j])) + nf[j];
      u32x4 ov = {pack2(o[0], o[1]), pack2(o[2], o[3]), pack2(o[4], o[5]), pack2(o[6], o[7])};
      *(u32x4*)(out + (size_t)tok * DM + n0) = ov;
    }
  }
}

extern "C" void kernel_launch(void* const* d_in, const int* in_sizes, int n_in, void* d_out, int out_size, void* d_ws, size_t ws_size,
                              hipStream_t stream) {
  const u16* x = (const u16*)d_in[0];
  const u16* mem0 = (const u16*)d_in[1];
  const u16* Wq = (const u16*)d_in[2];
  const u16* Wk = (const u16*)d_in[3];
  const u16* Wv = (const u16*)d_in[4];
  const u16* Wo = (const u16*)d_in[5];
  const u16* w_r = (const u16*)d_in[8];
  const u16* wg = (const u16*)d_in[9];
  const u16* wu = (const u16*)d_in[10];
  const u16* wd = (const u16*)d_in[11];
  char* ws = (char*)d_ws;
  u16* out = (u16*)d_out;
  (void)hipFuncSetAttribute((const void*)chain_kernel, hipFuncAttributeMaxDynamicSharedMemorySize, CH_LDS);
  (void)hipFuncSetAttribute((const void*)qkv_gemm_kernel, hipFuncAttributeMaxDynamicSharedMemorySize, 65536);
  prep_kernel<<<PREP_BLOCKS, 256, 0, stream>>>(x, mem0, Wq, Wk, Wv, Wo, w_r, ws);
  qkv_gemm_kernel<<<16 * 96, 256, 65536, stream>>>(ws);
  mod_gateup_kernel<<<14, 256, 0, stream>>>(wg, wu, ws);
  mod_down_kernel<<<40, 256, 0, stream>>>(wd, ws);
  chain_kernel<<<128, 256, CH_LDS, stream>>>(ws);
  attn_kernel<<<512, 256, 0, stream>>>(ws);
  wo_gemm_kernel<<<160, 256, 49152, stream>>>(x, ws, out);
}
```

```cpp
#include <hip/hip_runtime.h>
#include <hip/hip_bf16.h>
#include <stdint.h>

constexpr int S_TOK = 2048, DM = 640, NH = 32, DH = 128, HD = 4096, NMEM = 8, SEG = 32, NSEG = 64;
constexpr int FF = 1728, CSEL = 8;
constexpr int KS_DM = DM / 32;
constexpr int KS_HD = HD / 32;

typedef unsigned int u32;
typedef unsigned short u16;
typedef __attribute__((ext_vector_type(4))) u32 u32x4;
typedef __attribute__((ext_vector_type(2))) u32 u32x2;
typedef __attribute__((ext_vector_type(4))) float f32x4;
typedef __attribute__((ext_vector_type(8))) short bf16x8;
typedef __attribute__((ext_vector_type(4))) short bf16x4;
typedef __attribute__((ext_vector_type(2))) __bf16 nbf16x2;

#define SENT 0xFFFFFFFFu

__device__ __forceinline__ float bf2f(u16 v) { return __uint_as_float(((u32)v) << 16); }
__device__ __forceinline__ float bflo(u32 v) { return __uint_as_float(v << 16); }
__device__ __forceinline__ float bfhi(u32 v) { return __uint_as_float(v & 0xffff0000u); }
__device__ __forceinline__ u32 pack2(float a, float b) {
  nbf16x2 v = {(__bf16)a, (__bf16)b};
  return __builtin_bit_cast(u32, v);
}
__device__ __forceinline__ float bfr(float a) { return (float)(__bf16)a; }
__device__ __forceinline__ bf16x8 as_bf8(u32x4 v) { return __builtin_bit_cast(bf16x8, v); }
__device__ __forceinline__ bf16x4 as_bf4(u32x2 v) { return __builtin_bit_cast(bf16x4, v); }
__device__ __forceinline__ f32x4 mfma32(bf16x8 a, bf16x8 b, f32x4 c) { return __builtin_amdgcn_mfma_f32_16x16x32_bf16(a, b, c, 0, 0, 0); }
__device__ __forceinline__ f32x4 mfma16(bf16x4 a, bf16x4 b, f32x4 c) {
  u32x2 a2 = __builtin_bit_cast(u32x2, a), b2 = __builtin_bit_cast(u32x2, b);
  u32x4 a4 = {a2.x, a2.y, 0u, 0u}, b4 = {b2.x, b2.y, 0u, 0u};
  return __builtin_amdgcn_mfma_f32_16x16x32_bf16(__builtin_bit_cast(bf16x8, a4), __builtin_bit_cast(bf16x8, b4), c, 0, 0, 0);
}
__device__ __forceinline__ float xr16_max(float v) { auto r = __builtin_amdgcn_permlane16_swap(__float_as_uint(v), __float_as_uint(v), false, false); return fmaxf(__uint_as_float(r[0]), __uint_as_float(r[1])); }
__device__ __forceinline__ float xr32_max(float v) { auto r = __builtin_amdgcn_permlane32_swap(__float_as_uint(v), __float_as_uint(v), false, false); return fmaxf(__uint_as_float(r[0]), __uint_as_float(r[1])); }
__device__ __forceinline__ float xr16_add(float v) { auto r = __builtin_amdgcn_permlane16_swap(__float_as_uint(v), __float_as_uint(v), false, false); return __uint_as_float(r[0]) + __uint_as_float(r[1]); }
__device__ __forceinline__ float xr32_add(float v) { auto r = __builtin_amdgcn_permlane32_swap(__float_as_uint(v), __float_as_uint(v), false, false); return __uint_as_float(r[0]) + __uint_as_float(r[1]); }
__device__ __forceinline__ float xr8_add(float v) { return v + __uint_as_float(__builtin_amdgcn_update_dpp(0u, __float_as_uint(v), 0x128  , 0xf, 0xf, false)); }
__device__ __forceinline__ int p32(int T, int r) { return 8 * (r >> 2) + 4 * T + (r & 3); }

constexpr size_t SZ_XN = (size_t)S_TOK * DM * 2;
constexpr size_t OFF_XN = 0;
constexpr size_t OFF_XNF = OFF_XN + SZ_XN;
constexpr size_t OFF_R = OFF_XNF + SZ_XN;
constexpr size_t OFF_WF = OFF_R + 8192;
constexpr size_t SZ_WF = (size_t)768 * 20 * 1024;
constexpr size_t OFF_WOF = OFF_WF + SZ_WF;
constexpr size_t SZ_WOF = (size_t)40 * 128 * 1024;
constexpr size_t OFF_Q = OFF_WOF + SZ_WOF;
constexpr size_t SZ_Q = (size_t)S_TOK * HD * 2;
constexpr size_t OFF_K = OFF_Q + SZ_Q;
constexpr size_t OFF_VT = OFF_K + SZ_Q;
constexpr size_t OFF_KM = OFF_VT + SZ_Q;
constexpr size_t SZ_KM = (size_t)NSEG * NMEM * HD * 2;
constexpr size_t OFF_VMT = OFF_KM + SZ_KM;
constexpr size_t OFF_OF = OFF_VMT + SZ_KM;
constexpr size_t OFF_INBOX = OFF_OF + SZ_Q;
constexpr u32 NPROD = 128, NRED = 80;
constexpr u32 INBOX_SLOT = NRED * NPROD * 128;
constexpr size_t OFF_MEMB = OFF_INBOX + 2 * (size_t)INBOX_SLOT;
constexpr u32 MEM_STEP = NRED * 128;
constexpr size_t SZ_MEMB = (size_t)65 * MEM_STEP;
constexpr size_t OFF_HB = OFF_MEMB + ((SZ_MEMB + 255) / 256) * 256;
constexpr size_t OFF_YP = OFF_HB + 32768;
constexpr size_t OFF_SEL = OFF_YP + (size_t)8 * 8 * 640 * 4;
constexpr size_t OFF_ERR = OFF_SEL + 256;
constexpr size_t OFF_MCNT = OFF_ERR + 256;
constexpr size_t OFF_OCNT = OFF_MCNT + (size_t)64 * 32 * 64;
constexpr size_t OFF_FLAG_END = OFF_OCNT + 16 * 256;
constexpr size_t WS_NEED = OFF_FLAG_END;

constexpr int PREP_LN_BLOCKS = S_TOK / 4;
constexpr int PREP_WQKV_BLOCKS = 3 * 20 * 16;
constexpr int PREP_WO_BLOCKS = 128 * 3;
constexpr int PREP_INIT_BLOCKS = 64;
constexpr int PREP_BLOCKS = PREP_LN_BLOCKS + PREP_WQKV_BLOCKS + PREP_WO_BLOCKS + PREP_INIT_BLOCKS;

__device__ __forceinline__ void tile_weights(const u16* __restrict__ W, int N, int k0, int n0, int ncols, u16* __restrict__ dst,
                                             int nks_total, int ks, int nt0, u16* lds) {
  const int tid = threadIdx.x;
  const int chunks_per_row = ncols / 8;
  for (int i = tid; i < 32 * chunks_per_row; i += 256) {
    int row = i / chunks_per_row, ch = i % chunks_per_row;
    u32x4 v = *(const u32x4*)(W + (size_t)(k0 + row) * N + n0 + ch * 8);
    *(u32x4*)(lds + row * 264 + ch * 8) = v;
  }
  __syncthreads();
  const int ntiles = ncols / 16;
  for (int o = tid; o < ntiles * 64; o += 256) {
    int t = o >> 6, l = o & 63, r = l & 15, g = l >> 4;
    int col = 32 * (t >> 1) + p32(t & 1, r);
    u16 e[8];
#pragma unroll
    for (int j = 0; j < 8; ++j) e[j] = lds[(8 * g + j) * 264 + col];
    u32x4 v = {(u32)e[0] | ((u32)e[1] << 16), (u32)e[2] | ((u32)e[3] << 16), (u32)e[4] | ((u32)e[5] << 16), (u32)e[6] | ((u32)e[7] << 16)};
    *(u32x4*)(dst + (((size_t)(nt0 + t) * nks_total + ks) * 64 + l) * 8) = v;
  }
}

__global__ __launch_bounds__(256) void prep_kernel(const u16* __restrict__ x, const u16* __restrict__ mem0, const u16* __restrict__ Wq,
                                                   const u16* __restrict__ Wk, const u16* __restrict__ Wv, const u16* __restrict__ Wo,
                                                   const u16* __restrict__ w_r, char* __restrict__ ws) {
  __shared__ __attribute__((aligned(16))) u16 lds[32 * 264];
  const int b = blockIdx.x, tid = threadIdx.x;
  if (b < PREP_LN_BLOCKS) {
    const int w = tid >> 6, l = tid & 63;
    const int t = b * 4 + w;
    const u16* xr = x + (size_t)t * DM;
    u32x4 c0 = *(const u32x4*)(xr + l * 8);
    u32x4 c1 = {0, 0, 0, 0};
    if (l < 16) c1 = *(const u32x4*)(xr + 512 + l * 8);
    float v[16];
    v[0] = bflo(c0.x); v[1] = bfhi(c0.x); v[2] = bflo(c0.y); v[3] = bfhi(c0.y); v[4] = bflo(c0.z); v[5] = bfhi(c0.z); v[6] = bflo(c0.w); v[7] = bfhi(c0.w);
    v[8] = bflo(c1.x); v[9] = bfhi(c1.x); v[10] = bflo(c1.y); v[11] = bfhi(c1.y); v[12] = bflo(c1.z); v[13] = bfhi(c1.z); v[14] = bflo(c1.w); v[15] = bfhi(c1.w);
    float s = 0.f;
#pragma unroll
    for (int j = 0; j < 16; ++j) s += v[j];
#pragma unroll
    for (int o = 32; o > 0; o >>= 1) s += __shfl_xor(s, o);
    const float mean = s * (1.0f / DM);
    float q = 0.f;
#pragma unroll
    for (int j = 0; j < 8; ++j) { float d = v[j] - mean; q += d * d; }
    if (l < 16) {
#pragma unroll
      for (int j = 8; j < 16; ++j) { float d = v[j] - mean; q += d * d; }
    }
#pragma unroll
    for (int o = 32; o > 0; o >>= 1) q += __shfl_xor(q, o);
    const float var_b = bfr(q * (1.0f / DM));
    const float mean_b = bfr(mean);
    const float den = bfr(sqrtf(bfr(var_b + 1e-05f)));
    float y[16];
#pragma unroll
    for (int j = 0; j < 16; ++j) y[j] = bfr(bfr(v[j] - mean_b) / den);
    u32x4 w0 = *(const u32x4*)(w_r + l * 8);
    u32x4 w1 = {0, 0, 0, 0};
    if (l < 16) w1 = *(const u32x4*)(w_r + 512 + l * 8);
    float rs = y[0] * bflo(w0.x) + y[1] * bfhi(w0.x) + y[2] * bflo(w0.y) + y[3] * bfhi(w0.y) + y[4] * bflo(w0.z) + y[5] * bfhi(w0.z) + y[6] * bflo(w0.w) + y[7] * bfhi(w0.w);
    if (l < 16) rs += y[8] * bflo(w1.x) + y[9] * bfhi(w1.x) + y[10] * bflo(w1.y) + y[11] * bfhi(w1.y) + y[12] * bflo(w1.z) + y[13] * bfhi(w1.z) + y[14] * bflo(w1.w) + y[15] * bfhi(w1.w);
#pragma unroll
    for (int o = 32; o > 0; o >>= 1) rs += __shfl_xor(rs, o);
    if (l == 0) ((float*)(ws + OFF_R))[t] = bfr(rs);
    u16* xn = (u16*)(ws + OFF_XN);
    u16* xnf = (u16*)(ws + OFF_XNF);
    const int sg = t >> 5, tt = t & 31;
    const int mt = 2 * sg + ((tt >> 2) & 1), r = 4 * (tt >> 3) + (tt & 3);
    u32x4 o0 = {pack2(y[0], y[1]), pack2(y[2], y[3]), pack2(y[4], y[5]), pack2(y[6], y[7])};
    *(u32x4*)(xn + (size_t)t * DM + l * 8) = o0;
    { int c = l; *(u32x4*)(xnf + (((size_t)mt * 20 + (c >> 2)) * 64 + (c & 3) * 16 + r) * 8) = o0; }
    if (l < 16) {
      u32x4 o1 = {pack2(y[8], y[9]), pack2(y[10], y[11]), pack2(y[12], y[13]), pack2(y[14], y[15])};
      *(u32x4*)(xn + (size_t)t * DM + 512 + l * 8) = o1;
      int c = 64 + l;
      *(u32x4*)(xnf + (((size_t)mt * 20 + (c >> 2)) * 64 + (c & 3) * 16 + r) * 8) = o1;
    }
  } else if (b < PREP_LN_BLOCKS + PREP_WQKV_BLOCKS) {
    int i = b - PREP_LN_BLOCKS;
    int mat = i / 320, rem = i % 320, ks = rem / 16, nb = rem % 16;
    const u16* W = mat == 0 ? Wq : (mat == 1 ? Wk : Wv);
    tile_weights(W, HD, ks * 32, nb * 256, 256, (u16*)(ws + OFF_WF), 20, ks, mat * 256 + nb * 16, lds);
  } else if (b < PREP_LN_BLOCKS + PREP_WQKV_BLOCKS + PREP_WO_BLOCKS) {
    int i = b - PREP_LN_BLOCKS - PREP_WQKV_BLOCKS;
    int ks = i / 3, nb = i % 3;
    tile_weights(Wo, DM, ks * 32, nb * 256, nb == 2 ? 128 : 256, (u16*)(ws + OFF_WOF), 128, ks, nb * 16, lds);
  } else {
    int i = b - (PREP_LN_BLOCKS + PREP_WQKV_BLOCKS + PREP_WO_BLOCKS);
    u32x4 sv = {SENT, SENT, SENT, SENT};
    u32x4* p = (u32x4*)(ws + OFF_INBOX);
    const size_t n16 = (2 * (size_t)INBOX_SLOT) / 16;
    for (size_t j = (size_t)i * 256 + tid; j < n16; j += (size_t)PREP_INIT_BLOCKS * 256) p[j] = sv;
    u32x4* pm = (u32x4*)(ws + OFF_MEMB + MEM_STEP);
    const size_t m16 = (size_t)64 * MEM_STEP / 16;
    for (size_t j = (size_t)i * 256 + tid; j < m16; j += (size_t)PREP_INIT_BLOCKS * 256) pm[j] = sv;
    if (i == 0) {
      for (int j = tid; j < 640; j += 256) {
        int p_ = j >> 3, m = j & 7;
        u32x4 v = *(const u32x4*)(mem0 + m * DM + p_ * 8);
        if (v.x == SENT) v.x = 0xFFC0FFC0u;
        if (v.y == SENT) v.y = 0xFFC0FFC0u;
        if (v.z == SENT) v.z = 0xFFC0FFC0u;
        if (v.w == SENT) v.w = 0xFFC0FFC0u;
        *(u32x4*)(ws + OFF_MEMB + (size_t)j * 16) = v;
      }
      if (tid == 0) *(u32*)(ws + OFF_ERR) = 0;
    }
    if (i == 1) {
      u32x4* pf = (u32x4*)(ws + OFF_MCNT);
      for (int j = tid; j < (int)((OFF_FLAG_END - OFF_MCNT) / 16); j += 256) pf[j] = (u32x4){0, 0, 0, 0};
    }
  }
}

__device__ __forceinline__ void glds16(const void* g, void* l) {
  __builtin_amdgcn_global_load_lds((const __attribute__((address_space(1))) void*)g, (__attribute__((address_space(3))) void*)l, 16, 0, 0);
}

__global__ __launch_bounds__(256, 2) void qkv_gemm_kernel(char* __restrict__ ws) {
  extern __shared__ __attribute__((aligned(16))) char smem[];
  const int tid = threadIdx.x, w = __builtin_amdgcn_readfirstlane(tid >> 6), l = tid & 63;
  const int nb = 32 + blockIdx.x % 64, mb = blockIdx.x / 64;
  const char* xnf = ws + OFF_XNF;
  const char* wf = ws + OFF_WF;
  const int mat = nb >> 5, h = nb & 31;
  const int wr = w >> 1, wc = w & 1;
  f32x4 acc[4][4];
#pragma unroll
  for (int i = 0; i < 4; ++i)
#pragma unroll
    for (int j = 0; j < 4; ++j) acc[i][j] = (f32x4){0, 0, 0, 0};

  auto stage = [&](int t, int buf) {
#pragma unroll
    for (int kk = 0; kk < 2; ++kk) {
#pragma unroll
      for (int i = 0; i < 4; ++i) {
        int f = 4 * w + i;
        int ks = 2 * t + kk;
        const char* src = (f < 8) ? xnf + (((size_t)(mb * 8 + f) * 20 + ks) * 1024) : wf + (((size_t)(nb * 8 + (f - 8)) * 20 + ks) * 1024);
        glds16(src + l * 16, smem + buf * 32768 + (kk * 16 + f) * 1024 + l * 16);
      }
    }
  };
  stage(0, 0);
  for (int t = 0; t < 10; ++t) {
    asm volatile("s_waitcnt vmcnt(0)" ::: "memory");
    __syncthreads();
    if (t + 1 < 10) stage(t + 1, (t + 1) & 1);
    const char* base = smem + (t & 1) * 32768;
#pragma unroll
    for (int kk = 0; kk < 2; ++kk) {
      bf16x8 a[4], bb[4];
#pragma unroll
      for (int i = 0; i < 4; ++i) a[i] = *(const bf16x8*)(base + (kk * 16 + 4 * wr + i) * 1024 + l * 16);
#pragma unroll
      for (int j = 0; j < 4; ++j) bb[j] = *(const bf16x8*)(base + (kk * 16 + 8 + 4 * wc + j) * 1024 + l * 16);
      if (mat < 2) {
#pragma unroll
        for (int i = 0; i < 4; ++i)
#pragma unroll
          for (int j = 0; j < 4; ++j) acc[i][j] = mfma32(bb[j], a[i], acc[i][j]);
      } else {
#pragma unroll
        for (int i = 0; i < 4; ++i)
#pragma unroll
          for (int j = 0; j < 4; ++j) acc[i][j] = mfma32(a[i], bb[j], acc[i][j]);
      }
    }
  }
  const int c = l & 15, g = l >> 4;
  if (mat < 2) {
    u16* dst = (u16*)(ws + (mat == 0 ? OFF_Q : OFF_K));
#pragma unroll
    for (int i = 0; i < 4; ++i) {
      int mt = mb * 8 + 4 * wr + i;
      int tok = 32 * (mt >> 1) + p32(mt & 1, c);
#pragma unroll
      for (int u = 0; u < 2; ++u) {
        f32x4 a0 = acc[i][2 * u], a1 = acc[i][2 * u + 1];
        u32x4 v = {pack2(a0.x, a0.y), pack2(a0.z, a0.w), pack2(a1.x, a1.y), pack2(a1.z, a1.w)};
        *(u32x4*)(dst + (size_t)tok * HD + h * 128 + 32 * (2 * wc + u) + 8 * g) = v;
      }
    }
  } else {
    u16* dst = (u16*)(ws + OFF_VT);
#pragma unroll
    for (int ip = 0; ip < 2; ++ip) {
      int sg = mb * 4 + 2 * wr + ip;
#pragma unroll
      for (int j = 0; j < 4; ++j) {
        int ntl = 4 * wc + j;
        int e = 32 * (ntl >> 1) + p32(ntl & 1, c);
        f32x4 a0 = acc[2 * ip][j], a1 = acc[2 * ip + 1][j];
        u32x4 v = {pack2(a0.x, a0.y), pack2(a0.z, a0.w), pack2(a1.x, a1.y), pack2(a1.z, a1.w)};
        *(u32x4*)(dst + (((size_t)sg * 32 + h) * 128 + e) * 32 + 8 * g) = v;
      }
    }
  }
}

constexpr u32 SPIN_CAP = 1u << 16;
__device__ __forceinline__ bool has_sent(u32x4 v) { return (v.x == SENT) | (v.y == SENT) | (v.z == SENT) | (v.w == SENT); }
__device__ __forceinline__ u32 desent(float f) { u32 b = __float_as_uint(f); return b == SENT ? 0x7FC00000u : b; }

constexpr int CH_MEMF = 0, CH_PART = 20480, CH_WV = 40960, CH_WO = 81920, CH_SRED = 122880, CH_LDS = 123904;

#define MFMA_A(acc, wa, mb) asm volatile("v_mfma_f32_16x16x32_bf16 %0, %1, %2, %0" : "+v"(acc) : "a"(wa), "v"(mb))
#define MFMA_V(acc, wa, mb) asm volatile("v_mfma_f32_16x16x32_bf16 %0, %1, %2, %0" : "+v"(acc) : "v"(wa), "v"(mb))
#define MFMA_A0(acc, wa, mb) asm volatile("v_mfma_f32_16x16x32_bf16 %0, %1, %2, 0" : "=&v"(acc) : "a"(wa), "v"(mb))
#define MFMA_V0(acc, wa, mb) asm volatile("v_mfma_f32_16x16x32_bf16 %0, %1, %2, 0" : "=&v"(acc) : "v"(wa), "v"(mb))
#define MFMA_NV(acc, wa, mb) asm volatile("s_nop 1\n\tv_mfma_f32_16x16x32_bf16 %0, %1, %2, %0" : "+v"(acc) : "v"(wa), "v"(mb))
#define MFMA_NV0(acc, wa, mb) asm volatile("s_nop 1\n\tv_mfma_f32_16x16x32_bf16 %0, %1, %2, 0" : "=&v"(acc) : "v"(wa), "v"(mb))

#define STAMP(i)
__device__ __forceinline__ void chain_role(char* __restrict__ ws, char* smem, const int c) {
  const int tid = threadIdx.x, w = __builtin_amdgcn_readfirstlane(tid >> 6), l = tid & 63;
  const int h = c >> 2, gs = c & 3;
  const int m = l & 15, g = l >> 4;
  const char* wf = ws + OFF_WF;
  const char* wof = ws + OFF_WOF;
  bf16x8 wq[2][20], wk[2][20];
#pragma unroll
  for (int T = 0; T < 2; ++T)
#pragma unroll
    for (int i = 0; i < 20; ++i) {
      const int ks = (5 * w + i) % 20;
      wq[T][i] = *(const bf16x8*)(wf + (((size_t)(2 * (4 * h + w) + T) * 20 + ks) * 64 + l) * 16);
      wk[T][i] = *(const bf16x8*)(wf + (((size_t)(256 + 2 * (4 * h + w) + T) * 20 + ks) * 64 + l) * 16);
    }
  for (int i = tid; i < 40 * 64; i += 256) {
    int f = i >> 6, ll = i & 63;
    *(u32x4*)(smem + CH_WV + i * 16) = *(const u32x4*)(wf + (((size_t)(512 + 2 * (4 * h + gs) + f / 20) * 20 + f % 20) * 64 + ll) * 16);
    *(u32x4*)(smem + CH_WO + i * 16) = *(const u32x4*)(wof + (((size_t)f * 128 + (4 * h + gs)) * 64 + ll) * 16);
  }
  for (int i = tid; i < 1280; i += 256) *(u32x4*)(smem + CH_MEMF + i * 16) = (u32x4){0, 0, 0, 0};
  __syncthreads();

  const __amdgpu_buffer_rsrc_t rin = __builtin_amdgcn_make_buffer_rsrc(ws + OFF_INBOX, 0, 2 * INBOX_SLOT, 0x00020000);
  const __amdgpu_buffer_rsrc_t rmem = __builtin_amdgcn_make_buffer_rsrc(ws + OFF_MEMB, 0, (u32)SZ_MEMB, 0x00020000);
  const u16* Kx = (const u16*)(ws + OFF_K);
  const u16* VT = (const u16*)(ws + OFF_VT);
  const __amdgpu_buffer_rsrc_t rkm = __builtin_amdgcn_make_buffer_rsrc(ws + OFF_KM, 0, (u32)(2 * SZ_KM), 0x00020000);
  u32* mcnt = (u32*)(ws + OFF_MCNT);
  u32* err = (u32*)(ws + OFF_ERR);
  const float scale = 0.08838834764831845f;
  const char* lmem = smem + CH_MEMF + l * 16;

  for (int s = 0; s < NSEG; ++s) {
    bf16x8 kx[2], vx[2], wv[2][5];
#pragma unroll
    for (int T = 0; T < 2; ++T) {
      kx[T] = *(const bf16x8*)(Kx + (size_t)(32 * s + p32(T, m)) * HD + h * 128 + 32 * w + 8 * g);
      vx[T] = *(const bf16x8*)(VT + (((size_t)s * 32 + h) * 128 + 32 * gs + p32(T, m)) * 32 + 8 * g);
#pragma unroll
      for (int i = 0; i < 5; ++i) wv[T][i] = *(const bf16x8*)(smem + CH_WV + (T * 20 + 5 * w + i) * 1024 + l * 16);
    }
    {
      const u32 base = s * MEM_STEP + w * 2560;
      u32x4 a, b, d;
      u32 spins = 0;
      for (;;) {
        a = __builtin_amdgcn_raw_buffer_load_b128(rmem, base + l * 16, 0, 16);
        b = __builtin_amdgcn_raw_buffer_load_b128(rmem, base + 1024 + l * 16, 0, 16);
        d = __builtin_amdgcn_raw_buffer_load_b128(rmem, base + 2048 + (l & 31) * 16, 0, 16);
        bool bad = has_sent(a) | has_sent(b) | has_sent(d);
        if (!__any(bad)) break;
        if (++spins > SPIN_CAP) { if (l == 0) *err = 1; break; }
      }
      int i0 = w * 160 + l, i1 = i0 + 64, i2 = i0 + 128;
      *(u32x4*)(smem + CH_MEMF + ((i0 >> 3) * 16 + (i0 & 7)) * 16) = a;
      *(u32x4*)(smem + CH_MEMF + ((i1 >> 3) * 16 + (i1 & 7)) * 16) = b;
      if (l < 32) *(u32x4*)(smem + CH_MEMF + ((i2 >> 3) * 16 + (i2 & 7)) * 16) = d;
    }
    STAMP(0);
    __syncthreads();
    STAMP(1);
    if (s > 0 && tid == 0) __hip_atomic_fetch_add(mcnt + ((s - 1) * 32 + h) * 16, 1u, __ATOMIC_RELAXED, __HIP_MEMORY_SCOPE_AGENT);
    const f32x4 z4 = {0, 0, 0, 0};
    f32x4 aq0, aq1, ak0, ak1, av0, av1;
    {
      bf16x8 mfA[5], mfB[5];
      const char* lc0 = lmem + (5 * w) * 1024;
      const char* lc1 = lmem + (5 * ((w + 1) & 3)) * 1024;
      const char* lc2 = lmem + (5 * ((w + 2) & 3)) * 1024;
      const char* lc3 = lmem + (5 * ((w + 3) & 3)) * 1024;
#pragma unroll
      for (int i = 0; i < 5; ++i) mfA[i] = *(const bf16x8*)(lc0 + i * 1024);
#pragma unroll
      for (int i = 0; i < 5; ++i) mfB[i] = *(const bf16x8*)(lc1 + i * 1024);
      MFMA_V0(av0, mfA[0], wv[0][0]); MFMA_V0(av1, mfA[0], wv[1][0]);
#pragma unroll
      for (int i = 1; i < 5; ++i) { MFMA_V(av0, mfA[i], wv[0][i]); MFMA_V(av1, mfA[i], wv[1][i]); }
      MFMA_A0(aq0, wq[0][0], mfA[0]); MFMA_A0(aq1, wq[1][0], mfA[0]); MFMA_A0(ak0, wk[0][0], mfA[0]); MFMA_A0(ak1, wk[1][0], mfA[0]);
#pragma unroll
      for (int i = 1; i < 5; ++i) { MFMA_A(aq0, wq[0][i], mfA[i]); MFMA_A(aq1, wq[1][i], mfA[i]); MFMA_A(ak0, wk[0][i], mfA[i]); MFMA_A(ak1, wk[1][i], mfA[i]); }
#pragma unroll
      for (int i = 0; i < 5; ++i) mfA[i] = *(const bf16x8*)(lc2 + i * 1024);
#pragma unroll
      for (int i = 0; i < 5; ++i) { MFMA_A(aq0, wq[0][5 + i], mfB[i]); MFMA_A(aq1, wq[1][5 + i], mfB[i]); MFMA_A(ak0, wk[0][5 + i], mfB[i]); MFMA_A(ak1, wk[1][5 + i], mfB[i]); }
#pragma unroll
      for (int i = 0; i < 5; ++i) mfB[i] = *(const bf16x8*)(lc3 + i * 1024);
#pragma unroll
      for (int i = 0; i < 5; ++i) { MFMA_A(aq0, wq[0][10 + i], mfA[i]); MFMA_A(aq1, wq[1][10 + i], mfA[i]); MFMA_A(ak0, wk[0][10 + i], mfA[i]); MFMA_A(ak1, wk[1][10 + i], mfA[i]); }
      MFMA_A(aq0, wq[0][15], mfB[0]); MFMA_A(aq1, wq[1][15], mfB[0]); MFMA_A(ak0, wk[0][15], mfB[0]); MFMA_A(ak1, wk[1][15], mfB[0]);
#pragma unroll
      for (int i = 1; i < 5; ++i) { MFMA_V(aq0, wq[0][15 + i], mfB[i]); MFMA_V(aq1, wq[1][15 + i], mfB[i]); MFMA_V(ak0, wk[0][15 + i], mfB[i]); MFMA_V(ak1, wk[1][15 + i], mfB[i]); }
      asm volatile("s_nop 7\n\ts_nop 7" : "+v"(aq0), "+v"(aq1), "+v"(ak0), "+v"(ak1), "+v"(av0), "+v"(av1));
    }
    STAMP(2);
    bf16x8 wo[10];
#pragma unroll
    for (int i = 0; i < 10; ++i) wo[i] = *(const bf16x8*)(smem + CH_WO + (10 * w + i) * 1024 + l * 16);
    u32x4 qp = {pack2(aq0.x, aq0.y), pack2(aq0.z, aq0.w), pack2(aq1.x, aq1.y), pack2(aq1.z, aq1.w)};
    u32x4 kp = {pack2(ak0.x, ak0.y), pack2(ak0.z, ak0.w), pack2(ak1.x, ak1.y), pack2(ak1.z, ak1.w)};
    f32x4 sM, sX0, sX1;
    MFMA_NV0(sM, kp, qp); MFMA_V0(sX0, kx[0], qp); MFMA_V0(sX1, kx[1], qp);
    asm volatile("s_nop 7\n\ts_nop 1" : "+v"(sM), "+v"(sX0), "+v"(sX1));
    {
      char* pp = smem + CH_PART + w * 5120 + l * 16;
      *(f32x4*)(pp + 3072) = av0; *(f32x4*)(pp + 4096) = av1;
      *(f32x4*)(pp) = sM; *(f32x4*)(pp + 1024) = sX0; *(f32x4*)(pp + 2048) = sX1;
    }
    if (w == gs && m < 8) __builtin_amdgcn_raw_buffer_store_b128(kp, rkm, (u32)(((s * 8 + m) * HD + h * 128 + 32 * w + 8 * g) * 2), 0, 16);
    __syncthreads();
    STAMP(3);
    f32x4 t[5];
#pragma unroll
    for (int i = 0; i < 5; ++i) {
      const char* pp = smem + CH_PART + i * 1024 + l * 16;
      f32x4 a0 = *(const f32x4*)(pp), a1 = *(const f32x4*)(pp + 5120), a2 = *(const f32x4*)(pp + 10240), a3 = *(const f32x4*)(pp + 15360);
      t[i] = (a0 + a1) + (a2 + a3);
    }
    float sm_[4], sx[8];
#pragma unroll
    for (int i = 0; i < 4; ++i) {
      sm_[i] = g < 2 ? bfr(bfr(t[0][i]) * scale) : -INFINITY;
      sx[i] = bfr(bfr(t[1][i]) * scale);
      sx[4 + i] = bfr(bfr(t[2][i]) * scale);
    }
    float mx = fmaxf(fmaxf(fmaxf(sx[0], sx[1]), fmaxf(sx[2], sx[3])), fmaxf(fmaxf(sx[4], sx[5]), fmaxf(sx[6], sx[7])));
    mx = fmaxf(mx, fmaxf(fmaxf(sm_[0], sm_[1]), fmaxf(sm_[2], sm_[3])));
    mx = xr32_max(xr16_max(mx));
    float sum = 0.f;
#pragma unroll
    for (int i = 0; i < 8; ++i) { sx[i] = __expf(sx[i] - mx); sum += sx[i]; }
#pragma unroll
    for (int i = 0; i < 4; ++i) { sm_[i] = __expf(sm_[i] - mx); sum += sm_[i]; }
    sum = xr32_add(xr16_add(sum));
    const float inv = __builtin_amdgcn_rcpf(sum);
    u32x4 px = {pack2(sx[0] * inv, sx[1] * inv), pack2(sx[2] * inv, sx[3] * inv), pack2(sx[4] * inv, sx[5] * inv), pack2(sx[6] * inv, sx[7] * inv)};
    u32x2 pm = {pack2(sm_[0] * inv, sm_[1] * inv), pack2(sm_[2] * inv, sm_[3] * inv)};
    u32x2 vm0 = {pack2(t[3].x, t[3].y), pack2(t[3].z, t[3].w)};
    u32x2 vm1 = {pack2(t[4].x, t[4].y), pack2(t[4].z, t[4].w)};
    f32x4 o0, o1;
    {
      u32x4 pm4 = {pm.x, pm.y, 0u, 0u}, vm04 = {vm0.x, vm0.y, 0u, 0u}, vm14 = {vm1.x, vm1.y, 0u, 0u};
      MFMA_NV0(o0, vx[0], px); MFMA_V0(o1, vx[1], px);
      MFMA_NV(o0, vm04, pm4); MFMA_NV(o1, vm14, pm4);
      asm volatile("s_nop 7\n\ts_nop 1" : "+v"(o0), "+v"(o1));
    }
    u32x4 op = {pack2(o0.x, o0.y), pack2(o0.z, o0.w), pack2(o1.x, o1.y), pack2(o1.z, o1.w)};
    const int slot = s & 1;
    f32x4 d[10];
    MFMA_NV0(d[0], wo[0], op);
#pragma unroll
    for (int i = 1; i < 10; ++i) MFMA_V0(d[i], wo[i], op);
    asm volatile("s_nop 7\n\ts_nop 1" : "+v"(d[0]), "+v"(d[1]), "+v"(d[2]), "+v"(d[3]), "+v"(d[4]), "+v"(d[5]), "+v"(d[6]), "+v"(d[7]), "+v"(d[8]), "+v"(d[9]));
    if (m < 8) {
      const u32 obase = slot * INBOX_SLOT + ((20 * w + g) * NPROD + c) * 128 + m * 16;
#pragma unroll
      for (int i = 0; i < 5; ++i) {
        u32x4 v = {min(pack2(d[2 * i].x, d[2 * i].y), 0xFFFFFFFEu), min(pack2(d[2 * i].z, d[2 * i].w), 0xFFFFFFFEu),
                   min(pack2(d[2 * i + 1].x, d[2 * i + 1].y), 0xFFFFFFFEu), min(pack2(d[2 * i + 1].z, d[2 * i + 1].w), 0xFFFFFFFEu)};
        __builtin_amdgcn_raw_buffer_store_b128(v, rin, obase + i * (4 * NPROD * 128), 0, 16);
      }
    }
    STAMP(4);
    if (w == 0 && g < 2) {
      __builtin_amdgcn_raw_buffer_store_b64(vm0, rkm, (u32)(SZ_KM + (((s * 32 + h) * 128 + 32 * gs + p32(0, m)) * 8 + 4 * g) * 2), 0, 16);
      __builtin_amdgcn_raw_buffer_store_b64(vm1, rkm, (u32)(SZ_KM + (((s * 32 + h) * 128 + 32 * gs + p32(1, m)) * 8 + 4 * g) * 2), 0, 16);
    }
    if (c < (int)NRED) {
      const u32 base = slot * INBOX_SLOT + c * NPROD * 128 + w * 4096 + l * 16;
      u32x4 v[4];
      u32 spins = 0;
      for (;;) {
        bool bad = false;
#pragma unroll
        for (int i = 0; i < 4; ++i) v[i] = __builtin_amdgcn_raw_buffer_load_b128(rin, base + i * 1024, 0, 16);
#pragma unroll
        for (int i = 0; i < 4; ++i) bad |= has_sent(v[i]);
        if (!__any(bad)) break;
        if (++spins > SPIN_CAP) { if (l == 0) *err = 2; break; }
      }
      STAMP(5);
      float a8[8];
#pragma unroll
      for (int j = 0; j < 4; ++j) {
        a8[2 * j] = (bflo(v[0][j]) + bflo(v[1][j])) + (bflo(v[2][j]) + bflo(v[3][j]));
        a8[2 * j + 1] = (bfhi(v[0][j]) + bfhi(v[1][j])) + (bfhi(v[2][j]) + bfhi(v[3][j]));
      }
#pragma unroll
      for (int j = 0; j < 8; ++j) a8[j] = xr32_add(xr16_add(xr8_add(a8[j])));
      if (l < 8) {
        *(f32x4*)(smem + CH_SRED + (w * 8 + l) * 32) = (f32x4){a8[0], a8[1], a8[2], a8[3]};
        *(f32x4*)(smem + CH_SRED + (w * 8 + l) * 32 + 16) = (f32x4){a8[4], a8[5], a8[6], a8[7]};
      }
      __syncthreads();
      if (w == 0 && l < 8) {
        const f32x4* sr = (const f32x4*)(smem + CH_SRED);
        f32x4 a0 = (sr[2 * l] + sr[16 + 2 * l]) + (sr[32 + 2 * l] + sr[48 + 2 * l]);
        f32x4 a1 = (sr[2 * l + 1] + sr[16 + 2 * l + 1]) + (sr[32 + 2 * l + 1] + sr[48 + 2 * l + 1]);
        u32x4 o = {min(pack2(a0.x, a0.y), 0xFFFFFFFEu), min(pack2(a0.z, a0.w), 0xFFFFFFFEu), min(pack2(a1.x, a1.y), 0xFFFFFFFEu), min(pack2(a1.z, a1.w), 0xFFFFFFFEu)};
        __builtin_amdgcn_raw_buffer_store_b128(o, rmem, (s + 1) * MEM_STEP + c * 128 + l * 16, 0, 16);
      }
      const u32x4 sv = {SENT, SENT, SENT, SENT};
#pragma unroll
      for (int i = 0; i < 4; ++i) __builtin_amdgcn_raw_buffer_store_b128(sv, rin, base + i * 1024, 0, 16);
      STAMP(6);
    }
  }
  asm volatile("s_waitcnt vmcnt(0)" ::: "memory");
  __syncthreads();
  if (tid == 0) __hip_atomic_fetch_add(mcnt + ((NSEG - 1) * 32 + h) * 16, 1u, __ATOMIC_RELAXED, __HIP_MEMORY_SCOPE_AGENT);
}

__device__ __forceinline__ void block_top8(const float* __restrict__ r, int* sidx, float* sval, float* swv, int* swi) {
  const int tid = threadIdx.x, w = tid >> 6, l = tid & 63;
  float v[8];
#pragma unroll
  for (int j = 0; j < 8; ++j) v[j] = r[tid + 256 * j];
  for (int round = 0; round < CSEL; ++round) {
    float bv = v[0]; int bi = tid;
#pragma unroll
    for (int j = 1; j < 8; ++j) if (v[j] > bv) { bv = v[j]; bi = tid + 256 * j; }
#pragma unroll
    for (int o = 32; o > 0; o >>= 1) {
      float ov = __shfl_xor(bv, o); int oi = __shfl_xor(bi, o);
      if (ov > bv || (ov == bv && oi < bi)) { bv = ov; bi = oi; }
    }
    if (l == 0) { swv[w] = bv; swi[w] = bi; }
    __syncthreads();
    float fv = swv[0]; int fi = swi[0];
#pragma unroll
    for (int k = 1; k < 4; ++k) { float ov = swv[k]; int oi = swi[k]; if (ov > fv || (ov == fv && oi < fi)) { fv = ov; fi = oi; } }
    if (tid == 0) { sidx[round] = fi; sval[round] = fv; }
#pragma unroll
    for (int j = 0; j < 8; ++j) if (tid + 256 * j == fi) v[j] = -INFINITY;
    __syncthreads();
  }
}

__global__ __launch_bounds__(256) void mod_gateup_kernel(const u16* __restrict__ wg, const u16* __restrict__ wu, char* __restrict__ ws) {
  __shared__ float xs[8][640];
  __shared__ float red[4][2][8][128];
  __shared__ int sidx[8]; __shared__ float sval[8]; __shared__ float swv[4]; __shared__ int swi[4];
  const int tid = threadIdx.x, w = tid >> 6, l = tid & 63;
  block_top8((const float*)(ws + OFF_R), sidx, sval, swv, swi);
  const u16* xn = (const u16*)(ws + OFF_XN);
  for (int i = tid; i < 8 * 640; i += 256) { int rr = i / 640, k = i % 640; xs[rr][k] = bf2f(xn[(size_t)sidx[rr] * DM + k]); }
  if (blockIdx.x == 0 && tid < 8) {
    ((int*)(ws + OFF_SEL))[tid] = sidx[tid];
    ((float*)(ws + OFF_SEL))[8 + tid] = bfr(1.0f / (1.0f + __expf(-sval[tid])));
  }
  __syncthreads();
  const int n0 = blockIdx.x * 128 + 2 * l;
  const bool ok = n0 < FF;
  float ag[8][2], au[8][2];
#pragma unroll
  for (int rr = 0; rr < 8; ++rr) { ag[rr][0] = ag[rr][1] = au[rr][0] = au[rr][1] = 0.f; }
  if (ok) {
#pragma unroll 4
    for (int k = 160 * w; k < 160 * w + 160; ++k) {
      u32 g2 = *(const u32*)(wg + (size_t)k * FF + n0);
      u32 u2 = *(const u32*)(wu + (size_t)k * FF + n0);
      float g0 = bflo(g2), g1 = bfhi(g2), u0 = bflo(u2), u1 = bfhi(u2);
#pragma unroll
      for (int rr = 0; rr < 8; ++rr) {
        float xv = xs[rr][k];
        ag[rr][0] += xv * g0; ag[rr][1] += xv * g1; au[rr][0] += xv * u0; au[rr][1] += xv * u1;
      }
    }
  }
#pragma unroll
  for (int rr = 0; rr < 8; ++rr) {
    red[w][0][rr][2 * l] = ag[rr][0]; red[w][0][rr][2 * l + 1] = ag[rr][1];
    red[w][1][rr][2 * l] = au[rr][0]; red[w][1][rr][2 * l + 1] = au[rr][1];
  }
  __syncthreads();
  u16* hb = (u16*)(ws + OFF_HB);
  for (int i = tid; i < 8 * 128; i += 256) {
    int rr = i >> 7, cc = i & 127;
    int n = blockIdx.x * 128 + cc;
    if (n < FF) {
      float gsum = bfr((red[0][0][rr][cc] + red[1][0][rr][cc]) + (red[2][0][rr][cc] + red[3][0][rr][cc]));
      float usum = bfr((red[0][1][rr][cc] + red[1][1][rr][cc]) + (red[2][1][rr][cc] + red[3][1][rr][cc]));
      float sg = bfr(gsum / (1.0f + __expf(-gsum)));
      float hv = bfr(sg * usum);
      hb[rr * FF + n] = (u16)(__float_as_uint(hv) >> 16);
    }
  }
}

__global__ __launch_bounds__(256) void mod_down_kernel(const u16* __restrict__ wd, char* __restrict__ ws) {
  __shared__ float hs[8][216];
  __shared__ float red[4][8][128];
  const int tid = threadIdx.x, w = tid >> 6, l = tid & 63;
  const int cb = blockIdx.x % 5, kb = blockIdx.x / 5;
  const u16* hb = (const u16*)(ws + OFF_HB);
  for (int i = tid; i < 8 * 216; i += 256) { int rr = i / 216, k = i % 216; hs[rr][k] = bf2f(hb[rr * FF + kb * 216 + k]); }
  __syncthreads();
  const int n0 = cb * 128 + 2 * l;
  float a[8][2];
#pragma unroll
  for (int rr = 0; rr < 8; ++rr) a[rr][0] = a[rr][1] = 0.f;
#pragma unroll 6
  for (int k = 54 * w; k < 54 * w + 54; ++k) {
    u32 d2 = *(const u32*)(wd + (size_t)(kb * 216 + k) * DM + n0);
    float d0 = bflo(d2), d1 = bfhi(d2);
#pragma unroll
    for (int rr = 0; rr < 8; ++rr) { float hv = hs[rr][k]; a[rr][0] += hv * d0; a[rr][1] += hv * d1; }
  }
#pragma unroll
  for (int rr = 0; rr < 8; ++rr) { red[w][rr][2 * l] = a[rr][0]; red[w][rr][2 * l + 1] = a[rr][1]; }
  __syncthreads();
  float* yp = (float*)(ws + OFF_YP);
  for (int i = tid; i < 8 * 128; i += 256) {
    int rr = i >> 7, cc = i & 127;
    yp[((size_t)kb * 8 + rr) * DM + cb * 128 + cc] = (red[0][rr][cc] + red[1][rr][cc]) + (red[2][rr][cc] + red[3][rr][cc]);
  }
}

__device__ __forceinline__ void wait_count(const u32* p, u32 want, u32* err) {
  u32 spins = 0;
  for (;;) {
    u32 v = __builtin_amdgcn_readfirstlane(__hip_atomic_load(p, __ATOMIC_RELAXED, __HIP_MEMORY_SCOPE_AGENT));
    if (v >= want) break;
    __builtin_amdgcn_s_sleep(8);
    if (++spins > (1u << 20)) { *err = 3; break; }
  }
}

__device__ __forceinline__ void worker_qattn(char* __restrict__ ws, char* smem, const int mb, const int h) {
  const int tid = threadIdx.x, w = __builtin_amdgcn_readfirstlane(tid >> 6), l = tid & 63;
  const char* xnf = ws + OFF_XNF;
  const char* wf = ws + OFF_WF;
  const int wr = w >> 1, wc = w & 1;
  const f32x4 z4 = {0, 0, 0, 0};
  const int c = l & 15, g = l >> 4;
  {
    f32x4 acc[4][4];
#pragma unroll
    for (int i = 0; i < 4; ++i)
#pragma unroll
      for (int j = 0; j < 4; ++j) acc[i][j] = z4;
    auto stage = [&](int t, int buf) {
#pragma unroll
      for (int kk = 0; kk < 2; ++kk) {
#pragma unroll
        for (int i = 0; i < 4; ++i) {
          int f = 4 * w + i;
          int ks = 2 * t + kk;
          const char* src = (f < 8) ? xnf + (((size_t)(mb * 8 + f) * 20 + ks) * 1024) : wf + (((size_t)(h * 8 + (f - 8)) * 20 + ks) * 1024);
          glds16(src + l * 16, smem + buf * 32768 + (kk * 16 + f) * 1024 + l * 16);
        }
      }
    };
    stage(0, 0);
    for (int t = 0; t < 10; ++t) {
      asm volatile("s_waitcnt vmcnt(0)" ::: "memory");
      __syncthreads();
      if (t + 1 < 10) stage(t + 1, (t + 1) & 1);
      const char* base = smem + (t & 1) * 32768;
#pragma unroll
      for (int kk = 0; kk < 2; ++kk) {
        bf16x8 a[4], bb[4];
#pragma unroll
        for (int i = 0; i < 4; ++i) a[i] = *(const bf16x8*)(base + (kk * 16 + 4 * wr + i) * 1024 + l * 16);
#pragma unroll
        for (int j = 0; j < 4; ++j) bb[j] = *(const bf16x8*)(base + (kk * 16 + 8 + 4 * wc + j) * 1024 + l * 16);
#pragma unroll
        for (int i = 0; i < 4; ++i)
#pragma unroll
          for (int j = 0; j < 4; ++j) acc[i][j] = mfma32(bb[j], a[i], acc[i][j]);
      }
    }
#pragma unroll
    for (int i = 0; i < 4; ++i)
#pragma unroll
      for (int u = 0; u < 2; ++u) {
        f32x4 a0 = acc[i][2 * u], a1 = acc[i][2 * u + 1];
        u32x4 v = {pack2(a0.x, a0.y), pack2(a0.z, a0.w), pack2(a1.x, a1.y), pack2(a1.z, a1.w)};
        *(u32x4*)(smem + 65536 + (((4 * wr + i) * 4 + 2 * wc + u) * 64 + l) * 16) = v;
      }
  }
  __syncthreads();
  const int s = mb * 4 + w;
  const u16* Kx = (const u16*)(ws + OFF_K);
  const u16* VT = (const u16*)(ws + OFF_VT);
  const __amdgpu_buffer_rsrc_t rkm = __builtin_amdgcn_make_buffer_rsrc(ws + OFF_KM, 0, (u32)(2 * SZ_KM), 0x00020000);
  const __amdgpu_buffer_rsrc_t rof = __builtin_amdgcn_make_buffer_rsrc(ws + OFF_OF, 0, (u32)SZ_Q, 0x00020000);
  const float scale = 0.08838834764831845f;
  f32x4 sM[2], sX[2][2];
#pragma unroll
  for (int T = 0; T < 2; ++T) { sM[T] = z4; sX[0][T] = z4; sX[1][T] = z4; }
  bf16x8 q[4][2];
#pragma unroll
  for (int ks = 0; ks < 4; ++ks) {
    bf16x8 kx[2];
#pragma unroll
    for (int T = 0; T < 2; ++T) {
      q[ks][T] = *(const bf16x8*)(smem + 65536 + (((2 * w + T) * 4 + ks) * 64 + l) * 16);
      kx[T] = *(const bf16x8*)(Kx + (size_t)(32 * s + p32(T, c)) * HD + h * 128 + 32 * ks + 8 * g);
    }
#pragma unroll
    for (int T = 0; T < 2; ++T) {
      sX[0][T] = mfma32(kx[0], q[ks][T], sX[0][T]);
      sX[1][T] = mfma32(kx[1], q[ks][T], sX[1][T]);
    }
  }
  wait_count((const u32*)(ws + OFF_MCNT) + (s * 32 + h) * 16, 4u, (u32*)(ws + OFF_ERR));
#pragma unroll
  for (int ks = 0; ks < 4; ++ks) {
    u32x4 km = __builtin_amdgcn_raw_buffer_load_b128(rkm, (u32)(((s * 8 + (c & 7)) * HD + h * 128 + 32 * ks + 8 * g) * 2), 0, 16);
#pragma unroll
    for (int T = 0; T < 2; ++T) sM[T] = mfma32(as_bf8(km), q[ks][T], sM[T]);
  }
  u32x4 px[2]; u32x2 pm[2];
#pragma unroll
  for (int T = 0; T < 2; ++T) {
    const int qi = p32(T, c);
    float sm_[4], sx[8];
#pragma unroll
    for (int i = 0; i < 4; ++i) {
      sm_[i] = g < 2 ? bfr(bfr(sM[T][i]) * scale) : -INFINITY;
      float a = bfr(bfr(sX[0][T][i]) * scale), b = bfr(bfr(sX[1][T][i]) * scale);
      sx[i] = (8 * g + i <= qi) ? a : -1e9f;
      sx[4 + i] = (8 * g + 4 + i <= qi) ? b : -1e9f;
    }
    float mx = fmaxf(fmaxf(fmaxf(sx[0], sx[1]), fmaxf(sx[2], sx[3])), fmaxf(fmaxf(sx[4], sx[5]), fmaxf(sx[6], sx[7])));
    mx = fmaxf(mx, fmaxf(fmaxf(sm_[0], sm_[1]), fmaxf(sm_[2], sm_[3])));
    mx = xr32_max(xr16_max(mx));
    float sum = 0.f;
#pragma unroll
    for (int i = 0; i < 8; ++i) { sx[i] = __expf(sx[i] - mx); sum += sx[i]; }
#pragma unroll
    for (int i = 0; i < 4; ++i) { sm_[i] = __expf(sm_[i] - mx); sum += sm_[i]; }
    sum = xr32_add(xr16_add(sum));
    const float inv = 1.0f / sum;
    px[T] = (u32x4){pack2(sx[0] * inv, sx[1] * inv), pack2(sx[2] * inv, sx[3] * inv), pack2(sx[4] * inv, sx[5] * inv), pack2(sx[6] * inv, sx[7] * inv)};
    pm[T] = (u32x2){pack2(sm_[0] * inv, sm_[1] * inv), pack2(sm_[2] * inv, sm_[3] * inv)};
  }
#pragma unroll
  for (int u = 0; u < 4; ++u) {
    f32x4 o[2][2];
#pragma unroll
    for (int E = 0; E < 2; ++E) {
      const int e = 32 * u + p32(E, c);
      bf16x8 vx = *(const bf16x8*)(VT + (((size_t)s * 32 + h) * 128 + e) * 32 + 8 * g);
      u32x2 vm = __builtin_amdgcn_raw_buffer_load_b64(rkm, (u32)(SZ_KM + (((s * 32 + h) * 128 + e) * 8 + 4 * (g & 1)) * 2), 0, 16);
      if (g >= 2) vm = (u32x2){0, 0};
#pragma unroll
      for (int T = 0; T < 2; ++T) {
        o[E][T] = mfma32(vx, as_bf8(px[T]), z4);
        o[E][T] = mfma16(as_bf4(vm), as_bf4(pm[T]), o[E][T]);
      }
    }
#pragma unroll
    for (int T = 0; T < 2; ++T) {
      u32x4 v = {pack2(o[0][T].x, o[0][T].y), pack2(o[0][T].z, o[0][T].w), pack2(o[1][T].x, o[1][T].y), pack2(o[1][T].z, o[1][T].w)};
      __builtin_amdgcn_raw_buffer_store_b128(v, rof, (u32)((((2 * s + T) * 128 + (4 * h + u)) * 64 + l) * 16), 0, 16);
    }
  }
  asm volatile("s_waitcnt vmcnt(0)" ::: "memory");
  __syncthreads();
  if (tid == 0) __hip_atomic_fetch_add((u32*)(ws + OFF_OCNT) + mb * 64, 1u, __ATOMIC_RELAXED, __HIP_MEMORY_SCOPE_AGENT);
}

__device__ __forceinline__ void worker_wo(const u16* __restrict__ x, char* __restrict__ ws, u16* __restrict__ out, char* smem, const int mb, const int nb) {
  const int tid = threadIdx.x, w = __builtin_amdgcn_readfirstlane(tid >> 6), l = tid & 63;
  if (w == 0) {
    wait_count((const u32*)(ws + OFF_OCNT) + (mb >> 1) * 64, 32u, (u32*)(ws + OFF_ERR));
    __builtin_amdgcn_fence(__ATOMIC_ACQUIRE, "agent");
    asm volatile("s_waitcnt vmcnt(0)" ::: "memory");
  }
  __syncthreads();
  const char* of = ws + OFF_OF;
  const char* wof = ws + OFF_WOF;
  const int wr = w >> 1, wc = w & 1;
  f32x4 acc[2][4];
#pragma unroll
  for (int i = 0; i < 2; ++i)
#pragma unroll
    for (int j = 0; j < 4; ++j) acc[i][j] = (f32x4){0, 0, 0, 0};
  auto stage = [&](int t, int buf) {
#pragma unroll
    for (int kk = 0; kk < 4; ++kk) {
#pragma unroll
      for (int i = 0; i < 3; ++i) {
        int f = 3 * w + i;
        int ks = 4 * t + kk;
        const char* src = (f < 4) ? of + (((size_t)(mb * 4 + f) * 128 + ks) * 1024) : wof + (((size_t)(nb * 8 + (f - 4)) * 128 + ks) * 1024);
        glds16(src + l * 16, smem + buf * 49152 + (kk * 12 + f) * 1024 + l * 16);
      }
    }
  };
  stage(0, 0);
  for (int t = 0; t < 32; ++t) {
    asm volatile("s_waitcnt vmcnt(0)" ::: "memory");
    __syncthreads();
    if (t + 1 < 32) stage(t + 1, (t + 1) & 1);
    const char* base = smem + (t & 1) * 49152;
#pragma unroll
    for (int kk = 0; kk < 4; ++kk) {
      bf16x8 a[2], bb[4];
#pragma unroll
      for (int i = 0; i < 2; ++i) a[i] = *(const bf16x8*)(base + (kk * 12 + 2 * wr + i) * 1024 + l * 16);
#pragma unroll
      for (int j = 0; j < 4; ++j) bb[j] = *(const bf16x8*)(base + (kk * 12 + 4 + 4 * wc + j) * 1024 + l * 16);
#pragma unroll
      for (int i = 0; i < 2; ++i)
#pragma unroll
        for (int j = 0; j < 4; ++j) acc[i][j] = mfma32(bb[j], a[i], acc[i][j]);
    }
  }
  const int c = l & 15, g = l >> 4;
  const u16* xn = (const u16*)(ws + OFF_XN);
  const int* sel = (const int*)(ws + OFF_SEL);
  const float* rsel = (const float*)(ws + OFF_SEL) + 8;
  const float* yp = (const float*)(ws + OFF_YP);
#pragma unroll
  for (int i = 0; i < 2; ++i) {
    const int mt = mb * 4 + 2 * wr + i;
    const int tok = 32 * (mt >> 1) + p32(mt & 1, c);
    int slot = -1;
#pragma unroll
    for (int k = 0; k < 8; ++k) if (sel[k] == tok) slot = k;
#pragma unroll
    for (int u = 0; u < 2; ++u) {
      const int n0 = nb * 128 + 32 * (2 * wc + u) + 8 * g;
      f32x4 a0 = acc[i][2 * u], a1 = acc[i][2 * u + 1];
      float a[8] = {a0.x, a0.y, a0.z, a0.w, a1.x, a1.y, a1.z, a1.w};
      u32x4 xv = *(const u32x4*)(x + (size_t)tok * DM + n0);
      u32x4 nv = *(const u32x4*)(xn + (size_t)tok * DM + n0);
      float xf[8] = {bflo(xv.x), bfhi(xv.x), bflo(xv.y), bfhi(xv.y), bflo(xv.z), bfhi(xv.z), bflo(xv.w), bfhi(xv.w)};
      float nf[8] = {bflo(nv.x), bfhi(nv.x), bflo(nv.y), bfhi(nv.y), bflo(nv.z), bfhi(nv.z), bflo(nv.w), bfhi(nv.w)};
      if (slot >= 0) {
        const float rs = rsel[slot];
#pragma unroll
        for (int j = 0; j < 8; ++j) {
          float y = 0.f;
#pragma unroll
          for (int kb = 0; kb < 8; ++kb) y += yp[((size_t)kb * 8 + slot) * DM + n0 + j];
          y = bfr(bfr(y) * rs);
          nf[j] = bfr(nf[j] + y);
        }
      }
      float o[8];
#pragma unroll
      for (int j = 0; j < 8; ++j) o[j] = bfr(xf[j] + bfr(a[j])) + nf[j];
      u32x4 ov = {pack2(o[0], o[1]), pack2(o[2], o[3]), pack2(o[4], o[5]), pack2(o[6], o[7])};
      *(u32x4*)(out + (size_t)tok * DM + n0) = ov;
    }
  }
  __syncthreads();
}

__global__ __launch_bounds__(256, 1) void main_kernel(const u16* __restrict__ x, char* __restrict__ ws, u16* __restrict__ out) {
  extern __shared__ __attribute__((aligned(16))) char smem[];
  const int b = blockIdx.x;
  if (b < 128) {
    chain_role(ws, smem, b);
  } else {
    const int j = b - 128;
    int ia = 0;
    int iw = 0;
    for (;;) {
      const int mba = ia < 4 ? (j >> 5) + 4 * ia : 1000;
      const int u = j + 128 * iw;
      const int mbw = (iw < 2 && u < 160) ? u / 10 : 1000;
      if (mba == 1000 && mbw == 1000) break;
      if (mba <= mbw) { worker_qattn(ws, smem, mba, j & 31); ++ia; }
      else { worker_wo(x, ws, out, smem, 2 * mbw + (u % 10) / 5, u % 5); ++iw; }
    }
  }
}

extern "C" void kernel_launch(void* const* d_in, const int* in_sizes, int n_in, void* d_out, int out_size, void* d_ws, size_t ws_size,
                              hipStream_t stream) {
  const u16* x = (const u16*)d_in[0];
  const u16* mem0 = (const u16*)d_in[1];
  const u16* Wq = (const u16*)d_in[2];
  const u16* Wk = (const u16*)d_in[3];
  const u16* Wv = (const u16*)d_in[4];
  const u16* Wo = (const u16*)d_in[5];
  const u16* w_r = (const u16*)d_in[8];
  const u16* wg = (const u16*)d_in[9];
  const u16* wu = (const u16*)d_in[10];
  const u16* wd = (const u16*)d_in[11];
  char* ws = (char*)d_ws;
  u16* out = (u16*)d_out;
  (void)hipFuncSetAttribute((const void*)main_kernel, hipFuncAttributeMaxDynamicSharedMemorySize, CH_LDS);
  (void)hipFuncSetAttribute((const void*)qkv_gemm_kernel, hipFuncAttributeMaxDynamicSharedMemorySize, 65536);
  prep_kernel<<<PREP_BLOCKS, 256, 0, stream>>>(x, mem0, Wq, Wk, Wv, Wo, w_r, ws);
  qkv_gemm_kernel<<<16 * 64, 256, 65536, stream>>>(ws);
  mod_gateup_kernel<<<14, 256, 0, stream>>>(wg, wu, ws);
  mod_down_kernel<<<40, 256, 0, stream>>>(wd, ws);
  main_kernel<<<256, 256, CH_LDS, stream>>>(x, ws, out);
}
```

```cpp
#include <hip/hip_runtime.h>
#include <hip/hip_bf16.h>
#include <stdint.h>

constexpr int S_TOK = 2048, DM = 640, NH = 32, DH = 128, HD = 4096, NMEM = 8, SEG = 32, NSEG = 64;
constexpr int FF = 1728, CSEL = 8;
constexpr int KS_DM = DM / 32;
constexpr int KS_HD = HD / 32;

typedef unsigned int u32;
typedef unsigned short u16;
typedef __attribute__((ext_vector_type(4))) u32 u32x4;
typedef __attribute__((ext_vector_type(2))) u32 u32x2;
typedef __attribute__((ext_vector_type(4))) float f32x4;
typedef __attribute__((ext_vector_type(8))) short bf16x8;
typedef __attribute__((ext_vector_type(4))) short bf16x4;
typedef __attribute__((ext_vector_type(2))) __bf16 nbf16x2;

#define SENT 0xFFFFFFFFu

__device__ __forceinline__ float bf2f(u16 v) { return __uint_as_float(((u32)v) << 16); }
__device__ __forceinline__ float bflo(u32 v) { return __uint_as_float(v << 16); }
__device__ __forceinline__ float bfhi(u32 v) { return __uint_as_float(v & 0xffff0000u); }
__device__ __forceinline__ u32 pack2(float a, float b) {
  nbf16x2 v = {(__bf16)a, (__bf16)b};
  return __builtin_bit_cast(u32, v);
}
__device__ __forceinline__ float bfr(float a) { return (float)(__bf16)a; }
__device__ __forceinline__ bf16x8 as_bf8(u32x4 v) { return __builtin_bit_cast(bf16x8, v); }
__device__ __forceinline__ bf16x4 as_bf4(u32x2 v) { return __builtin_bit_cast(bf16x4, v); }
__device__ __forceinline__ f32x4 mfma32(bf16x8 a, bf16x8 b, f32x4 c) { return __builtin_amdgcn_mfma_f32_16x16x32_bf16(a, b, c, 0, 0, 0); }
__device__ __forceinline__ f32x4 mfma16(bf16x4 a, bf16x4 b, f32x4 c) {
  u32x2 a2 = __builtin_bit_cast(u32x2, a), b2 = __builtin_bit_cast(u32x2, b);
  u32x4 a4 = {a2.x, a2.y, 0u, 0u}, b4 = {b2.x, b2.y, 0u, 0u};
  return __builtin_amdgcn_mfma_f32_16x16x32_bf16(__builtin_bit_cast(bf16x8, a4), __builtin_bit_cast(bf16x8, b4), c, 0, 0, 0);
}
__device__ __forceinline__ float xr16_max(float v) { auto r = __builtin_amdgcn_permlane16_swap(__float_as_uint(v), __float_as_uint(v), false, false); return fmaxf(__uint_as_float(r[0]), __uint_as_float(r[1])); }
__device__ __forceinline__ float xr32_max(float v) { auto r = __builtin_amdgcn_permlane32_swap(__float_as_uint(v), __float_as_uint(v), false, false); return fmaxf(__uint_as_float(r[0]), __uint_as_float(r[1])); }
__device__ __forceinline__ float xr16_add(float v) { auto r = __builtin_amdgcn_permlane16_swap(__float_as_uint(v), __float_as_uint(v), false, false); return __uint_as_float(r[0]) + __uint_as_float(r[1]); }
__device__ __forceinline__ float xr32_add(float v) { auto r = __builtin_amdgcn_permlane32_swap(__float_as_uint(v), __float_as_uint(v), false, false); return __uint_as_float(r[0]) + __uint_as_float(r[1]); }
__device__ __forceinline__ float xr8_add(float v) { return v + __uint_as_float(__builtin_amdgcn_update_dpp(0u, __float_as_uint(v), 0x128  , 0xf, 0xf, false)); }
__device__ __forceinline__ int p32(int T, int r) { return 8 * (r >> 2) + 4 * T + (r & 3); }

constexpr size_t SZ_XN = (size_t)S_TOK * DM * 2;
constexpr size_t OFF_XN = 0;
constexpr size_t OFF_XNF = OFF_XN + SZ_XN;
constexpr size_t OFF_R = OFF_XNF + SZ_XN;
constexpr size_t OFF_WF = OFF_R + 8192;
constexpr size_t SZ_WF = (size_t)768 * 20 * 1024;
constexpr size_t OFF_WOF = OFF_WF + SZ_WF;
constexpr size_t SZ_WOF = (size_t)40 * 128 * 1024;
constexpr size_t OFF_Q = OFF_WOF + SZ_WOF;
constexpr size_t SZ_Q = (size_t)S_TOK * HD * 2;
constexpr size_t OFF_K = OFF_Q + SZ_Q;
constexpr size_t OFF_VT = OFF_K + SZ_Q;
constexpr size_t OFF_KM = OFF_VT + SZ_Q;
constexpr size_t SZ_KM = (size_t)NSEG * NMEM * HD * 2;
constexpr size_t OFF_VMT = OFF_KM + SZ_KM;
constexpr size_t OFF_OF = OFF_VMT + SZ_KM;
constexpr size_t OFF_INBOX = OFF_OF + SZ_Q;
constexpr u32 NPROD = 128, NRED = 80;
constexpr u32 INBOX_SLOT = NRED * NPROD * 128;
constexpr size_t OFF_MEMB = OFF_INBOX + 2 * (size_t)INBOX_SLOT;
constexpr u32 MREP = 8;
constexpr u32 MEM_REP = NRED * 128;
constexpr u32 MEM_STEP = MREP * MEM_REP;
constexpr size_t SZ_MEMB = (size_t)65 * MEM_STEP;
constexpr size_t OFF_HB = OFF_MEMB + ((SZ_MEMB + 255) / 256) * 256;
constexpr size_t OFF_YP = OFF_HB + 32768;
constexpr size_t OFF_SEL = OFF_YP + (size_t)16 * 8 * 640 * 4;
constexpr size_t OFF_ERR = OFF_SEL + 256;
constexpr size_t OFF_MCNT = OFF_ERR + 256;
constexpr size_t OFF_OSEG = OFF_MCNT + (size_t)64 * 32 * 64;
constexpr size_t OFF_KVC = OFF_OSEG + 64 * 64;
constexpr size_t OFF_HCNT = OFF_KVC + (size_t)16 * 32 * 64;
constexpr size_t OFF_YCNT = OFF_HCNT + 256;
constexpr size_t OFF_FLAG_END = OFF_YCNT + 256;
constexpr size_t WS_NEED = OFF_FLAG_END;

constexpr int PREP_LN_BLOCKS = S_TOK / 4;
constexpr int PREP_WQKV_BLOCKS = 3 * 20 * 16;
constexpr int PREP_WO_BLOCKS = 128 * 3;
constexpr int PREP_INIT_BLOCKS = 64;
constexpr int PREP_BLOCKS = PREP_LN_BLOCKS + PREP_WQKV_BLOCKS + PREP_WO_BLOCKS + PREP_INIT_BLOCKS;

__device__ __forceinline__ void tile_weights(const u16* __restrict__ W, int N, int k0, int n0, int ncols, u16* __restrict__ dst,
                                             int nks_total, int ks, int nt0, u16* lds) {
  const int tid = threadIdx.x;
  const int chunks_per_row = ncols / 8;
  for (int i = tid; i < 32 * chunks_per_row; i += 256) {
    int row = i / chunks_per_row, ch = i % chunks_per_row;
    u32x4 v = *(const u32x4*)(W + (size_t)(k0 + row) * N + n0 + ch * 8);
    *(u32x4*)(lds + row * 264 + ch * 8) = v;
  }
  __syncthreads();
  const int ntiles = ncols / 16;
  for (int o = tid; o < ntiles * 64; o += 256) {
    int t = o >> 6, l = o & 63, r = l & 15, g = l >> 4;
    int col = 32 * (t >> 1) + p32(t & 1, r);
    u16 e[8];
#pragma unroll
    for (int j = 0; j < 8; ++j) e[j] = lds[(8 * g + j) * 264 + col];
    u32x4 v = {(u32)e[0] | ((u32)e[1] << 16), (u32)e[2] | ((u32)e[3] << 16), (u32)e[4] | ((u32)e[5] << 16), (u32)e[6] | ((u32)e[7] << 16)};
    *(u32x4*)(dst + (((size_t)(nt0 + t) * nks_total + ks) * 64 + l) * 8) = v;
  }
}

__global__ __launch_bounds__(256) void prep_kernel(const u16* __restrict__ x, const u16* __restrict__ mem0, const u16* __restrict__ Wq,
                                                   const u16* __restrict__ Wk, const u16* __restrict__ Wv, const u16* __restrict__ Wo,
                                                   const u16* __restrict__ w_r, char* __restrict__ ws) {
  __shared__ __attribute__((aligned(16))) u16 lds[32 * 264];
  const int b = blockIdx.x, tid = threadIdx.x;
  if (b < PREP_LN_BLOCKS) {
    const int w = tid >> 6, l = tid & 63;
    const int t = b * 4 + w;
    const u16* xr = x + (size_t)t * DM;
    u32x4 c0 = *(const u32x4*)(xr + l * 8);
    u32x4 c1 = {0, 0, 0, 0};
    if (l < 16) c1 = *(const u32x4*)(xr + 512 + l * 8);
    float v[16];
    v[0] = bflo(c0.x); v[1] = bfhi(c0.x); v[2] = bflo(c0.y); v[3] = bfhi(c0.y); v[4] = bflo(c0.z); v[5] = bfhi(c0.z); v[6] = bflo(c0.w); v[7] = bfhi(c0.w);
    v[8] = bflo(c1.x); v[9] = bfhi(c1.x); v[10] = bflo(c1.y); v[11] = bfhi(c1.y); v[12] = bflo(c1.z); v[13] = bfhi(c1.z); v[14] = bflo(c1.w); v[15] = bfhi(c1.w);
    float s = 0.f;
#pragma unroll
    for (int j = 0; j < 16; ++j) s += v[j];
#pragma unroll
    for (int o = 32; o > 0; o >>= 1) s += __shfl_xor(s, o);
    const float mean = s * (1.0f / DM);
    float q = 0.f;
#pragma unroll
    for (int j = 0; j < 8; ++j) { float d = v[j] - mean; q += d * d; }
    if (l < 16) {
#pragma unroll
      for (int j = 8; j < 16; ++j) { float d = v[j] - mean; q += d * d; }
    }
#pragma unroll
    for (int o = 32; o > 0; o >>= 1) q += __shfl_xor(q, o);
    const float var_b = bfr(q * (1.0f / DM));
    const float mean_b = bfr(mean);
    const float den = bfr(sqrtf(bfr(var_b + 1e-05f)));
    float y[16];
#pragma unroll
    for (int j = 0; j < 16; ++j) y[j] = bfr(bfr(v[j] - mean_b) / den);
    u32x4 w0 = *(const u32x4*)(w_r + l * 8);
    u32x4 w1 = {0, 0, 0, 0};
    if (l < 16) w1 = *(const u32x4*)(w_r + 512 + l * 8);
    float rs = y[0] * bflo(w0.x) + y[1] * bfhi(w0.x) + y[2] * bflo(w0.y) + y[3] * bfhi(w0.y) + y[4] * bflo(w0.z) + y[5] * bfhi(w0.z) + y[6] * bflo(w0.w) + y[7] * bfhi(w0.w);
    if (l < 16) rs += y[8] * bflo(w1.x) + y[9] * bfhi(w1.x) + y[10] * bflo(w1.y) + y[11] * bfhi(w1.y) + y[12] * bflo(w1.z) + y[13] * bfhi(w1.z) + y[14] * bflo(w1.w) + y[15] * bfhi(w1.w);
#pragma unroll
    for (int o = 32; o > 0; o >>= 1) rs += __shfl_xor(rs, o);
    if (l == 0) ((float*)(ws + OFF_R))[t] = bfr(rs);
    u16* xn = (u16*)(ws + OFF_XN);
    u16* xnf = (u16*)(ws + OFF_XNF);
    const int sg = t >> 5, tt = t & 31;
    const int mt = 2 * sg + ((tt >> 2) & 1), r = 4 * (tt >> 3) + (tt & 3);
    u32x4 o0 = {pack2(y[0], y[1]), pack2(y[2], y[3]), pack2(y[4], y[5]), pack2(y[6], y[7])};
    *(u32x4*)(xn + (size_t)t * DM + l * 8) = o0;
    { int c = l; *(u32x4*)(xnf + (((size_t)mt * 20 + (c >> 2)) * 64 + (c & 3) * 16 + r) * 8) = o0; }
    if (l < 16) {
      u32x4 o1 = {pack2(y[8], y[9]), pack2(y[10], y[11]), pack2(y[12], y[13]), pack2(y[14], y[15])};
      *(u32x4*)(xn + (size_t)t * DM + 512 + l * 8) = o1;
      int c = 64 + l;
      *(u32x4*)(xnf + (((size_t)mt * 20 + (c >> 2)) * 64 + (c & 3) * 16 + r) * 8) = o1;
    }
  } else if (b < PREP_LN_BLOCKS + PREP_WQKV_BLOCKS) {
    int i = b - PREP_LN_BLOCKS;
    int mat = i / 320, rem = i % 320, ks = rem / 16, nb = rem % 16;
    const u16* W = mat == 0 ? Wq : (mat == 1 ? Wk : Wv);
    tile_weights(W, HD, ks * 32, nb * 256, 256, (u16*)(ws + OFF_WF), 20, ks, mat * 256 + nb * 16, lds);
  } else if (b < PREP_LN_BLOCKS + PREP_WQKV_BLOCKS + PREP_WO_BLOCKS) {
    int i = b - PREP_LN_BLOCKS - PREP_WQKV_BLOCKS;
    int ks = i / 3, nb = i % 3;
    tile_weights(Wo, DM, ks * 32, nb * 256, nb == 2 ? 128 : 256, (u16*)(ws + OFF_WOF), 128, ks, nb * 16, lds);
  } else {
    int i = b - (PREP_LN_BLOCKS + PREP_WQKV_BLOCKS + PREP_WO_BLOCKS);
    u32x4 sv = {SENT, SENT, SENT, SENT};
    u32x4* p = (u32x4*)(ws + OFF_INBOX);
    const size_t n16 = (2 * (size_t)INBOX_SLOT) / 16;
    for (size_t j = (size_t)i * 256 + tid; j < n16; j += (size_t)PREP_INIT_BLOCKS * 256) p[j] = sv;
    u32x4* pm = (u32x4*)(ws + OFF_MEMB + MEM_STEP);
    const size_t m16 = (size_t)64 * MEM_STEP / 16;
    for (size_t j = (size_t)i * 256 + tid; j < m16; j += (size_t)PREP_INIT_BLOCKS * 256) pm[j] = sv;
    if (i == 0) {
      for (int j = tid; j < 640; j += 256) {
        int p_ = j >> 3, m = j & 7;
        u32x4 v = *(const u32x4*)(mem0 + m * DM + p_ * 8);
        if (v.x == SENT) v.x = 0xFFC0FFC0u;
        if (v.y == SENT) v.y = 0xFFC0FFC0u;
        if (v.z == SENT) v.z = 0xFFC0FFC0u;
        if (v.w == SENT) v.w = 0xFFC0FFC0u;
        for (u32 rp = 0; rp < MREP; ++rp) *(u32x4*)(ws + OFF_MEMB + rp * MEM_REP + (size_t)j * 16) = v;
      }
      if (tid == 0) *(u32*)(ws + OFF_ERR) = 0;
    }
    if (i == 1) {
      u32x4* pf = (u32x4*)(ws + OFF_MCNT);
      for (int j = tid; j < (int)((OFF_FLAG_END - OFF_MCNT) / 16); j += 256) pf[j] = (u32x4){0, 0, 0, 0};
    }
  }
}

__device__ __forceinline__ void glds16(const void* g, void* l) {
  __builtin_amdgcn_global_load_lds((const __attribute__((address_space(1))) void*)g, (__attribute__((address_space(3))) void*)l, 16, 0, 0);
}

constexpr u32 SPIN_CAP = 1u << 16;
#define SLP_NR 56
#define SLP_RI 6
#define SLP_D1 10
#define SLP_D2 10
#define SLP_RM 4
__device__ __forceinline__ bool has_sent(u32x4 v) { return (v.x == SENT) | (v.y == SENT) | (v.z == SENT) | (v.w == SENT); }
__device__ __forceinline__ u32 desent(float f) { u32 b = __float_as_uint(f); return b == SENT ? 0x7FC00000u : b; }

template <int SLP = 8>
__device__ __forceinline__ void wait_count(const u32* p, u32 want, u32* err) {
  u32 spins = 0;
  for (;;) {
    u32 v = __builtin_amdgcn_readfirstlane(__hip_atomic_load(p, __ATOMIC_RELAXED, __HIP_MEMORY_SCOPE_AGENT));
    if (v >= want) break;
    __builtin_amdgcn_s_sleep(SLP);
    if (++spins > (1u << 20)) { *err = 3; break; }
  }
}

__device__ __forceinline__ u32x4 mk_rsrc(const void* p, u32 n) {
  const unsigned long long a = (unsigned long long)p;
  return (u32x4){(u32)__builtin_amdgcn_readfirstlane((u32)a), (u32)__builtin_amdgcn_readfirstlane((u32)(a >> 32) & 0xffffu), n, 0x00020000u};
}

constexpr int CH_MEMF = 0, CH_PART = 20480, CH_WV = 40960, CH_WO = 81920, CH_SRED = 122880, CH_LDS = 123904;

#define MFMA_A(acc, wa, mb) asm volatile("v_mfma_f32_16x16x32_bf16 %0, %1, %2, %0" : "+v"(acc) : "a"(wa), "v"(mb))
#define MFMA_V(acc, wa, mb) asm volatile("v_mfma_f32_16x16x32_bf16 %0, %1, %2, %0" : "+v"(acc) : "v"(wa), "v"(mb))
#define MFMA_A0(acc, wa, mb) asm volatile("v_mfma_f32_16x16x32_bf16 %0, %1, %2, 0" : "=&v"(acc) : "a"(wa), "v"(mb))
#define MFMA_V0(acc, wa, mb) asm volatile("v_mfma_f32_16x16x32_bf16 %0, %1, %2, 0" : "=&v"(acc) : "v"(wa), "v"(mb))
#define MFMA_NV(acc, wa, mb) asm volatile("s_nop 1\n\tv_mfma_f32_16x16x32_bf16 %0, %1, %2, %0" : "+v"(acc) : "v"(wa), "v"(mb))
#define MFMA_NV0(acc, wa, mb) asm volatile("s_nop 1\n\tv_mfma_f32_16x16x32_bf16 %0, %1, %2, 0" : "=&v"(acc) : "v"(wa), "v"(mb))

#define STAMP(i)
__device__ __forceinline__ void chain_role(char* __restrict__ ws, char* smem, const int c) {
  const int tid = threadIdx.x, w = __builtin_amdgcn_readfirstlane(tid >> 6), l = tid & 63;
  const int h = c >> 2, gs = c & 3;
  const int m = l & 15, g = l >> 4;
  const char* wf = ws + OFF_WF;
  const char* wof = ws + OFF_WOF;
  bf16x8 wq[2][20], wk[2][20];
#pragma unroll
  for (int T = 0; T < 2; ++T)
#pragma unroll
    for (int i = 0; i < 20; ++i) {
      const int ks = (5 * w + i) % 20;
      wq[T][i] = *(const bf16x8*)(wf + (((size_t)(2 * (4 * h + w) + T) * 20 + ks) * 64 + l) * 16);
      wk[T][i] = *(const bf16x8*)(wf + (((size_t)(256 + 2 * (4 * h + w) + T) * 20 + ks) * 64 + l) * 16);
    }
  for (int i = tid; i < 40 * 64; i += 256) {
    int f = i >> 6, ll = i & 63;
    *(u32x4*)(smem + CH_WV + i * 16) = *(const u32x4*)(wf + (((size_t)(512 + 2 * (4 * h + gs) + f / 20) * 20 + f % 20) * 64 + ll) * 16);
    *(u32x4*)(smem + CH_WO + i * 16) = *(const u32x4*)(wof + (((size_t)f * 128 + (4 * h + gs)) * 64 + ll) * 16);
  }
  for (int i = tid; i < 1280; i += 256) *(u32x4*)(smem + CH_MEMF + i * 16) = (u32x4){0, 0, 0, 0};
  __syncthreads();

  const __amdgpu_buffer_rsrc_t rin = __builtin_amdgcn_make_buffer_rsrc(ws + OFF_INBOX, 0, 2 * INBOX_SLOT, 0x00020000);
  const __amdgpu_buffer_rsrc_t rmem = __builtin_amdgcn_make_buffer_rsrc(ws + OFF_MEMB, 0, (u32)SZ_MEMB, 0x00020000);
  const u32x4 din = mk_rsrc(ws + OFF_INBOX, 2 * INBOX_SLOT), dmem = mk_rsrc(ws + OFF_MEMB, (u32)SZ_MEMB), dkv = mk_rsrc(ws + OFF_K, (u32)(2 * SZ_Q));
  const u32x4 dflag = mk_rsrc(ws + OFF_KVC, 16 * 32 * 64);
  const __amdgpu_buffer_rsrc_t rkm = __builtin_amdgcn_make_buffer_rsrc(ws + OFF_KM, 0, (u32)(2 * SZ_KM), 0x00020000);
  u32* mcnt = (u32*)(ws + OFF_MCNT);
  u32* err = (u32*)(ws + OFF_ERR);
  const float scale = 0.08838834764831845f;
  const char* lmem = smem + CH_MEMF + l * 16;

  u32x4 qt[8];
#pragma unroll
  for (int i = 0; i < 8; ++i) qt[i] = (u32x4){0, 0, 0, 0};
  u32x4 kv4[4];
  const u32 kvo0 = (u32)((p32(0, m) * HD + h * 128 + 32 * w + 8 * g) * 2), kvo1 = (u32)((p32(1, m) * HD + h * 128 + 32 * w + 8 * g) * 2);
  const u32 kvo2 = (u32)(SZ_Q + ((h * 128 + 32 * gs + p32(0, m)) * 32 + 8 * g) * 2), kvo3 = (u32)(SZ_Q + ((h * 128 + 32 * gs + p32(1, m)) * 32 + 8 * g) * 2);
  auto load_kv = [&](int sn) {
    const u32 so = (u32)sn * (32 * HD * 2);
    asm volatile("buffer_load_dwordx4 v[192:195], %[o0], %[rs], %[so] offen sc1\n\t"
                 "buffer_load_dwordx4 v[196:199], %[o1], %[rs], %[so] offen sc1\n\t"
                 "buffer_load_dwordx4 v[200:203], %[o2], %[rs], %[so] offen sc1\n\t"
                 "buffer_load_dwordx4 v[204:207], %[o3], %[rs], %[so] offen sc1"
                 : "=&{v[192:195]}"(kv4[0]), "=&{v[196:199]}"(kv4[1]), "=&{v[200:203]}"(kv4[2]), "=&{v[204:207]}"(kv4[3])
                 : [o0] "v"(kvo0), [o1] "v"(kvo1), [o2] "v"(kvo2), [o3] "v"(kvo3), [rs] "s"(dkv), [so] "s"(so)
                 : "memory");
  };
  wait_count((const u32*)(ws + OFF_KVC) + h * 16, 1u, err);
  load_kv(0);
  for (int s = 0; s < NSEG; ++s) {
    bf16x8 wv[2][5];
#pragma unroll
    for (int T = 0; T < 2; ++T)
#pragma unroll
      for (int i = 0; i < 5; ++i) wv[T][i] = *(const bf16x8*)(smem + CH_WV + (T * 20 + 5 * w + i) * 1024 + l * 16);
    {
      const u32 base = s * MEM_STEP + (c & (MREP - 1)) * MEM_REP + w * 2560;
      const u32 o0 = base + l * 16, o1 = base + 1024 + l * 16, o2 = base + 2048 + (l & 31) * 16;
      const int i0 = w * 160 + l, i1 = i0 + 64, i2 = w * 160 + 128 + (l & 31);
      const u32 lbase = (u32)(uintptr_t)(__attribute__((address_space(3))) char*)smem + CH_MEMF;
      const u32 la0 = lbase + ((i0 >> 3) * 16 + (i0 & 7)) * 16, la1 = lbase + ((i1 >> 3) * 16 + (i1 & 7)) * 16, la2 = lbase + ((i2 >> 3) * 16 + (i2 & 7)) * 16;
      u32 tmx, cnt;
      const u32 fo = (u32)((min(s + 1, NSEG - 1) >> 2) * 32 + h) * 64;
      asm volatile(
          "buffer_load_dwordx4 v[232:235], %[o0], %[rs], 0 offen sc1\n\t"
          "buffer_load_dwordx4 v[236:239], %[o1], %[rs], 0 offen sc1\n\t"
          "buffer_load_dwordx4 v[240:243], %[o2], %[rs], 0 offen sc1\n\t"
          "buffer_load_dword v230, off, %[rf], %[fo] sc1\n\t"
          "s_mov_b32 %[cnt], 0\n\t"
          "s_sleep %[slp]\n"
          "1:\n\t"
          "buffer_load_dwordx4 v[244:247], %[o0], %[rs], 0 offen sc1\n\t"
          "buffer_load_dwordx4 v[248:251], %[o1], %[rs], 0 offen sc1\n\t"
          "buffer_load_dwordx4 v[252:255], %[o2], %[rs], 0 offen sc1\n\t"
          "buffer_load_dword v231, off, %[rf], %[fo] sc1\n\t"
          "s_waitcnt vmcnt(4)\n\t"
          "v_add_u32 %[t], -1, v230\n\t"
          "v_max3_u32 %[t], %[t], v232, v233\n\t"
          "v_max_u32 %[t], %[t], v234\n\t"
          "v_max3_u32 %[t], %[t], v235, v236\n\t"
          "v_max3_u32 %[t], %[t], v237, v238\n\t"
          "v_max3_u32 %[t], %[t], v239, v240\n\t"
          "v_max3_u32 %[t], %[t], v241, v242\n\t"
          "v_max_u32 %[t], %[t], v243\n\t"
          "v_cmp_eq_u32 vcc, -1, %[t]\n\t"
          "s_cbranch_vccz 2f\n\t"
          "buffer_load_dwordx4 v[232:235], %[o0], %[rs], 0 offen sc1\n\t"
          "buffer_load_dwordx4 v[236:239], %[o1], %[rs], 0 offen sc1\n\t"
          "buffer_load_dwordx4 v[240:243], %[o2], %[rs], 0 offen sc1\n\t"
          "buffer_load_dword v230, off, %[rf], %[fo] sc1\n\t"
          "s_waitcnt vmcnt(4)\n\t"
          "v_add_u32 %[t], -1, v231\n\t"
          "v_max3_u32 %[t], %[t], v244, v245\n\t"
          "v_max_u32 %[t], %[t], v246\n\t"
          "v_max3_u32 %[t], %[t], v247, v248\n\t"
          "v_max3_u32 %[t], %[t], v249, v250\n\t"
          "v_max3_u32 %[t], %[t], v251, v252\n\t"
          "v_max3_u32 %[t], %[t], v253, v254\n\t"
          "v_max_u32 %[t], %[t], v255\n\t"
          "v_cmp_eq_u32 vcc, -1, %[t]\n\t"
          "s_cbranch_vccz 3f\n\t"
          "s_add_u32 %[cnt], %[cnt], 1\n\t"
          "s_cmp_lt_u32 %[cnt], 0x10000\n\t"
          "s_cbranch_scc1 1b\n"
          "3:\n\t"
          "ds_write_b128 %[la0], v[244:247]\n\t"
          "ds_write_b128 %[la1], v[248:251]\n\t"
          "ds_write_b128 %[la2], v[252:255]\n\t"
          "s_branch 4f\n"
          "2:\n\t"
          "ds_write_b128 %[la0], v[232:235]\n\t"
          "ds_write_b128 %[la1], v[236:239]\n\t"
          "ds_write_b128 %[la2], v[240:243]\n"
          "4:\n\t"
          "s_waitcnt lgkmcnt(0)"
          : "+{v[224:227]}"(qt[0]), "+{v[228:231]}"(qt[1]), "+{v[232:235]}"(qt[2]), "+{v[236:239]}"(qt[3]),
            "+{v[240:243]}"(qt[4]), "+{v[244:247]}"(qt[5]), "+{v[248:251]}"(qt[6]), "+{v[252:255]}"(qt[7]), [t] "=&v"(tmx), [cnt] "=&s"(cnt),
            "+{v[192:195]}"(kv4[0]), "+{v[196:199]}"(kv4[1]), "+{v[200:203]}"(kv4[2]), "+{v[204:207]}"(kv4[3])
          : [o0] "v"(o0), [o1] "v"(o1), [o2] "v"(o2), [rs] "s"(dmem), [la0] "v"(la0), [la1] "v"(la1), [la2] "v"(la2), [slp] "i"(SLP_D2), [rf] "s"(dflag), [fo] "s"(fo)
          : "vcc", "scc", "memory");
      if (cnt >= 0x10000u && l == 0) *err = 1;
    }
    STAMP(0);
    __syncthreads();
    STAMP(1);
    if (s > 0 && tid == 0) __hip_atomic_fetch_add(mcnt + ((s - 1) * 32 + h) * 16, 1u, __ATOMIC_RELAXED, __HIP_MEMORY_SCOPE_AGENT);
    if (s > 0 && c < (int)NRED) {
      const u32x4 sv = {SENT, SENT, SENT, SENT};
#pragma unroll
      for (int i = 0; i < 4; ++i) __builtin_amdgcn_raw_buffer_store_b128(sv, rin, ((s - 1) & 1) * INBOX_SLOT + c * NPROD * 128 + w * 4096 + l * 16 + i * 1024, 0, 16);
    }
    const f32x4 z4 = {0, 0, 0, 0};
    f32x4 aq0, aq1, ak0, ak1, av0, av1;
    {
      bf16x8 mfA[5], mfB[5];
      const char* lc0 = lmem + (5 * w) * 1024;
      const char* lc1 = lmem + (5 * ((w + 1) & 3)) * 1024;
      const char* lc2 = lmem + (5 * ((w + 2) & 3)) * 1024;
      const char* lc3 = lmem + (5 * ((w + 3) & 3)) * 1024;
#pragma unroll
      for (int i = 0; i < 5; ++i) mfA[i] = *(const bf16x8*)(lc0 + i * 1024);
#pragma unroll
      for (int i = 0; i < 5; ++i) mfB[i] = *(const bf16x8*)(lc1 + i * 1024);
      MFMA_V0(av0, mfA[0], wv[0][0]); MFMA_V0(av1, mfA[0], wv[1][0]);
#pragma unroll
      for (int i = 1; i < 5; ++i) { MFMA_V(av0, mfA[i], wv[0][i]); MFMA_V(av1, mfA[i], wv[1][i]); }
      MFMA_A0(aq0, wq[0][0], mfA[0]); MFMA_A0(aq1, wq[1][0], mfA[0]); MFMA_A0(ak0, wk[0][0], mfA[0]); MFMA_A0(ak1, wk[1][0], mfA[0]);
#pragma unroll
      for (int i = 1; i < 5; ++i) { MFMA_A(aq0, wq[0][i], mfA[i]); MFMA_A(aq1, wq[1][i], mfA[i]); MFMA_A(ak0, wk[0][i], mfA[i]); MFMA_A(ak1, wk[1][i], mfA[i]); }
#pragma unroll
      for (int i = 0; i < 5; ++i) mfA[i] = *(const bf16x8*)(lc2 + i * 1024);
#pragma unroll
      for (int i = 0; i < 5; ++i) { MFMA_A(aq0, wq[0][5 + i], mfB[i]); MFMA_A(aq1, wq[1][5 + i], mfB[i]); MFMA_A(ak0, wk[0][5 + i], mfB[i]); MFMA_A(ak1, wk[1][5 + i], mfB[i]); }
#pragma unroll
      for (int i = 0; i < 5; ++i) mfB[i] = *(const bf16x8*)(lc3 + i * 1024);
#pragma unroll
      for (int i = 0; i < 5; ++i) { MFMA_A(aq0, wq[0][10 + i], mfA[i]); MFMA_A(aq1, wq[1][10 + i], mfA[i]); MFMA_A(ak0, wk[0][10 + i], mfA[i]); MFMA_A(ak1, wk[1][10 + i], mfA[i]); }
      MFMA_A(aq0, wq[0][15], mfB[0]); MFMA_A(aq1, wq[1][15], mfB[0]); MFMA_A(ak0, wk[0][15], mfB[0]); MFMA_A(ak1, wk[1][15], mfB[0]);
#pragma unroll
      for (int i = 1; i < 5; ++i) { MFMA_V(aq0, wq[0][15 + i], mfB[i]); MFMA_V(aq1, wq[1][15 + i], mfB[i]); MFMA_V(ak0, wk[0][15 + i], mfB[i]); MFMA_V(ak1, wk[1][15 + i], mfB[i]); }
      asm volatile("s_nop 7\n\ts_nop 7" : "+v"(aq0), "+v"(aq1), "+v"(ak0), "+v"(ak1), "+v"(av0), "+v"(av1));
    }
    STAMP(2);
    bf16x8 wo[10];
#pragma unroll
    for (int i = 0; i < 10; ++i) wo[i] = *(const bf16x8*)(smem + CH_WO + (10 * w + i) * 1024 + l * 16);
    u32x4 qp = {pack2(aq0.x, aq0.y), pack2(aq0.z, aq0.w), pack2(aq1.x, aq1.y), pack2(aq1.z, aq1.w)};
    u32x4 kp = {pack2(ak0.x, ak0.y), pack2(ak0.z, ak0.w), pack2(ak1.x, ak1.y), pack2(ak1.z, ak1.w)};
    f32x4 sM, sX0, sX1;
    MFMA_NV0(sM, kp, qp); MFMA_V0(sX0, kv4[0], qp); MFMA_V0(sX1, kv4[1], qp);
    asm volatile("s_nop 7\n\ts_nop 1" : "+v"(sM), "+v"(sX0), "+v"(sX1));
    {
      char* pp = smem + CH_PART + w * 5120 + l * 16;
      *(f32x4*)(pp + 3072) = av0; *(f32x4*)(pp + 4096) = av1;
      *(f32x4*)(pp) = sM; *(f32x4*)(pp + 1024) = sX0; *(f32x4*)(pp + 2048) = sX1;
    }
    if (w == gs && m < 8) __builtin_amdgcn_raw_buffer_store_b128(kp, rkm, (u32)(((s * 8 + m) * HD + h * 128 + 32 * w + 8 * g) * 2), 0, 16);
    __syncthreads();
    STAMP(3);
    f32x4 t[5];
#pragma unroll
    for (int i = 0; i < 5; ++i) {
      const char* pp = smem + CH_PART + i * 1024 + l * 16;
      f32x4 a0 = *(const f32x4*)(pp), a1 = *(const f32x4*)(pp + 5120), a2 = *(const f32x4*)(pp + 10240), a3 = *(const f32x4*)(pp + 15360);
      t[i] = (a0 + a1) + (a2 + a3);
    }
    float sm_[4], sx[8];
#pragma unroll
    for (int i = 0; i < 4; ++i) {
      sm_[i] = g < 2 ? bfr(bfr(t[0][i]) * scale) : -INFINITY;
      sx[i] = bfr(bfr(t[1][i]) * scale);
      sx[4 + i] = bfr(bfr(t[2][i]) * scale);
    }
    float mx = fmaxf(fmaxf(fmaxf(sx[0], sx[1]), fmaxf(sx[2], sx[3])), fmaxf(fmaxf(sx[4], sx[5]), fmaxf(sx[6], sx[7])));
    mx = fmaxf(mx, fmaxf(fmaxf(sm_[0], sm_[1]), fmaxf(sm_[2], sm_[3])));
    mx = xr32_max(xr16_max(mx));
    float sum = 0.f;
#pragma unroll
    for (int i = 0; i < 8; ++i) { sx[i] = __expf(sx[i] - mx); sum += sx[i]; }
#pragma unroll
    for (int i = 0; i < 4; ++i) { sm_[i] = __expf(sm_[i] - mx); sum += sm_[i]; }
    sum = xr32_add(xr16_add(sum));
    const float inv = __builtin_amdgcn_rcpf(sum);
    u32x4 px = {pack2(sx[0] * inv, sx[1] * inv), pack2(sx[2] * inv, sx[3] * inv), pack2(sx[4] * inv, sx[5] * inv), pack2(sx[6] * inv, sx[7] * inv)};
    u32x2 pm = {pack2(sm_[0] * inv, sm_[1] * inv), pack2(sm_[2] * inv, sm_[3] * inv)};
    u32x2 vm0 = {pack2(t[3].x, t[3].y), pack2(t[3].z, t[3].w)};
    u32x2 vm1 = {pack2(t[4].x, t[4].y), pack2(t[4].z, t[4].w)};
    f32x4 o0, o1;
    {
      u32x4 pm4 = {pm.x, pm.y, 0u, 0u}, vm04 = {vm0.x, vm0.y, 0u, 0u}, vm14 = {vm1.x, vm1.y, 0u, 0u};
      MFMA_NV0(o0, kv4[2], px); MFMA_V0(o1, kv4[3], px);
      MFMA_NV(o0, vm04, pm4); MFMA_NV(o1, vm14, pm4);
      asm volatile("s_nop 7\n\ts_nop 1" : "+v"(o0), "+v"(o1));
    }
    u32x4 op = {pack2(o0.x, o0.y), pack2(o0.z, o0.w), pack2(o1.x, o1.y), pack2(o1.z, o1.w)};
    const int slot = s & 1;
    f32x4 d[10];
    MFMA_NV0(d[0], wo[0], op);
#pragma unroll
    for (int i = 1; i < 10; ++i) MFMA_V0(d[i], wo[i], op);
    asm volatile("s_nop 7\n\ts_nop 1" : "+v"(d[0]), "+v"(d[1]), "+v"(d[2]), "+v"(d[3]), "+v"(d[4]), "+v"(d[5]), "+v"(d[6]), "+v"(d[7]), "+v"(d[8]), "+v"(d[9]));
    if (m < 8 && s != NSEG - 1) {
      const u32 obase = slot * INBOX_SLOT + ((20 * w + g) * NPROD + c) * 128 + m * 16;
#pragma unroll
      for (int i = 0; i < 5; ++i) {
        u32x4 v = {min(pack2(d[2 * i].x, d[2 * i].y), 0xFFFFFFFEu), min(pack2(d[2 * i].z, d[2 * i].w), 0xFFFFFFFEu),
                   min(pack2(d[2 * i + 1].x, d[2 * i + 1].y), 0xFFFFFFFEu), min(pack2(d[2 * i + 1].z, d[2 * i + 1].w), 0xFFFFFFFEu)};
        __builtin_amdgcn_raw_buffer_store_b128(v, rin, obase + i * (4 * NPROD * 128), 0, 16);
      }
    }
    STAMP(4);
    if (w == 0 && g < 2) {
      __builtin_amdgcn_raw_buffer_store_b64(vm0, rkm, (u32)(SZ_KM + (((s * 32 + h) * 128 + 32 * gs + p32(0, m)) * 8 + 4 * g) * 2), 0, 16);
      __builtin_amdgcn_raw_buffer_store_b64(vm1, rkm, (u32)(SZ_KM + (((s * 32 + h) * 128 + 32 * gs + p32(1, m)) * 8 + 4 * g) * 2), 0, 16);
    }
    const int doit = (c - (int)NRED) & (s - (NSEG - 1));
    {
      const u32 base = slot * INBOX_SLOT + (c < (int)NRED ? c : 0) * NPROD * 128 + w * 4096 + l * 16;
      u32x4 v[4];
      u32 tmx, cnt;
      asm volatile(
          "s_mov_b32 %[cnt], 0\n\t"
          "s_cmp_ge_i32 %[doit], 0\n\t"
          "s_cbranch_scc1 4f\n\t"
          "s_sleep %[slp0]\n\t"
          "buffer_load_dwordx4 v[224:227], %[o], %[rs], 0 offen offset:0 sc1\n\t"
          "buffer_load_dwordx4 v[228:231], %[o], %[rs], 0 offen offset:1024 sc1\n\t"
          "buffer_load_dwordx4 v[232:235], %[o], %[rs], 0 offen offset:2048 sc1\n\t"
          "buffer_load_dwordx4 v[236:239], %[o], %[rs], 0 offen offset:3072 sc1\n\t"
          "s_sleep %[slp]\n"
          "1:\n\t"
          "buffer_load_dwordx4 v[240:243], %[o], %[rs], 0 offen offset:0 sc1\n\t"
          "buffer_load_dwordx4 v[244:247], %[o], %[rs], 0 offen offset:1024 sc1\n\t"
          "buffer_load_dwordx4 v[248:251], %[o], %[rs], 0 offen offset:2048 sc1\n\t"
          "buffer_load_dwordx4 v[252:255], %[o], %[rs], 0 offen offset:3072 sc1\n\t"
          "s_waitcnt vmcnt(4)\n\t"
          "v_max3_u32 %[t], v224, v225, v226\n\t"
          "v_max3_u32 %[t], %[t], v227, v228\n\t"
          "v_max3_u32 %[t], %[t], v229, v230\n\t"
          "v_max3_u32 %[t], %[t], v231, v232\n\t"
          "v_max3_u32 %[t], %[t], v233, v234\n\t"
          "v_max3_u32 %[t], %[t], v235, v236\n\t"
          "v_max3_u32 %[t], %[t], v237, v238\n\t"
          "v_max_u32 %[t], %[t], v239\n\t"
          "v_cmp_eq_u32 vcc, -1, %[t]\n\t"
          "s_cbranch_vccz 2f\n\t"
          "buffer_load_dwordx4 v[224:227], %[o], %[rs], 0 offen offset:0 sc1\n\t"
          "buffer_load_dwordx4 v[228:231], %[o], %[rs], 0 offen offset:1024 sc1\n\t"
          "buffer_load_dwordx4 v[232:235], %[o], %[rs], 0 offen offset:2048 sc1\n\t"
          "buffer_load_dwordx4 v[236:239], %[o], %[rs], 0 offen offset:3072 sc1\n\t"
          "s_waitcnt vmcnt(4)\n\t"
          "v_max3_u32 %[t], v240, v241, v242\n\t"
          "v_max3_u32 %[t], %[t], v243, v244\n\t"
          "v_max3_u32 %[t], %[t], v245, v246\n\t"
          "v_max3_u32 %[t], %[t], v247, v248\n\t"
          "v_max3_u32 %[t], %[t], v249, v250\n\t"
          "v_max3_u32 %[t], %[t], v251, v252\n\t"
          "v_max3_u32 %[t], %[t], v253, v254\n\t"
          "v_max_u32 %[t], %[t], v255\n\t"
          "v_cmp_eq_u32 vcc, -1, %[t]\n\t"
          "s_cbranch_vccz 3f\n\t"
          "s_add_u32 %[cnt], %[cnt], 1\n\t"
          "s_cmp_lt_u32 %[cnt], 0x10000\n\t"
          "s_cbranch_scc1 1b\n"
          "3:\n\t"
          "v_mov_b64 v[208:209], v[240:241]\n\t"
          "v_mov_b64 v[210:211], v[242:243]\n\t"
          "v_mov_b64 v[212:213], v[244:245]\n\t"
          "v_mov_b64 v[214:215], v[246:247]\n\t"
          "v_mov_b64 v[216:217], v[248:249]\n\t"
          "v_mov_b64 v[218:219], v[250:251]\n\t"
          "v_mov_b64 v[220:221], v[252:253]\n\t"
          "v_mov_b64 v[222:223], v[254:255]\n\t"
          "s_branch 4f\n"
          "2:\n\t"
          "v_mov_b64 v[208:209], v[224:225]\n\t"
          "v_mov_b64 v[210:211], v[226:227]\n\t"
          "v_mov_b64 v[212:213], v[228:229]\n\t"
          "v_mov_b64 v[214:215], v[230:231]\n\t"
          "v_mov_b64 v[216:217], v[232:233]\n\t"
          "v_mov_b64 v[218:219], v[234:235]\n\t"
          "v_mov_b64 v[220:221], v[236:237]\n\t"
          "v_mov_b64 v[222:223], v[238:239]\n\t"
          "4:"
          : "=&{v[208:211]}"(v[0]), "=&{v[212:215]}"(v[1]), "=&{v[216:219]}"(v[2]), "=&{v[220:223]}"(v[3]),
            "+{v[224:227]}"(qt[0]), "+{v[228:231]}"(qt[1]), "+{v[232:235]}"(qt[2]), "+{v[236:239]}"(qt[3]),
            "+{v[240:243]}"(qt[4]), "+{v[244:247]}"(qt[5]), "+{v[248:251]}"(qt[6]), "+{v[252:255]}"(qt[7]), [t] "=&v"(tmx), [cnt] "=&s"(cnt)
          : [o] "v"(base), [rs] "s"(din), [slp] "i"(SLP_D1), [slp0] "i"(SLP_RI), [doit] "s"(doit)
          : "vcc", "scc", "memory");
      load_kv(s != NSEG - 1 ? s + 1 : s);
      if (c >= (int)NRED && s != NSEG - 1) __builtin_amdgcn_s_sleep(SLP_NR);
      if (doit < 0) {
      if (cnt >= 0x10000u && l == 0) *err = 2;
      STAMP(5);
      float a8[8];
#pragma unroll
      for (int j = 0; j < 4; ++j) {
        a8[2 * j] = (bflo(v[0][j]) + bflo(v[1][j])) + (bflo(v[2][j]) + bflo(v[3][j]));
        a8[2 * j + 1] = (bfhi(v[0][j]) + bfhi(v[1][j])) + (bfhi(v[2][j]) + bfhi(v[3][j]));
      }
#pragma unroll
      for (int j = 0; j < 8; ++j) a8[j] = xr32_add(xr16_add(xr8_add(a8[j])));
      if (l < 8) {
        *(f32x4*)(smem + CH_SRED + (w * 8 + l) * 32) = (f32x4){a8[0], a8[1], a8[2], a8[3]};
        *(f32x4*)(smem + CH_SRED + (w * 8 + l) * 32 + 16) = (f32x4){a8[4], a8[5], a8[6], a8[7]};
      }
      __syncthreads();
      if (w == 0) {
        const int l8 = l & 7;
        const f32x4* sr = (const f32x4*)(smem + CH_SRED);
        f32x4 a0 = (sr[2 * l8] + sr[16 + 2 * l8]) + (sr[32 + 2 * l8] + sr[48 + 2 * l8]);
        f32x4 a1 = (sr[2 * l8 + 1] + sr[16 + 2 * l8 + 1]) + (sr[32 + 2 * l8 + 1] + sr[48 + 2 * l8 + 1]);
        u32x4 o = {min(pack2(a0.x, a0.y), 0xFFFFFFFEu), min(pack2(a0.z, a0.w), 0xFFFFFFFEu), min(pack2(a1.x, a1.y), 0xFFFFFFFEu), min(pack2(a1.z, a1.w), 0xFFFFFFFEu)};
        __builtin_amdgcn_raw_buffer_store_b128(o, rmem, (s + 1) * MEM_STEP + (l >> 3) * MEM_REP + c * 128 + l8 * 16, 0, 16);
      }
      STAMP(6);
      __builtin_amdgcn_s_sleep(SLP_RM);
      }
    }
  }
  asm volatile("s_waitcnt vmcnt(0)" ::"{v[224:227]}"(qt[0]), "{v[228:231]}"(qt[1]), "{v[232:235]}"(qt[2]), "{v[236:239]}"(qt[3]),
               "{v[240:243]}"(qt[4]), "{v[244:247]}"(qt[5]), "{v[248:251]}"(qt[6]), "{v[252:255]}"(qt[7]) : "memory");
  __syncthreads();
  if (tid == 0) __hip_atomic_fetch_add(mcnt + ((NSEG - 1) * 32 + h) * 16, 1u, __ATOMIC_RELAXED, __HIP_MEMORY_SCOPE_AGENT);
}

__device__ __forceinline__ int otid() { int t = threadIdx.x; asm volatile("" : "+v"(t)); return t; }
#define VMCNT(n) asm volatile("s_waitcnt vmcnt(%0)" ::"i"(n) : "memory")
#define RAWBAR() do { asm volatile("s_waitcnt lgkmcnt(0)" ::: "memory"); __builtin_amdgcn_s_barrier(); asm volatile("" ::: "memory"); } while (0)

__device__ __forceinline__ void block_top8(const float* __restrict__ r, int* sidx, float* sval, float* swv, int* swi) {
  const int tid = otid(), w = tid >> 6, l = tid & 63;
  float v[8];
#pragma unroll
  for (int j = 0; j < 8; ++j) v[j] = r[tid + 256 * j];
  for (int round = 0; round < CSEL; ++round) {
    float bv = v[0]; int bi = tid;
#pragma unroll
    for (int j = 1; j < 8; ++j) if (v[j] > bv) { bv = v[j]; bi = tid + 256 * j; }
#pragma unroll
    for (int o = 32; o > 0; o >>= 1) {
      float ov = __shfl_xor(bv, o); int oi = __shfl_xor(bi, o);
      if (ov > bv || (ov == bv && oi < bi)) { bv = ov; bi = oi; }
    }
    if (l == 0) { swv[w] = bv; swi[w] = bi; }
    __syncthreads();
    float fv = swv[0]; int fi = swi[0];
#pragma unroll
    for (int k = 1; k < 4; ++k) { float ov = swv[k]; int oi = swi[k]; if (ov > fv || (ov == fv && oi < fi)) { fv = ov; fi = oi; } }
    if (tid == 0) { sidx[round] = fi; sval[round] = fv; }
#pragma unroll
    for (int j = 0; j < 8; ++j) if (tid + 256 * j == fi) v[j] = -INFINITY;
    __syncthreads();
  }
}

__device__ __forceinline__ void worker_modA(const u16* __restrict__ wg, const u16* __restrict__ wu, char* __restrict__ ws, char* smem, const int blk) {
  float* xs = (float*)smem;
  float* red = (float*)(smem + 20480);
  int* sidx = (int*)(smem + 24576);
  float* sval = (float*)(smem + 24576 + 64);
  float* swv = (float*)(smem + 24576 + 128);
  int* swi = (int*)(smem + 24576 + 192);
  const int tid = otid(), w = tid >> 6, l = tid & 63;
  block_top8((const float*)(ws + OFF_R), sidx, sval, swv, swi);
  const u16* xn = (const u16*)(ws + OFF_XN);
  for (int i = tid; i < 8 * 640; i += 256) { int rr = i / 640, k = i % 640; xs[i] = bf2f(xn[(size_t)sidx[rr] * DM + k]); }
  const __amdgpu_buffer_rsrc_t rh = __builtin_amdgcn_make_buffer_rsrc(ws + OFF_HB, 0, (u32)(OFF_ERR - OFF_HB), 0x00020000);
  if (blk == 0 && tid < 8) {
    __builtin_amdgcn_raw_buffer_store_b32((u32)sidx[tid], rh, (u32)(OFF_SEL - OFF_HB) + tid * 4, 0, 16);
    __builtin_amdgcn_raw_buffer_store_b32(__float_as_uint(bfr(1.0f / (1.0f + __expf(-sval[tid])))), rh, (u32)(OFF_SEL - OFF_HB) + 32 + tid * 4, 0, 16);
  }
  __syncthreads();
  const int cp = tid & 7, kg = tid >> 3;
  const int n0 = blk * 16 + 2 * cp;
  float ag[8][2], au[8][2];
#pragma unroll
  for (int rr = 0; rr < 8; ++rr) { ag[rr][0] = ag[rr][1] = au[rr][0] = au[rr][1] = 0.f; }
  u32 g2[20], u2[20];
#pragma unroll
  for (int i = 0; i < 20; ++i) {
    g2[i] = *(const u32*)(wg + (size_t)(20 * kg + i) * FF + n0);
    u2[i] = *(const u32*)(wu + (size_t)(20 * kg + i) * FF + n0);
  }
#pragma unroll
  for (int i = 0; i < 20; ++i) {
    const float g0 = bflo(g2[i]), g1 = bfhi(g2[i]), u0 = bflo(u2[i]), u1 = bfhi(u2[i]);
#pragma unroll
    for (int rr = 0; rr < 8; ++rr) {
      const float xv = xs[rr * 640 + 20 * kg + i];
      ag[rr][0] += xv * g0; ag[rr][1] += xv * g1; au[rr][0] += xv * u0; au[rr][1] += xv * u1;
    }
  }
#pragma unroll
  for (int rr = 0; rr < 8; ++rr) {
#pragma unroll
    for (int e = 0; e < 2; ++e) {
      ag[rr][e] = xr32_add(xr16_add(xr8_add(ag[rr][e])));
      au[rr][e] = xr32_add(xr16_add(xr8_add(au[rr][e])));
    }
  }
  if (l < 8) {
#pragma unroll
    for (int rr = 0; rr < 8; ++rr) *(f32x4*)(red + (w * 8 + l) * 32 + rr * 4) = (f32x4){ag[rr][0], ag[rr][1], au[rr][0], au[rr][1]};
  }
  __syncthreads();
  if (tid < 64) {
    const int rr = tid >> 3, c2 = tid & 7;
    f32x4 a0 = *(const f32x4*)(red + (0 * 8 + c2) * 32 + rr * 4), a1 = *(const f32x4*)(red + (1 * 8 + c2) * 32 + rr * 4);
    f32x4 a2 = *(const f32x4*)(red + (2 * 8 + c2) * 32 + rr * 4), a3 = *(const f32x4*)(red + (3 * 8 + c2) * 32 + rr * 4);
    f32x4 t = (a0 + a1) + (a2 + a3);
    float hv[2];
#pragma unroll
    for (int e = 0; e < 2; ++e) {
      const float gsum = bfr(t[e]), usum = bfr(t[2 + e]);
      const float sg = bfr(gsum / (1.0f + __expf(-gsum)));
      hv[e] = sg * usum;
    }
    __builtin_amdgcn_raw_buffer_store_b32(pack2(hv[0], hv[1]), rh, (u32)((rr * FF + blk * 16 + 2 * c2) * 2), 0, 16);
  }
  asm volatile("s_waitcnt vmcnt(0)" ::: "memory");
  __syncthreads();
  if (tid == 0) __hip_atomic_fetch_add((u32*)(ws + OFF_HCNT), 1u, __ATOMIC_RELAXED, __HIP_MEMORY_SCOPE_AGENT);
}

__device__ __forceinline__ void worker_modB(const u16* __restrict__ wd, char* __restrict__ ws, char* smem, const int blk) {
  float* hs = (float*)smem;
  float* red = (float*)(smem + 4096);
  const int tid = otid(), w = tid >> 6, l = tid & 63;
  const int cb = blk % 5, kb = blk / 5;
  const int n0 = cb * 128 + 2 * l;
  u32 d2[27];
#pragma unroll
  for (int i = 0; i < 27; ++i) d2[i] = *(const u32*)(wd + (size_t)(kb * 108 + 27 * w + i) * DM + n0);
  wait_count((const u32*)(ws + OFF_HCNT), 108u, (u32*)(ws + OFF_ERR));
  const __amdgpu_buffer_rsrc_t rh = __builtin_amdgcn_make_buffer_rsrc(ws + OFF_HB, 0, (u32)(OFF_ERR - OFF_HB), 0x00020000);
  for (int i = tid; i < 8 * 54; i += 256) {
    const int rr = i / 54, k2 = i % 54;
    const u32 v = __builtin_amdgcn_raw_buffer_load_b32(rh, (u32)((rr * FF + kb * 108 + 2 * k2) * 2), 0, 16);
    hs[rr * 108 + 2 * k2] = bflo(v); hs[rr * 108 + 2 * k2 + 1] = bfhi(v);
  }
  __syncthreads();
  float a[8][2];
#pragma unroll
  for (int rr = 0; rr < 8; ++rr) a[rr][0] = a[rr][1] = 0.f;
#pragma unroll
  for (int i = 0; i < 27; ++i) {
    const float d0 = bflo(d2[i]), d1 = bfhi(d2[i]);
#pragma unroll
    for (int rr = 0; rr < 8; ++rr) { const float hv = hs[rr * 108 + 27 * w + i]; a[rr][0] += hv * d0; a[rr][1] += hv * d1; }
  }
#pragma unroll
  for (int rr = 0; rr < 8; ++rr) { red[(w * 8 + rr) * 128 + 2 * l] = a[rr][0]; red[(w * 8 + rr) * 128 + 2 * l + 1] = a[rr][1]; }
  __syncthreads();
  for (int i = tid; i < 8 * 128; i += 256) {
    const int rr = i >> 7, cc = i & 127;
    const float v = (red[rr * 128 + cc] + red[(8 + rr) * 128 + cc]) + (red[(16 + rr) * 128 + cc] + red[(24 + rr) * 128 + cc]);
    __builtin_amdgcn_raw_buffer_store_b32(__float_as_uint(v), rh, (u32)(OFF_YP - OFF_HB) + (u32)(((kb * 8 + rr) * DM + cb * 128 + cc) * 4), 0, 16);
  }
  asm volatile("s_waitcnt vmcnt(0)" ::: "memory");
  __syncthreads();
  if (tid == 0) __hip_atomic_fetch_add((u32*)(ws + OFF_YCNT), 1u, __ATOMIC_RELAXED, __HIP_MEMORY_SCOPE_AGENT);
}

__device__ __forceinline__ void worker_kv(char* __restrict__ ws, char* smem, const int mb, const int h) {
  const int tid = otid(), w = __builtin_amdgcn_readfirstlane(tid >> 6), l = tid & 63;
  const char* xnf = ws + OFF_XNF;
  const char* wf = ws + OFF_WF;
  const int wr = w >> 1, wc = w & 1;
  const int c = l & 15, g = l >> 4;
  f32x4 acc[4][8];
#pragma unroll
  for (int i = 0; i < 4; ++i)
#pragma unroll
    for (int j = 0; j < 8; ++j) acc[i][j] = (f32x4){0, 0, 0, 0};
  const char* src[6];
#pragma unroll
  for (int i = 0; i < 6; ++i) {
    const int f = 6 * w + i;
    src[i] = ((f < 8) ? xnf + (size_t)(mb * 8 + f) * 20480 : (f < 16 ? wf + (size_t)(256 + h * 8 + (f - 8)) * 20480 : wf + (size_t)(512 + h * 8 + (f - 16)) * 20480)) + l * 16;
  }
  auto stage = [&](int ks) {
#pragma unroll
    for (int i = 0; i < 6; ++i) glds16(src[i] + ks * 1024, smem + (ks % 5) * 24576 + (6 * w + i) * 1024 + l * 16);
  };
#pragma unroll
  for (int ks = 0; ks < 4; ++ks) stage(ks);
#pragma unroll
  for (int ks = 0; ks < 20; ++ks) {
    VMCNT(6 * (ks < 17 ? 3 : 19 - ks));
    RAWBAR();
    if (ks + 4 < 20) stage(ks + 4);
    const char* base = smem + (ks % 5) * 24576 + l * 16;
    bf16x8 a[4], bb[8];
#pragma unroll
    for (int i = 0; i < 4; ++i) a[i] = *(const bf16x8*)(base + (4 * wr + i) * 1024);
#pragma unroll
    for (int j = 0; j < 8; ++j) bb[j] = *(const bf16x8*)(base + (8 + 8 * wc + j) * 1024);
    if (wc == 0) {
#pragma unroll
      for (int i = 0; i < 4; ++i)
#pragma unroll
        for (int j = 0; j < 8; ++j) acc[i][j] = mfma32(bb[j], a[i], acc[i][j]);
    } else {
#pragma unroll
      for (int i = 0; i < 4; ++i)
#pragma unroll
        for (int j = 0; j < 8; ++j) acc[i][j] = mfma32(a[i], bb[j], acc[i][j]);
    }
  }
  const __amdgpu_buffer_rsrc_t rkv = __builtin_amdgcn_make_buffer_rsrc(ws + OFF_K, 0, (u32)(2 * SZ_Q), 0x00020000);
  if (wc == 0) {
#pragma unroll
    for (int i = 0; i < 4; ++i) {
      const int mt = mb * 8 + 4 * wr + i;
      const int tok = 32 * (mt >> 1) + p32(mt & 1, c);
#pragma unroll
      for (int u = 0; u < 4; ++u) {
        f32x4 a0 = acc[i][2 * u], a1 = acc[i][2 * u + 1];
        u32x4 v = {pack2(a0.x, a0.y), pack2(a0.z, a0.w), pack2(a1.x, a1.y), pack2(a1.z, a1.w)};
        __builtin_amdgcn_raw_buffer_store_b128(v, rkv, (u32)((tok * HD + h * 128 + 32 * u + 8 * g) * 2), 0, 16);
      }
    }
  } else {
#pragma unroll
    for (int ip = 0; ip < 2; ++ip) {
      const int sg = mb * 4 + 2 * wr + ip;
#pragma unroll
      for (int j = 0; j < 8; ++j) {
        const int e = 32 * (j >> 1) + p32(j & 1, c);
        f32x4 a0 = acc[2 * ip][j], a1 = acc[2 * ip + 1][j];
        u32x4 v = {pack2(a0.x, a0.y), pack2(a0.z, a0.w), pack2(a1.x, a1.y), pack2(a1.z, a1.w)};
        __builtin_amdgcn_raw_buffer_store_b128(v, rkv, (u32)(SZ_Q + (((sg * 32 + h) * 128 + e) * 32 + 8 * g) * 2), 0, 16);
      }
    }
  }
  asm volatile("s_waitcnt vmcnt(0)" ::: "memory");
  __syncthreads();
  if (tid == 0) __hip_atomic_fetch_add((u32*)(ws + OFF_KVC) + (mb * 32 + h) * 16, 1u, __ATOMIC_RELAXED, __HIP_MEMORY_SCOPE_AGENT);
}

__device__ __forceinline__ void worker_qattn(char* __restrict__ ws, char* smem, const int mb, const int h) {
  const int tid = otid(), w = __builtin_amdgcn_readfirstlane(tid >> 6), l = tid & 63;
  const char* xnf = ws + OFF_XNF;
  const char* wf = ws + OFF_WF;
  const int wr = w >> 1, wc = w & 1;
  const f32x4 z4 = {0, 0, 0, 0};
  const int c = l & 15, g = l >> 4;
  {
    f32x4 acc[4][4];
#pragma unroll
    for (int i = 0; i < 4; ++i)
#pragma unroll
      for (int j = 0; j < 4; ++j) acc[i][j] = z4;
    const char* src[4];
#pragma unroll
    for (int i = 0; i < 4; ++i) {
      const int f = 4 * w + i;
      src[i] = ((f < 8) ? xnf + (size_t)(mb * 8 + f) * 20480 : wf + (size_t)(h * 8 + (f - 8)) * 20480) + l * 16;
    }
    auto stage = [&](int ks) {
#pragma unroll
      for (int i = 0; i < 4; ++i) glds16(src[i] + ks * 1024, smem + (ks % 5) * 16384 + (4 * w + i) * 1024 + l * 16);
    };
#pragma unroll
    for (int ks = 0; ks < 4; ++ks) stage(ks);
#pragma unroll
    for (int ks = 0; ks < 20; ++ks) {
      VMCNT(4 * (ks < 17 ? 3 : 19 - ks));
      RAWBAR();
      if (ks + 4 < 20) stage(ks + 4);
      const char* base = smem + (ks % 5) * 16384 + l * 16;
      bf16x8 a[4], bb[4];
#pragma unroll
      for (int i = 0; i < 4; ++i) a[i] = *(const bf16x8*)(base + (4 * wr + i) * 1024);
#pragma unroll
      for (int j = 0; j < 4; ++j) bb[j] = *(const bf16x8*)(base + (8 + 4 * wc + j) * 1024);
#pragma unroll
      for (int i = 0; i < 4; ++i)
#pragma unroll
        for (int j = 0; j < 4; ++j) acc[i][j] = mfma32(bb[j], a[i], acc[i][j]);
    }
#pragma unroll
    for (int i = 0; i < 4; ++i)
#pragma unroll
      for (int u = 0; u < 2; ++u) {
        f32x4 a0 = acc[i][2 * u], a1 = acc[i][2 * u + 1];
        u32x4 v = {pack2(a0.x, a0.y), pack2(a0.z, a0.w), pack2(a1.x, a1.y), pack2(a1.z, a1.w)};
        *(u32x4*)(smem + 81920 + (((4 * wr + i) * 4 + 2 * wc + u) * 64 + l) * 16) = v;
      }
  }
  __syncthreads();
  const int s = mb * 4 + w;
  wait_count((const u32*)(ws + OFF_KVC) + (mb * 32 + h) * 16, 1u, (u32*)(ws + OFF_ERR));
  const __amdgpu_buffer_rsrc_t rkv = __builtin_amdgcn_make_buffer_rsrc(ws + OFF_K, 0, (u32)(2 * SZ_Q), 0x00020000);
  const __amdgpu_buffer_rsrc_t rkm = __builtin_amdgcn_make_buffer_rsrc(ws + OFF_KM, 0, (u32)(2 * SZ_KM), 0x00020000);
  const __amdgpu_buffer_rsrc_t rof = __builtin_amdgcn_make_buffer_rsrc(ws + OFF_OF, 0, (u32)SZ_Q, 0x00020000);
  const float scale = 0.08838834764831845f;
  f32x4 sM[2], sX[2][2];
#pragma unroll
  for (int T = 0; T < 2; ++T) { sM[T] = z4; sX[0][T] = z4; sX[1][T] = z4; }
  bf16x8 q[4][2];
#pragma unroll
  for (int ks = 0; ks < 4; ++ks) {
    bf16x8 kx[2];
#pragma unroll
    for (int T = 0; T < 2; ++T) {
      q[ks][T] = *(const bf16x8*)(smem + 81920 + (((2 * w + T) * 4 + ks) * 64 + l) * 16);
      kx[T] = as_bf8(__builtin_amdgcn_raw_buffer_load_b128(rkv, (u32)(((32 * s + p32(T, c)) * HD + h * 128 + 32 * ks + 8 * g) * 2), 0, 16));
    }
#pragma unroll
    for (int T = 0; T < 2; ++T) {
      sX[0][T] = mfma32(kx[0], q[ks][T], sX[0][T]);
      sX[1][T] = mfma32(kx[1], q[ks][T], sX[1][T]);
    }
  }
  bf16x8 vxa[4][2];
#pragma unroll
  for (int u = 0; u < 4; ++u)
#pragma unroll
    for (int E = 0; E < 2; ++E)
      vxa[u][E] = as_bf8(__builtin_amdgcn_raw_buffer_load_b128(rkv, (u32)(SZ_Q + (((s * 32 + h) * 128 + 32 * u + p32(E, c)) * 32 + 8 * g) * 2), 0, 16));
  wait_count((const u32*)(ws + OFF_MCNT) + (s * 32 + h) * 16, 4u, (u32*)(ws + OFF_ERR));
  u32x2 vma[4][2];
  {
    u32x4 km[4];
#pragma unroll
    for (int ks = 0; ks < 4; ++ks) km[ks] = __builtin_amdgcn_raw_buffer_load_b128(rkm, (u32)(((s * 8 + (c & 7)) * HD + h * 128 + 32 * ks + 8 * g) * 2), 0, 16);
#pragma unroll
    for (int u = 0; u < 4; ++u)
#pragma unroll
      for (int E = 0; E < 2; ++E)
        vma[u][E] = __builtin_amdgcn_raw_buffer_load_b64(rkm, (u32)(SZ_KM + (((s * 32 + h) * 128 + 32 * u + p32(E, c)) * 8 + 4 * (g & 1)) * 2), 0, 16);
#pragma unroll
    for (int ks = 0; ks < 4; ++ks)
#pragma unroll
      for (int T = 0; T < 2; ++T) sM[T] = mfma32(as_bf8(km[ks]), q[ks][T], sM[T]);
  }
  u32x4 px[2]; u32x2 pm[2];
#pragma unroll
  for (int T = 0; T < 2; ++T) {
    const int qi = p32(T, c);
    float sm_[4], sx[8];
#pragma unroll
    for (int i = 0; i < 4; ++i) {
      sm_[i] = g < 2 ? bfr(bfr(sM[T][i]) * scale) : -INFINITY;
      float a = bfr(bfr(sX[0][T][i]) * scale), b = bfr(bfr(sX[1][T][i]) * scale);
      sx[i] = (8 * g + i <= qi) ? a : -1e9f;
      sx[4 + i] = (8 * g + 4 + i <= qi) ? b : -1e9f;
    }
    float mx = fmaxf(fmaxf(fmaxf(sx[0], sx[1]), fmaxf(sx[2], sx[3])), fmaxf(fmaxf(sx[4], sx[5]), fmaxf(sx[6], sx[7])));
    mx = fmaxf(mx, fmaxf(fmaxf(sm_[0], sm_[1]), fmaxf(sm_[2], sm_[3])));
    mx = xr32_max(xr16_max(mx));
    float sum = 0.f;
#pragma unroll
    for (int i = 0; i < 8; ++i) { sx[i] = __expf(sx[i] - mx); sum += sx[i]; }
#pragma unroll
    for (int i = 0; i < 4; ++i) { sm_[i] = __expf(sm_[i] - mx); sum += sm_[i]; }
    sum = xr32_add(xr16_add(sum));
    const float inv = 1.0f / sum;
    px[T] = (u32x4){pack2(sx[0] * inv, sx[1] * inv), pack2(sx[2] * inv, sx[3] * inv), pack2(sx[4] * inv, sx[5] * inv), pack2(sx[6] * inv, sx[7] * inv)};
    pm[T] = (u32x2){pack2(sm_[0] * inv, sm_[1] * inv), pack2(sm_[2] * inv, sm_[3] * inv)};
  }
#pragma unroll
  for (int u = 0; u < 4; ++u) {
    f32x4 o[2][2];
#pragma unroll
    for (int E = 0; E < 2; ++E) {
      u32x2 vm = vma[u][E];
      if (g >= 2) vm = (u32x2){0, 0};
#pragma unroll
      for (int T = 0; T < 2; ++T) {
        o[E][T] = mfma32(vxa[u][E], as_bf8(px[T]), z4);
        o[E][T] = mfma16(as_bf4(vm), as_bf4(pm[T]), o[E][T]);
      }
    }
#pragma unroll
    for (int T = 0; T < 2; ++T) {
      u32x4 v = {pack2(o[0][T].x, o[0][T].y), pack2(o[0][T].z, o[0][T].w), pack2(o[1][T].x, o[1][T].y), pack2(o[1][T].z, o[1][T].w)};
      __builtin_amdgcn_raw_buffer_store_b128(v, rof, (u32)((((2 * s + T) * 128 + (4 * h + u)) * 64 + l) * 16), 0, 16);
    }
  }
  asm volatile("s_waitcnt vmcnt(0)" ::: "memory");
  if (l == 0) __hip_atomic_fetch_add((u32*)(ws + OFF_OSEG) + s * 16, 1u, __ATOMIC_RELAXED, __HIP_MEMORY_SCOPE_AGENT);
  __syncthreads();
}

__device__ __forceinline__ void final_store2(char* __restrict__ ws, u16* __restrict__ out, const int tok, const int n0, const f32x4 a0, const f32x4 a1,
                                             const u32x4 xv, const u32x4 nv, const int slot) {
  const float* rsel = (const float*)(ws + OFF_SEL) + 8;
  const float* yp = (const float*)(ws + OFF_YP);
  float a[8] = {a0.x, a0.y, a0.z, a0.w, a1.x, a1.y, a1.z, a1.w};
  float xf[8] = {bflo(xv.x), bfhi(xv.x), bflo(xv.y), bfhi(xv.y), bflo(xv.z), bfhi(xv.z), bflo(xv.w), bfhi(xv.w)};
  float nf[8] = {bflo(nv.x), bfhi(nv.x), bflo(nv.y), bfhi(nv.y), bflo(nv.z), bfhi(nv.z), bflo(nv.w), bfhi(nv.w)};
  if (slot >= 0) {
    const float rs = rsel[slot];
#pragma unroll
    for (int j = 0; j < 8; ++j) {
      float y = 0.f;
#pragma unroll
      for (int kb = 0; kb < 16; ++kb) y += yp[((size_t)kb * 8 + slot) * DM + n0 + j];
      y = bfr(bfr(y) * rs);
      nf[j] = bfr(nf[j] + y);
    }
  }
  float o[8];
#pragma unroll
  for (int j = 0; j < 8; ++j) o[j] = bfr(xf[j] + bfr(a[j])) + nf[j];
  u32x4 ov = {pack2(o[0], o[1]), pack2(o[2], o[3]), pack2(o[4], o[5]), pack2(o[6], o[7])};
  *(u32x4*)(out + (size_t)tok * DM + n0) = ov;
}

__device__ __forceinline__ void worker_wo_fine(const u16* __restrict__ x, char* __restrict__ ws, u16* __restrict__ out, char* smem, const int par, const int np,
                                               const int sg0, const int sgstep) {
  const int tid = otid(), w = __builtin_amdgcn_readfirstlane(tid >> 6), l = tid & 63;
  const int c = l & 15, g = l >> 4;
  const char* wof = ws + OFF_WOF + ((size_t)(2 * np) * 128 + 32 * w) * 1024 + l * 16;
  bf16x8 b0[32], b1[32];
  const int rot = (np * 8) / 5;
#pragma unroll
  for (int i = 0; i < 32; ++i) { const int kk = (i + rot) & 31; b0[i] = *(const bf16x8*)(wof + kk * 1024); b1[i] = *(const bf16x8*)(wof + 131072 + kk * 1024); }
  const __amdgpu_buffer_rsrc_t rof = __builtin_amdgcn_make_buffer_rsrc(ws + OFF_OF, 0, (u32)SZ_Q, 0x00020000);
  const u16* xn = (const u16*)(ws + OFF_XN);
  int sel[8];
  if (w == 0) {
    wait_count((const u32*)(ws + OFF_YCNT), 80u, (u32*)(ws + OFF_ERR));
    __builtin_amdgcn_fence(__ATOMIC_ACQUIRE, "agent");
#pragma unroll
    for (int k = 0; k < 8; ++k) sel[k] = __builtin_amdgcn_readfirstlane(((const int*)(ws + OFF_SEL))[k]);
  }
  int it = 0;
  for (int sg = sg0; sg < 64; sg += sgstep, ++it) {
    const int mt = 2 * sg + par;
    const int tok = 32 * sg + p32(par, c), n0 = 32 * np + 8 * g;
    u32x4 xv, nv;
    if (w == 0) { xv = *(const u32x4*)(x + (size_t)tok * DM + n0); nv = *(const u32x4*)(xn + (size_t)tok * DM + n0); }
    if (w == 1) {
      if (sg < 63) wait_count<100>((const u32*)(ws + OFF_OSEG) + sg * 16, 32u, (u32*)(ws + OFF_ERR));
      else wait_count<4>((const u32*)(ws + OFF_OSEG) + sg * 16, 32u, (u32*)(ws + OFF_ERR));
      __builtin_amdgcn_fence(__ATOMIC_ACQUIRE, "agent");
      asm volatile("s_waitcnt vmcnt(0)" ::: "memory");
    }
    __syncthreads();
    f32x4 acc0, acc1;
    const u32 abase = (u32)(((mt * 128 + 32 * w) * 64 + l) * 16);
    u32x4 a[32];
#pragma unroll
    for (int i = 0; i < 32; ++i) {
      a[i] = __builtin_amdgcn_raw_buffer_load_b128(rof, abase, ((i + rot) & 31) * 1024, 0);
    }
    __builtin_amdgcn_sched_barrier(0);
    MFMA_A0(acc0, b0[0], a[0]); MFMA_A0(acc1, b1[0], a[0]);
#pragma unroll
    for (int i = 1; i < 32; ++i) { MFMA_A(acc0, b0[i], a[i]); MFMA_A(acc1, b1[i], a[i]); }
    asm volatile("s_nop 7\n\ts_nop 7" : "+v"(acc0), "+v"(acc1));
    char* sb = smem + (it & 1) * 8192;
    if (w > 0) {
      *(f32x4*)(sb + (w * 2) * 1024 + l * 16) = acc0;
      *(f32x4*)(sb + (w * 2 + 1) * 1024 + l * 16) = acc1;
    }
    __syncthreads();
    if (w == 0) {
      const f32x4* p = (const f32x4*)(sb + l * 16);
      acc0 = (acc0 + p[2 * 64]) + (p[4 * 64] + p[6 * 64]);
      acc1 = (acc1 + p[3 * 64]) + (p[5 * 64] + p[7 * 64]);
      int slot = -1;
#pragma unroll
      for (int k = 0; k < 8; ++k) if (sel[k] == tok) slot = k;
      final_store2(ws, out, tok, n0, acc0, acc1, xv, nv, slot);
    }
  }
}

#define WSTAMP(i)
template <typename T> __device__ __forceinline__ T* opq(T* p) { asm volatile("" : "+s"(p)); return p; }

__global__ __launch_bounds__(256, 1) void main_kernel(const u16* __restrict__ x, const u16* __restrict__ wg, const u16* __restrict__ wu,
                                                      const u16* __restrict__ wd, char* __restrict__ ws, u16* __restrict__ out) {
  extern __shared__ __attribute__((aligned(16))) char smem[];
  const int b = blockIdx.x;
  if (b < 128) {
    chain_role(ws, smem, b);
  } else {
    const int j = b - 128;
    for (int i = 0; i < 4; ++i) {
      worker_kv(opq(ws), smem, 4 * i + (j >> 5), j & 31);
      WSTAMP(i);
      if (i == 0 && j < 108) worker_modA(opq(wg), opq(wu), opq(ws), smem, j);
      if (i == 1 && j < 80) worker_modB(opq(wd), opq(ws), smem, j);
    }
    if (j < 40) {
      worker_wo_fine(opq(x), opq(ws), out, smem, j / 20, j % 20, 0, 1);
    } else {
      for (int a = j - 40; a < 512; a += 88) { worker_qattn(opq(ws), smem, a >> 5, a & 31); WSTAMP(4 + a / 88); }
    }
    WSTAMP(15);
  }
}

extern "C" void kernel_launch(void* const* d_in, const int* in_sizes, int n_in, void* d_out, int out_size, void* d_ws, size_t ws_size,
                              hipStream_t stream) {
  const u16* x = (const u16*)d_in[0];
  const u16* mem0 = (const u16*)d_in[1];
  const u16* Wq = (const u16*)d_in[2];
  const u16* Wk = (const u16*)d_in[3];
  const u16* Wv = (const u16*)d_in[4];
  const u16* Wo = (const u16*)d_in[5];
  const u16* w_r = (const u16*)d_in[8];
  const u16* wg = (const u16*)d_in[9];
  const u16* wu = (const u16*)d_in[10];
  const u16* wd = (const u16*)d_in[11];
  char* ws = (char*)d_ws;
  u16* out = (u16*)d_out;
  (void)hipFuncSetAttribute((const void*)main_kernel, hipFuncAttributeMaxDynamicSharedMemorySize, CH_LDS);
  prep_kernel<<<PREP_BLOCKS, 256, 0, stream>>>(x, mem0, Wq, Wk, Wv, Wo, w_r, ws);
  main_kernel<<<256, 256, CH_LDS, stream>>>(x, wg, wu, wd, ws, out);
}
```

```cpp
#include <hip/hip_runtime.h>
#include <hip/hip_bf16.h>
#include <stdint.h>

constexpr int S_TOK = 2048, DM = 640, NH = 32, DH = 128, HD = 4096, NMEM = 8, SEG = 32, NSEG = 64;
constexpr int FF = 1728, CSEL = 8;
constexpr int KS_DM = DM / 32;
constexpr int KS_HD = HD / 32;

typedef unsigned int u32;
typedef unsigned short u16;
typedef __attribute__((ext_vector_type(4))) u32 u32x4;
typedef __attribute__((ext_vector_type(2))) u32 u32x2;
typedef __attribute__((ext_vector_type(4))) float f32x4;
typedef __attribute__((ext_vector_type(8))) short bf16x8;
typedef __attribute__((ext_vector_type(4))) short bf16x4;
typedef __attribute__((ext_vector_type(2))) __bf16 nbf16x2;

#define SENT 0xFFFFFFFFu

__device__ __forceinline__ float bf2f(u16 v) { return __uint_as_float(((u32)v) << 16); }
__device__ __forceinline__ float bflo(u32 v) { return __uint_as_float(v << 16); }
__device__ __forceinline__ float bfhi(u32 v) { return __uint_as_float(v & 0xffff0000u); }
__device__ __forceinline__ u32 pack2(float a, float b) {
  nbf16x2 v = {(__bf16)a, (__bf16)b};
  return __builtin_bit_cast(u32, v);
}
__device__ __forceinline__ float bfr(float a) { return (float)(__bf16)a; }
__device__ __forceinline__ bf16x8 as_bf8(u32x4 v) { return __builtin_bit_cast(bf16x8, v); }
__device__ __forceinline__ bf16x4 as_bf4(u32x2 v) { return __builtin_bit_cast(bf16x4, v); }
__device__ __forceinline__ f32x4 mfma32(bf16x8 a, bf16x8 b, f32x4 c) { return __builtin_amdgcn_mfma_f32_16x16x32_bf16(a, b, c, 0, 0, 0); }
__device__ __forceinline__ f32x4 mfma16(bf16x4 a, bf16x4 b, f32x4 c) {
  u32x2 a2 = __builtin_bit_cast(u32x2, a), b2 = __builtin_bit_cast(u32x2, b);
  u32x4 a4 = {a2.x, a2.y, 0u, 0u}, b4 = {b2.x, b2.y, 0u, 0u};
  return __builtin_amdgcn_mfma_f32_16x16x32_bf16(__builtin_bit_cast(bf16x8, a4), __builtin_bit_cast(bf16x8, b4), c, 0, 0, 0);
}
__device__ __forceinline__ float xr16_max(float v) { auto r = __builtin_amdgcn_permlane16_swap(__float_as_uint(v), __float_as_uint(v), false, false); return fmaxf(__uint_as_float(r[0]), __uint_as_float(r[1])); }
__device__ __forceinline__ float xr32_max(float v) { auto r = __builtin_amdgcn_permlane32_swap(__float_as_uint(v), __float_as_uint(v), false, false); return fmaxf(__uint_as_float(r[0]), __uint_as_float(r[1])); }
__device__ __forceinline__ float xr16_add(float v) { auto r = __builtin_amdgcn_permlane16_swap(__float_as_uint(v), __float_as_uint(v), false, false); return __uint_as_float(r[0]) + __uint_as_float(r[1]); }
__device__ __forceinline__ float xr32_add(float v) { auto r = __builtin_amdgcn_permlane32_swap(__float_as_uint(v), __float_as_uint(v), false, false); return __uint_as_float(r[0]) + __uint_as_float(r[1]); }
__device__ __forceinline__ float xr8_max(float v) { return fmaxf(v, __uint_as_float(__builtin_amdgcn_update_dpp(0u, __float_as_uint(v), 0x128  , 0xf, 0xf, false))); }
__device__ __forceinline__ float xr8_add(float v) { return v + __uint_as_float(__builtin_amdgcn_update_dpp(0u, __float_as_uint(v), 0x128  , 0xf, 0xf, false)); }
__device__ __forceinline__ int p32(int T, int r) { return 8 * (r >> 2) + 4 * T + (r & 3); }

constexpr size_t SZ_XN = (size_t)S_TOK * DM * 2;
constexpr size_t OFF_XN = 0;
constexpr size_t OFF_XNF = OFF_XN + SZ_XN;
constexpr size_t OFF_R = OFF_XNF + SZ_XN;
constexpr size_t OFF_WF = OFF_R + 8192;
constexpr size_t SZ_WF = (size_t)768 * 20 * 1024;
constexpr size_t OFF_WOF = OFF_WF + SZ_WF;
constexpr size_t SZ_WOF = (size_t)40 * 128 * 1024;
constexpr size_t OFF_Q = OFF_WOF + SZ_WOF;
constexpr size_t SZ_Q = (size_t)S_TOK * HD * 2;
constexpr size_t OFF_K = OFF_Q + SZ_Q;
constexpr size_t OFF_VT = OFF_K + SZ_Q;
constexpr size_t OFF_KM = OFF_VT + SZ_Q;
constexpr size_t SZ_KM = (size_t)NSEG * NMEM * HD * 2;
constexpr size_t OFF_VMT = OFF_KM + SZ_KM;
constexpr size_t OFF_OF = OFF_VMT + SZ_KM;
constexpr size_t OFF_INBOX = OFF_OF + SZ_Q;
constexpr u32 NPROD = 128, NRED = 80;
constexpr u32 INBOX_SLOT = NRED * NPROD * 128;
constexpr size_t OFF_MEMB = OFF_INBOX + 2 * (size_t)INBOX_SLOT;
constexpr u32 MREP = 8;
constexpr u32 MEM_REP = NRED * 128;
constexpr u32 MEM_STEP = MREP * MEM_REP;
constexpr size_t SZ_MEMB = (size_t)65 * MEM_STEP;
constexpr size_t OFF_HB = OFF_MEMB + ((SZ_MEMB + 255) / 256) * 256;
constexpr size_t OFF_YP = OFF_HB + 32768;
constexpr size_t OFF_SEL = OFF_YP + (size_t)16 * 8 * 640 * 4;
constexpr size_t OFF_ERR = OFF_SEL + 256;
constexpr size_t OFF_MCNT = OFF_ERR + 256;
constexpr size_t OFF_OSEG = OFF_MCNT + (size_t)64 * 32 * 64;
constexpr size_t OFF_KVC = OFF_OSEG + 64 * 64;
constexpr size_t OFF_HCNT = OFF_KVC + (size_t)16 * 32 * 64;
constexpr size_t OFF_YCNT = OFF_HCNT + 256;
constexpr size_t OFF_FLAG_END = OFF_YCNT + 256;
constexpr size_t WS_NEED = OFF_FLAG_END;

constexpr int PREP_LN_BLOCKS = S_TOK / 4;
constexpr int PREP_WQKV_BLOCKS = 3 * 20 * 16;
constexpr int PREP_WO_BLOCKS = 128 * 3;
constexpr int PREP_INIT_BLOCKS = 64;
constexpr int PREP_BLOCKS = PREP_LN_BLOCKS + PREP_WQKV_BLOCKS + PREP_WO_BLOCKS + PREP_INIT_BLOCKS;

__device__ __forceinline__ void tile_weights(const u16* __restrict__ W, int N, int k0, int n0, int ncols, u16* __restrict__ dst,
                                             int nks_total, int ks, int nt0, u16* lds) {
  const int tid = threadIdx.x;
  const int chunks_per_row = ncols / 8;
  for (int i = tid; i < 32 * chunks_per_row; i += 256) {
    int row = i / chunks_per_row, ch = i % chunks_per_row;
    u32x4 v = *(const u32x4*)(W + (size_t)(k0 + row) * N + n0 + ch * 8);
    *(u32x4*)(lds + row * 264 + ch * 8) = v;
  }
  __syncthreads();
  const int ntiles = ncols / 16;
  for (int o = tid; o < ntiles * 64; o += 256) {
    int t = o >> 6, l = o & 63, r = l & 15, g = l >> 4;
    int col = 32 * (t >> 1) + p32(t & 1, r);
    u16 e[8];
#pragma unroll
    for (int j = 0; j < 8; ++j) e[j] = lds[(8 * g + j) * 264 + col];
    u32x4 v = {(u32)e[0] | ((u32)e[1] << 16), (u32)e[2] | ((u32)e[3] << 16), (u32)e[4] | ((u32)e[5] << 16), (u32)e[6] | ((u32)e[7] << 16)};
    *(u32x4*)(dst + (((size_t)(nt0 + t) * nks_total + ks) * 64 + l) * 8) = v;
  }
}

__global__ __launch_bounds__(256) void prep_kernel(const u16* __restrict__ x, const u16* __restrict__ mem0, const u16* __restrict__ Wq,
                                                   const u16* __restrict__ Wk, const u16* __restrict__ Wv, const u16* __restrict__ Wo,
                                                   const u16* __restrict__ w_r, char* __restrict__ ws) {
  __shared__ __attribute__((aligned(16))) u16 lds[32 * 264];
  const int b = blockIdx.x, tid = threadIdx.x;
  if (b < PREP_LN_BLOCKS) {
    const int w = tid >> 6, l = tid & 63;
    const int t = b * 4 + w;
    const u16* xr = x + (size_t)t * DM;
    u32x4 c0 = *(const u32x4*)(xr + l * 8);
    u32x4 c1 = {0, 0, 0, 0};
    if (l < 16) c1 = *(const u32x4*)(xr + 512 + l * 8);
    float v[16];
    v[0] = bflo(c0.x); v[1] = bfhi(c0.x); v[2] = bflo(c0.y); v[3] = bfhi(c0.y); v[4] = bflo(c0.z); v[5] = bfhi(c0.z); v[6] = bflo(c0.w); v[7] = bfhi(c0.w);
    v[8] = bflo(c1.x); v[9] = bfhi(c1.x); v[10] = bflo(c1.y); v[11] = bfhi(c1.y); v[12] = bflo(c1.z); v[13] = bfhi(c1.z); v[14] = bflo(c1.w); v[15] = bfhi(c1.w);
    float s = 0.f;
#pragma unroll
    for (int j = 0; j < 16; ++j) s += v[j];
#pragma unroll
    for (int o = 32; o > 0; o >>= 1) s += __shfl_xor(s, o);
    const float mean = s * (1.0f / DM);
    float q = 0.f;
#pragma unroll
    for (int j = 0; j < 8; ++j) { float d = v[j] - mean; q += d * d; }
    if (l < 16) {
#pragma unroll
      for (int j = 8; j < 16; ++j) { float d = v[j] - mean; q += d * d; }
    }
#pragma unroll
    for (int o = 32; o > 0; o >>= 1) q += __shfl_xor(q, o);
    const float var_b = bfr(q * (1.0f / DM));
    const float mean_b = bfr(mean);
    const float den = bfr(sqrtf(bfr(var_b + 1e-05f)));
    float y[16];
#pragma unroll
    for (int j = 0; j < 16; ++j) y[j] = bfr(bfr(v[j] - mean_b) / den);
    u32x4 w0 = *(const u32x4*)(w_r + l * 8);
    u32x4 w1 = {0, 0, 0, 0};
    if (l < 16) w1 = *(const u32x4*)(w_r + 512 + l * 8);
    float rs = y[0] * bflo(w0.x) + y[1] * bfhi(w0.x) + y[2] * bflo(w0.y) + y[3] * bfhi(w0.y) + y[4] * bflo(w0.z) + y[5] * bfhi(w0.z) + y[6] * bflo(w0.w) + y[7] * bfhi(w0.w);
    if (l < 16) rs += y[8] * bflo(w1.x) + y[9] * bfhi(w1.x) + y[10] * bflo(w1.y) + y[11] * bfhi(w1.y) + y[12] * bflo(w1.z) + y[13] * bfhi(w1.z) + y[14] * bflo(w1.w) + y[15] * bfhi(w1.w);
#pragma unroll
    for (int o = 32; o > 0; o >>= 1) rs += __shfl_xor(rs, o);
    if (l == 0) ((float*)(ws + OFF_R))[t] = bfr(rs);
    u16* xn = (u16*)(ws + OFF_XN);
    u16* xnf = (u16*)(ws + OFF_XNF);
    const int sg = t >> 5, tt = t & 31;
    const int mt = 2 * sg + ((tt >> 2) & 1), r = 4 * (tt >> 3) + (tt & 3);
    u32x4 o0 = {pack2(y[0], y[1]), pack2(y[2], y[3]), pack2(y[4], y[5]), pack2(y[6], y[7])};
    *(u32x4*)(xn + (size_t)t * DM + l * 8) = o0;
    { int c = l; *(u32x4*)(xnf + (((size_t)mt * 20 + (c >> 2)) * 64 + (c & 3) * 16 + r) * 8) = o0; }
    if (l < 16) {
      u32x4 o1 = {pack2(y[8], y[9]), pack2(y[10], y[11]), pack2(y[12], y[13]), pack2(y[14], y[15])};
      *(u32x4*)(xn + (size_t)t * DM + 512 + l * 8) = o1;
      int c = 64 + l;
      *(u32x4*)(xnf + (((size_t)mt * 20 + (c >> 2)) * 64 + (c & 3) * 16 + r) * 8) = o1;
    }
  } else if (b < PREP_LN_BLOCKS + PREP_WQKV_BLOCKS) {
    int i = b - PREP_LN_BLOCKS;
    int mat = i / 320, rem = i % 320, ks = rem / 16, nb = rem % 16;
    const u16* W = mat == 0 ? Wq : (mat == 1 ? Wk : Wv);
    tile_weights(W, HD, ks * 32, nb * 256, 256, (u16*)(ws + OFF_WF), 20, ks, mat * 256 + nb * 16, lds);
  } else if (b < PREP_LN_BLOCKS + PREP_WQKV_BLOCKS + PREP_WO_BLOCKS) {
    int i = b - PREP_LN_BLOCKS - PREP_WQKV_BLOCKS;
    int ks = i / 3, nb = i % 3;
    tile_weights(Wo, DM, ks * 32, nb * 256, nb == 2 ? 128 : 256, (u16*)(ws + OFF_WOF), 128, ks, nb * 16, lds);
  } else {
    int i = b - (PREP_LN_BLOCKS + PREP_WQKV_BLOCKS + PREP_WO_BLOCKS);
    u32x4 sv = {SENT, SENT, SENT, SENT};
    u32x4* p = (u32x4*)(ws + OFF_INBOX);
    const size_t n16 = (2 * (size_t)INBOX_SLOT) / 16;
    for (size_t j = (size_t)i * 256 + tid; j < n16; j += (size_t)PREP_INIT_BLOCKS * 256) p[j] = sv;
    u32x4* pm = (u32x4*)(ws + OFF_MEMB + MEM_STEP);
    const size_t m16 = (size_t)64 * MEM_STEP / 16;
    for (size_t j = (size_t)i * 256 + tid; j < m16; j += (size_t)PREP_INIT_BLOCKS * 256) pm[j] = sv;
    if (i == 0) {
      for (int j = tid; j < 640; j += 256) {
        int p_ = j >> 3, m = j & 7;
        u32x4 v = *(const u32x4*)(mem0 + m * DM + p_ * 8);
        if (v.x == SENT) v.x = 0xFFC0FFC0u;
        if (v.y == SENT) v.y = 0xFFC0FFC0u;
        if (v.z == SENT) v.z = 0xFFC0FFC0u;
        if (v.w == SENT) v.w = 0xFFC0FFC0u;
        for (u32 rp = 0; rp < MREP; ++rp) *(u32x4*)(ws + OFF_MEMB + rp * MEM_REP + (size_t)j * 16) = v;
      }
      if (tid == 0) *(u32*)(ws + OFF_ERR) = 0;
    }
    if (i == 1) {
      u32x4* pf = (u32x4*)(ws + OFF_MCNT);
      for (int j = tid; j < (int)((OFF_FLAG_END - OFF_MCNT) / 16); j += 256) pf[j] = (u32x4){0, 0, 0, 0};
    }
  }
}

__device__ __forceinline__ void glds16(const void* g, void* l) {
  __builtin_amdgcn_global_load_lds((const __attribute__((address_space(1))) void*)g, (__attribute__((address_space(3))) void*)l, 16, 0, 0);
}

constexpr u32 SPIN_CAP = 1u << 16;
#define SLP_NR 56
#define SLP_RI 6
#define SLP_D1 10
#define SLP_D2 10
#define SLP_RM 4
__device__ __forceinline__ bool has_sent(u32x4 v) { return (v.x == SENT) | (v.y == SENT) | (v.z == SENT) | (v.w == SENT); }
__device__ __forceinline__ u32 desent(float f) { u32 b = __float_as_uint(f); return b == SENT ? 0x7FC00000u : b; }

template <int SLP = 8>
__device__ __forceinline__ void wait_count(const u32* p, u32 want, u32* err) {
  u32 spins = 0;
  for (;;) {
    u32 v = __builtin_amdgcn_readfirstlane(__hip_atomic_load(p, __ATOMIC_RELAXED, __HIP_MEMORY_SCOPE_AGENT));
    if (v >= want) break;
    __builtin_amdgcn_s_sleep(SLP);
    if (++spins > (1u << 20)) { *err = 3; break; }
  }
}

__device__ __forceinline__ u32x4 mk_rsrc(const void* p, u32 n) {
  const unsigned long long a = (unsigned long long)p;
  return (u32x4){(u32)__builtin_amdgcn_readfirstlane((u32)a), (u32)__builtin_amdgcn_readfirstlane((u32)(a >> 32) & 0xffffu), n, 0x00020000u};
}

constexpr int CH_MEMF = 0, CH_PART = 20480, CH_WV = 40960, CH_WO = 81920, CH_SRED = 122880, CH_LDS = 123904;

#define MFMA_A(acc, wa, mb) asm volatile("v_mfma_f32_16x16x32_bf16 %0, %1, %2, %0" : "+v"(acc) : "a"(wa), "v"(mb))
#define MFMA_V(acc, wa, mb) asm volatile("v_mfma_f32_16x16x32_bf16 %0, %1, %2, %0" : "+v"(acc) : "v"(wa), "v"(mb))
#define MFMA_A0(acc, wa, mb) asm volatile("v_mfma_f32_16x16x32_bf16 %0, %1, %2, 0" : "=&v"(acc) : "a"(wa), "v"(mb))
#define MFMA_V0(acc, wa, mb) asm volatile("v_mfma_f32_16x16x32_bf16 %0, %1, %2, 0" : "=&v"(acc) : "v"(wa), "v"(mb))
#define MFMA_NV(acc, wa, mb) asm volatile("s_nop 1\n\tv_mfma_f32_16x16x32_bf16 %0, %1, %2, %0" : "+v"(acc) : "v"(wa), "v"(mb))
#define MFMA_NV0(acc, wa, mb) asm volatile("s_nop 1\n\tv_mfma_f32_16x16x32_bf16 %0, %1, %2, 0" : "=&v"(acc) : "v"(wa), "v"(mb))

#define STAMP(i)
__device__ __forceinline__ void chain_role(char* __restrict__ ws, char* smem, const int c) {
  const int tid = threadIdx.x, w = __builtin_amdgcn_readfirstlane(tid >> 6), l = tid & 63;
  const int h = c >> 2, gs = c & 3;
  const int m = l & 15, g = l >> 4;
  const char* wf = ws + OFF_WF;
  const char* wof = ws + OFF_WOF;
  bf16x8 wq[2][20], wk[2][20];
#pragma unroll
  for (int T = 0; T < 2; ++T)
#pragma unroll
    for (int i = 0; i < 20; ++i) {
      const int ks = (5 * w + i) % 20;
      wq[T][i] = *(const bf16x8*)(wf + (((size_t)(2 * (4 * h + w) + T) * 20 + ks) * 64 + l) * 16);
      wk[T][i] = *(const bf16x8*)(wf + (((size_t)(256 + 2 * (4 * h + w) + T) * 20 + ks) * 64 + l) * 16);
    }
  for (int i = tid; i < 40 * 64; i += 256) {
    int f = i >> 6, ll = i & 63;
    *(u32x4*)(smem + CH_WV + i * 16) = *(const u32x4*)(wf + (((size_t)(512 + 2 * (4 * h + gs) + f / 20) * 20 + f % 20) * 64 + ll) * 16);
    *(u32x4*)(smem + CH_WO + i * 16) = *(const u32x4*)(wof + (((size_t)f * 128 + (4 * h + gs)) * 64 + ll) * 16);
  }
  for (int i = tid; i < 1280; i += 256) *(u32x4*)(smem + CH_MEMF + i * 16) = (u32x4){0, 0, 0, 0};
  __syncthreads();

  const __amdgpu_buffer_rsrc_t rin = __builtin_amdgcn_make_buffer_rsrc(ws + OFF_INBOX, 0, 2 * INBOX_SLOT, 0x00020000);
  const __amdgpu_buffer_rsrc_t rmem = __builtin_amdgcn_make_buffer_rsrc(ws + OFF_MEMB, 0, (u32)SZ_MEMB, 0x00020000);
  const u32x4 din = mk_rsrc(ws + OFF_INBOX, 2 * INBOX_SLOT), dmem = mk_rsrc(ws + OFF_MEMB, (u32)SZ_MEMB), dkv = mk_rsrc(ws + OFF_K, (u32)(2 * SZ_Q));
  const u32x4 dflag = mk_rsrc(ws + OFF_KVC, 16 * 32 * 64);
  const __amdgpu_buffer_rsrc_t rkm = __builtin_amdgcn_make_buffer_rsrc(ws + OFF_KM, 0, (u32)(2 * SZ_KM), 0x00020000);
  u32* mcnt = (u32*)(ws + OFF_MCNT);
  u32* err = (u32*)(ws + OFF_ERR);
  const float scale = 0.08838834764831845f;
  const char* lmem = smem + CH_MEMF + l * 16;

  u32x4 qt[8];
#pragma unroll
  for (int i = 0; i < 8; ++i) qt[i] = (u32x4){0, 0, 0, 0};
  u32x4 kv4[4];
  const u32 kvo0 = (u32)((p32(0, m) * HD + h * 128 + 32 * w + 8 * g) * 2), kvo1 = (u32)((p32(1, m) * HD + h * 128 + 32 * w + 8 * g) * 2);
  const u32 kvo2 = (u32)(SZ_Q + ((h * 128 + 32 * gs + p32(0, m)) * 32 + 8 * g) * 2), kvo3 = (u32)(SZ_Q + ((h * 128 + 32 * gs + p32(1, m)) * 32 + 8 * g) * 2);
  auto load_kv = [&](int sn) {
    const u32 so = (u32)sn * (32 * HD * 2);
    asm volatile("buffer_load_dwordx4 v[192:195], %[o0], %[rs], %[so] offen sc1\n\t"
                 "buffer_load_dwordx4 v[196:199], %[o1], %[rs], %[so] offen sc1\n\t"
                 "buffer_load_dwordx4 v[200:203], %[o2], %[rs], %[so] offen sc1\n\t"
                 "buffer_load_dwordx4 v[204:207], %[o3], %[rs], %[so] offen sc1"
                 : "=&{v[192:195]}"(kv4[0]), "=&{v[196:199]}"(kv4[1]), "=&{v[200:203]}"(kv4[2]), "=&{v[204:207]}"(kv4[3])
                 : [o0] "v"(kvo0), [o1] "v"(kvo1), [o2] "v"(kvo2), [o3] "v"(kvo3), [rs] "s"(dkv), [so] "s"(so)
                 : "memory");
  };
  const __amdgpu_buffer_rsrc_t rmc = __builtin_amdgcn_make_buffer_rsrc(ws + OFF_MCNT, 0, 64 * 32 * 64, 0x00020000);
  const u32 mco = tid == 0 ? (u32)h * 64 : 0x80000000u;
  const u32 rso = c < (int)NRED ? (u32)(c * NPROD * 128 + w * 4096 + l * 16) : 0x80000000u;
  wait_count((const u32*)(ws + OFF_KVC) + h * 16, 1u, err);
  load_kv(0);
  for (int s = 0; s < NSEG; ++s) {
    bf16x8 wv[2][5];
#pragma unroll
    for (int T = 0; T < 2; ++T)
#pragma unroll
      for (int i = 0; i < 5; ++i) wv[T][i] = *(const bf16x8*)(smem + CH_WV + (T * 20 + 5 * w + i) * 1024 + l * 16);
    u32x4 mfo[5];
    {
      const u32 o = s * MEM_STEP + (c & (MREP - 1)) * MEM_REP + w * 2560 + (m >> 3) * 512 + g * 128 + (m & 7) * 16;
      const u32 o2 = m < 8 ? o : 0x80000000u;
      const u32 la = (u32)(uintptr_t)(__attribute__((address_space(3))) char*)smem + CH_MEMF + w * 5120 + (m >> 3) * 1024 + (g * 16 + (m & 7)) * 16;
      const u32 la2 = (u32)(uintptr_t)(__attribute__((address_space(3))) char*)smem + CH_MEMF + w * 5120 + 4096 + l * 16;
      u32 tmx, cnt;
      const u32 fo = (u32)((min(s + 1, NSEG - 1) >> 2) * 32 + h) * 64;
      asm volatile(
          "buffer_load_dwordx4 v[232:235], %[o], %[rs], 0 offen sc1\n\t"
          "buffer_load_dwordx4 v[236:239], %[o], %[rs], 0 offen offset:1024 sc1\n\t"
          "buffer_load_dwordx4 v[240:243], %[o2], %[rs], 0 offen offset:2048 sc1\n\t"
          "buffer_load_dword v230, off, %[rf], %[fo] sc1\n\t"
          "s_mov_b32 %[cnt], 0\n\t"
          "s_sleep %[slp]\n"
          "1:\n\t"
          "buffer_load_dwordx4 v[244:247], %[o], %[rs], 0 offen sc1\n\t"
          "buffer_load_dwordx4 v[248:251], %[o], %[rs], 0 offen offset:1024 sc1\n\t"
          "buffer_load_dwordx4 v[252:255], %[o2], %[rs], 0 offen offset:2048 sc1\n\t"
          "buffer_load_dword v231, off, %[rf], %[fo] sc1\n\t"
          "s_waitcnt vmcnt(4)\n\t"
          "v_add_u32 %[t], -1, v230\n\t"
          "v_max3_u32 %[t], %[t], v232, v233\n\t"
          "v_max3_u32 %[t], %[t], v234, v235\n\t"
          "v_max3_u32 %[t], %[t], v236, v237\n\t"
          "v_max3_u32 %[t], %[t], v238, v239\n\t"
          "v_max3_u32 %[t], %[t], v240, v241\n\t"
          "v_max3_u32 %[t], %[t], v242, v243\n\t"
          "v_cmp_eq_u32 vcc, -1, %[t]\n\t"
          "s_cbranch_vccz 2f\n\t"
          "buffer_load_dwordx4 v[232:235], %[o], %[rs], 0 offen sc1\n\t"
          "buffer_load_dwordx4 v[236:239], %[o], %[rs], 0 offen offset:1024 sc1\n\t"
          "buffer_load_dwordx4 v[240:243], %[o2], %[rs], 0 offen offset:2048 sc1\n\t"
          "buffer_load_dword v230, off, %[rf], %[fo] sc1\n\t"
          "s_waitcnt vmcnt(4)\n\t"
          "v_add_u32 %[t], -1, v231\n\t"
          "v_max3_u32 %[t], %[t], v244, v245\n\t"
          "v_max3_u32 %[t], %[t], v246, v247\n\t"
          "v_max3_u32 %[t], %[t], v248, v249\n\t"
          "v_max3_u32 %[t], %[t], v250, v251\n\t"
          "v_max3_u32 %[t], %[t], v252, v253\n\t"
          "v_max3_u32 %[t], %[t], v254, v255\n\t"
          "v_cmp_eq_u32 vcc, -1, %[t]\n\t"
          "s_cbranch_vccz 3f\n\t"
          "s_add_u32 %[cnt], %[cnt], 1\n\t"
          "s_cmp_lt_u32 %[cnt], 0x10000\n\t"
          "s_cbranch_scc1 1b\n"
          "3:\n\t"
          "ds_write_b128 %[la], v[244:247] offset:0\n\t"
          "ds_write_b128 %[la], v[248:251] offset:2048\n\t"
          "ds_write_b128 %[la2], v[252:255]\n\t"
          "v_mov_b64 v[172:173], v[244:245]\n\t"
          "v_mov_b64 v[174:175], v[246:247]\n\t"
          "v_mov_b32_dpp v176, v244 row_ror:8 row_mask:0xf bank_mask:0xf\n\t"
          "v_mov_b32_dpp v177, v245 row_ror:8 row_mask:0xf bank_mask:0xf\n\t"
          "v_mov_b32_dpp v178, v246 row_ror:8 row_mask:0xf bank_mask:0xf\n\t"
          "v_mov_b32_dpp v179, v247 row_ror:8 row_mask:0xf bank_mask:0xf\n\t"
          "v_mov_b64 v[180:181], v[248:249]\n\t"
          "v_mov_b64 v[182:183], v[250:251]\n\t"
          "v_mov_b32_dpp v184, v248 row_ror:8 row_mask:0xf bank_mask:0xf\n\t"
          "v_mov_b32_dpp v185, v249 row_ror:8 row_mask:0xf bank_mask:0xf\n\t"
          "v_mov_b32_dpp v186, v250 row_ror:8 row_mask:0xf bank_mask:0xf\n\t"
          "v_mov_b32_dpp v187, v251 row_ror:8 row_mask:0xf bank_mask:0xf\n\t"
          "v_mov_b64 v[188:189], v[252:253]\n\t"
          "v_mov_b64 v[190:191], v[254:255]\n\t"
          "s_branch 4f\n"
          "2:\n\t"
          "ds_write_b128 %[la], v[232:235] offset:0\n\t"
          "ds_write_b128 %[la], v[236:239] offset:2048\n\t"
          "ds_write_b128 %[la2], v[240:243]\n\t"
          "v_mov_b64 v[172:173], v[232:233]\n\t"
          "v_mov_b64 v[174:175], v[234:235]\n\t"
          "v_mov_b32_dpp v176, v232 row_ror:8 row_mask:0xf bank_mask:0xf\n\t"
          "v_mov_b32_dpp v177, v233 row_ror:8 row_mask:0xf bank_mask:0xf\n\t"
          "v_mov_b32_dpp v178, v234 row_ror:8 row_mask:0xf bank_mask:0xf\n\t"
          "v_mov_b32_dpp v179, v235 row_ror:8 row_mask:0xf bank_mask:0xf\n\t"
          "v_mov_b64 v[180:181], v[236:237]\n\t"
          "v_mov_b64 v[182:183], v[238:239]\n\t"
          "v_mov_b32_dpp v184, v236 row_ror:8 row_mask:0xf bank_mask:0xf\n\t"
          "v_mov_b32_dpp v185, v237 row_ror:8 row_mask:0xf bank_mask:0xf\n\t"
          "v_mov_b32_dpp v186, v238 row_ror:8 row_mask:0xf bank_mask:0xf\n\t"
          "v_mov_b32_dpp v187, v239 row_ror:8 row_mask:0xf bank_mask:0xf\n\t"
          "v_mov_b64 v[188:189], v[240:241]\n\t"
          "v_mov_b64 v[190:191], v[242:243]\n\t"
          "4:\n\t"
          "s_nop 1"
          : "+{v[224:227]}"(qt[0]), "+{v[228:231]}"(qt[1]), "+{v[232:235]}"(qt[2]), "+{v[236:239]}"(qt[3]), "+{v[240:243]}"(qt[4]), "+{v[244:247]}"(qt[5]), "+{v[248:251]}"(qt[6]), "+{v[252:255]}"(qt[7]), [t] "=&v"(tmx), [cnt] "=&s"(cnt),
            "+{v[192:195]}"(kv4[0]), "+{v[196:199]}"(kv4[1]), "+{v[200:203]}"(kv4[2]), "+{v[204:207]}"(kv4[3]),
            "=&{v[172:175]}"(mfo[0]), "=&{v[176:179]}"(mfo[1]), "=&{v[180:183]}"(mfo[2]), "=&{v[184:187]}"(mfo[3]), "=&{v[188:191]}"(mfo[4])
          : [o] "v"(o), [o2] "v"(o2), [rs] "s"(dmem), [la] "v"(la), [la2] "v"(la2), [slp] "i"(SLP_D2), [rf] "s"(dflag), [fo] "s"(fo)
          : "vcc", "scc", "memory");
      if (cnt >= 0x10000u && l == 0) *err = 1;
    }
    STAMP(0);
    f32x4 aq0, aq1, ak0, ak1, av0, av1;
    bf16x8 wo[10];
    {
      bf16x8 mfA[5], mfB[5];
      const char* lc1 = lmem + (5 * ((w + 1) & 3)) * 1024;
      const char* lc2 = lmem + (5 * ((w + 2) & 3)) * 1024;
      const char* lc3 = lmem + (5 * ((w + 3) & 3)) * 1024;
      MFMA_V0(av0, mfo[0], wv[0][0]); MFMA_V0(av1, mfo[0], wv[1][0]);
#pragma unroll
      for (int i = 1; i < 5; ++i) { MFMA_V(av0, mfo[i], wv[0][i]); MFMA_V(av1, mfo[i], wv[1][i]); }
      MFMA_A0(aq0, wq[0][0], mfo[0]); MFMA_A0(aq1, wq[1][0], mfo[0]); MFMA_A0(ak0, wk[0][0], mfo[0]); MFMA_A0(ak1, wk[1][0], mfo[0]);
#pragma unroll
      for (int i = 1; i < 3; ++i) { MFMA_A(aq0, wq[0][i], mfo[i]); MFMA_A(aq1, wq[1][i], mfo[i]); MFMA_A(ak0, wk[0][i], mfo[i]); MFMA_A(ak1, wk[1][i], mfo[i]); }
      asm volatile("s_waitcnt lgkmcnt(0)" ::: "memory");
      __syncthreads();
      STAMP(1);
#pragma unroll
      for (int i = 0; i < 5; ++i) mfB[i] = *(const bf16x8*)(lc1 + i * 1024);
#pragma unroll
      for (int i = 0; i < 5; ++i) mfA[i] = *(const bf16x8*)(lc2 + i * 1024);
      __builtin_amdgcn_sched_barrier(0);
#pragma unroll
      for (int i = 3; i < 5; ++i) { MFMA_A(aq0, wq[0][i], mfo[i]); MFMA_A(aq1, wq[1][i], mfo[i]); MFMA_A(ak0, wk[0][i], mfo[i]); MFMA_A(ak1, wk[1][i], mfo[i]); }
#pragma unroll
      for (int i = 0; i < 5; ++i) { MFMA_A(aq0, wq[0][5 + i], mfB[i]); MFMA_A(aq1, wq[1][5 + i], mfB[i]); MFMA_A(ak0, wk[0][5 + i], mfB[i]); MFMA_A(ak1, wk[1][5 + i], mfB[i]); }
#pragma unroll
      for (int i = 0; i < 5; ++i) mfB[i] = *(const bf16x8*)(lc3 + i * 1024);
      (void)__builtin_amdgcn_raw_ptr_buffer_atomic_add_i32(1, rmc, s > 0 ? mco : 0x80000000u, (u32)(s - 1) * (32 * 64), 16);
      MFMA_A(aq0, wq[0][10], mfA[0]); MFMA_A(aq1, wq[1][10], mfA[0]); MFMA_A(ak0, wk[0][10], mfA[0]); MFMA_A(ak1, wk[1][10], mfA[0]);
      {
        const u32x4 sv = {SENT, SENT, SENT, SENT};
        const u32 ro = s > 0 ? rso : 0x80000000u;
        const u32 rb = ((s - 1) & 1) * INBOX_SLOT;
#pragma unroll
        for (int i = 1; i < 5; ++i) {
          __builtin_amdgcn_raw_buffer_store_b128(sv, rin, ro, rb + (i - 1) * 1024, 16);
          wo[2 * i - 2] = *(const bf16x8*)(smem + CH_WO + (10 * w + 2 * i - 2) * 1024 + l * 16);
          wo[2 * i - 1] = *(const bf16x8*)(smem + CH_WO + (10 * w + 2 * i - 1) * 1024 + l * 16);
          __builtin_amdgcn_sched_barrier(0);
          MFMA_A(aq0, wq[0][10 + i], mfA[i]); MFMA_A(aq1, wq[1][10 + i], mfA[i]); MFMA_A(ak0, wk[0][10 + i], mfA[i]); MFMA_A(ak1, wk[1][10 + i], mfA[i]);
        }
      }
      wo[8] = *(const bf16x8*)(smem + CH_WO + (10 * w + 8) * 1024 + l * 16);
      wo[9] = *(const bf16x8*)(smem + CH_WO + (10 * w + 9) * 1024 + l * 16);
      __builtin_amdgcn_sched_barrier(0);
      MFMA_A(aq0, wq[0][15], mfB[0]); MFMA_A(aq1, wq[1][15], mfB[0]); MFMA_A(ak0, wk[0][15], mfB[0]); MFMA_A(ak1, wk[1][15], mfB[0]);
#pragma unroll
      for (int i = 1; i < 5; ++i) { MFMA_V(aq0, wq[0][15 + i], mfB[i]); MFMA_V(aq1, wq[1][15 + i], mfB[i]); MFMA_V(ak0, wk[0][15 + i], mfB[i]); MFMA_V(ak1, wk[1][15 + i], mfB[i]); }
      asm volatile("s_nop 7\n\ts_nop 7" : "+v"(aq0), "+v"(aq1), "+v"(ak0), "+v"(ak1), "+v"(av0), "+v"(av1));
    }
    STAMP(2);
    u32x4 qp = {pack2(aq0.x, aq0.y), pack2(aq0.z, aq0.w), pack2(aq1.x, aq1.y), pack2(aq1.z, aq1.w)};
    u32x4 kp = {pack2(ak0.x, ak0.y), pack2(ak0.z, ak0.w), pack2(ak1.x, ak1.y), pack2(ak1.z, ak1.w)};
    f32x4 sM, sX0, sX1;
    MFMA_NV0(sM, kp, qp); MFMA_V0(sX0, kv4[0], qp); MFMA_V0(sX1, kv4[1], qp);
    asm volatile("s_nop 7\n\ts_nop 1" : "+v"(sM), "+v"(sX0), "+v"(sX1));
    {
      char* pp = smem + CH_PART + w * 5120 + l * 16;
      *(f32x4*)(pp + 3072) = av0; *(f32x4*)(pp + 4096) = av1;
      *(f32x4*)(pp) = sM; *(f32x4*)(pp + 1024) = sX0; *(f32x4*)(pp + 2048) = sX1;
    }
    if (w == gs && m < 8) __builtin_amdgcn_raw_buffer_store_b128(kp, rkm, (u32)(((s * 8 + m) * HD + h * 128 + 32 * w + 8 * g) * 2), 0, 16);
    __syncthreads();
    STAMP(3);
    f32x4 ta, tb, tc;
    {
      const char* pa = smem + CH_PART + (m < 8 ? 1024 : 2048) + ((m & 7) + 16 * g) * 16;
      const char* pb = smem + CH_PART + (g < 2 ? 3072 : 4096) + (m + 16 * (g & 1)) * 16;
      const char* pc = smem + CH_PART + l * 16;
      ta = (*(const f32x4*)(pa) + *(const f32x4*)(pa + 5120)) + (*(const f32x4*)(pa + 10240) + *(const f32x4*)(pa + 15360));
      tc = (*(const f32x4*)(pc) + *(const f32x4*)(pc + 5120)) + (*(const f32x4*)(pc + 10240) + *(const f32x4*)(pc + 15360));
      tb = (*(const f32x4*)(pb) + *(const f32x4*)(pb + 5120)) + (*(const f32x4*)(pb + 10240) + *(const f32x4*)(pb + 15360));
    }
    float sx[8];
    {
      const u32 u0 = pack2(ta.x, ta.y), u1 = pack2(ta.z, ta.w), u2 = pack2(tc.x, tc.y), u3 = pack2(tc.z, tc.w);
      const u32 r0 = pack2(bflo(u0) * scale, bfhi(u0) * scale), r1 = pack2(bflo(u1) * scale, bfhi(u1) * scale);
      const u32 r2 = pack2(bflo(u2) * scale, bfhi(u2) * scale), r3 = pack2(bflo(u3) * scale, bfhi(u3) * scale);
      const bool mv = (m < 8) & (g < 2);
      const u32 q2 = mv ? r2 : 0xFF80FF80u, q3 = mv ? r3 : 0xFF80FF80u;
      sx[0] = bflo(r0); sx[1] = bfhi(r0); sx[2] = bflo(r1); sx[3] = bfhi(r1);
      sx[4] = bflo(q2); sx[5] = bfhi(q2); sx[6] = bflo(q3); sx[7] = bfhi(q3);
    }
    float mx = fmaxf(fmaxf(fmaxf(sx[0], sx[1]), fmaxf(sx[2], sx[3])), fmaxf(fmaxf(sx[4], sx[5]), fmaxf(sx[6], sx[7])));
    mx = xr32_max(xr16_max(xr8_max(mx)));
    const float L2E = 1.4426950408889634f;
    const float mxl = -mx * L2E;
#pragma unroll
    for (int i = 0; i < 8; ++i) sx[i] = __builtin_amdgcn_exp2f(__builtin_fmaf(sx[i], L2E, mxl));
    float sum = ((sx[0] + sx[1]) + (sx[2] + sx[3])) + ((sx[4] + sx[5]) + (sx[6] + sx[7]));
    sum = xr32_add(xr16_add(xr8_add(sum)));
    const float inv = __builtin_amdgcn_rcpf(sum);
    const u32 pa0 = pack2(sx[0] * inv, sx[1] * inv), pa1 = pack2(sx[2] * inv, sx[3] * inv);
    u32x4 px = {pa0, pa1, (u32)__builtin_amdgcn_update_dpp(0u, pa0, 0x128, 0xf, 0xf, false), (u32)__builtin_amdgcn_update_dpp(0u, pa1, 0x128, 0xf, 0xf, false)};
    u32x4 pm4 = {pack2(sx[4] * inv, sx[5] * inv), pack2(sx[6] * inv, sx[7] * inv), 0u, 0u};
    const u32x2 vmb = {pack2(tb.x, tb.y), pack2(tb.z, tb.w)};
    f32x4 o0, o1;
    {
      u32x4 vm04 = {vmb.x, vmb.y, 0u, 0u};
      u32x4 vm14 = {__builtin_amdgcn_permlane32_swap(vmb.x, vmb.x, false, false)[1], __builtin_amdgcn_permlane32_swap(vmb.y, vmb.y, false, false)[1], 0u, 0u};
      MFMA_NV0(o0, kv4[2], px); MFMA_V0(o1, kv4[3], px);
      MFMA_NV(o0, vm04, pm4); MFMA_NV(o1, vm14, pm4);
      asm volatile("s_nop 7\n\ts_nop 1" : "+v"(o0), "+v"(o1));
    }
    u32x4 op = {pack2(o0.x, o0.y), pack2(o0.z, o0.w), pack2(o1.x, o1.y), pack2(o1.z, o1.w)};
    const int slot = s & 1;
    {
      f32x4 d[10];
      const u32 obase = (m < 8 && s != NSEG - 1) ? slot * INBOX_SLOT + ((20 * w + g) * NPROD + c) * 128 + m * 16 : 0x80000000u;
#define PUB(i) do { u32x4 v_ = {min(pack2(d[2 * (i)].x, d[2 * (i)].y), 0xFFFFFFFEu), min(pack2(d[2 * (i)].z, d[2 * (i)].w), 0xFFFFFFFEu), \
                               min(pack2(d[2 * (i) + 1].x, d[2 * (i) + 1].y), 0xFFFFFFFEu), min(pack2(d[2 * (i) + 1].z, d[2 * (i) + 1].w), 0xFFFFFFFEu)}; \
                   __builtin_amdgcn_raw_buffer_store_b128(v_, rin, obase, (i) * (4 * NPROD * 128), 16); } while (0)
      MFMA_NV0(d[0], wo[0], op); MFMA_V0(d[1], wo[1], op); MFMA_V0(d[2], wo[2], op); MFMA_V0(d[3], wo[3], op);
      asm volatile("" : "+v"(d[0]), "+v"(d[1]));
      PUB(0);
      MFMA_V0(d[4], wo[4], op); MFMA_V0(d[5], wo[5], op);
      asm volatile("" : "+v"(d[2]), "+v"(d[3]));
      PUB(1);
      MFMA_V0(d[6], wo[6], op); MFMA_V0(d[7], wo[7], op);
      asm volatile("" : "+v"(d[4]), "+v"(d[5]));
      PUB(2);
      MFMA_V0(d[8], wo[8], op); MFMA_V0(d[9], wo[9], op);
      asm volatile("" : "+v"(d[6]), "+v"(d[7]));
      PUB(3);
      asm volatile("s_nop 7\n\ts_nop 1" : "+v"(d[8]), "+v"(d[9]));
      PUB(4);
    }
    STAMP(4);
    if (w == 0) __builtin_amdgcn_raw_buffer_store_b64(vmb, rkm, (u32)(SZ_KM + (((s * 32 + h) * 128 + 32 * gs + p32(g >> 1, m)) * 8 + 4 * (g & 1)) * 2), 0, 16);
    const int doit = (c - (int)NRED) & (s - (NSEG - 1));
    {
      const u32 base = slot * INBOX_SLOT + (c < (int)NRED ? c : 0) * NPROD * 128 + w * 4096 + l * 16;
      u32x4 v[4];
      u32 tmx, cnt;
      asm volatile(
          "s_mov_b32 %[cnt], 0\n\t"
          "s_cmp_ge_i32 %[doit], 0\n\t"
          "s_cbranch_scc1 4f\n\t"
          "s_sleep %[slp0]\n\t"
          "buffer_load_dwordx4 v[224:227], %[o], %[rs], 0 offen offset:0 sc1\n\t"
          "buffer_load_dwordx4 v[228:231], %[o], %[rs], 0 offen offset:1024 sc1\n\t"
          "buffer_load_dwordx4 v[232:235], %[o], %[rs], 0 offen offset:2048 sc1\n\t"
          "buffer_load_dwordx4 v[236:239], %[o], %[rs], 0 offen offset:3072 sc1\n\t"
          "s_sleep %[slp]\n"
          "1:\n\t"
          "buffer_load_dwordx4 v[240:243], %[o], %[rs], 0 offen offset:0 sc1\n\t"
          "buffer_load_dwordx4 v[244:247], %[o], %[rs], 0 offen offset:1024 sc1\n\t"
          "buffer_load_dwordx4 v[248:251], %[o], %[rs], 0 offen offset:2048 sc1\n\t"
          "buffer_load_dwordx4 v[252:255], %[o], %[rs], 0 offen offset:3072 sc1\n\t"
          "s_waitcnt vmcnt(4)\n\t"
          "v_max3_u32 %[t], v224, v225, v226\n\t"
          "v_max3_u32 %[t], %[t], v227, v228\n\t"
          "v_max3_u32 %[t], %[t], v229, v230\n\t"
          "v_max3_u32 %[t], %[t], v231, v232\n\t"
          "v_max3_u32 %[t], %[t], v233, v234\n\t"
          "v_max3_u32 %[t], %[t], v235, v236\n\t"
          "v_max3_u32 %[t], %[t], v237, v238\n\t"
          "v_max_u32 %[t], %[t], v239\n\t"
          "v_cmp_eq_u32 vcc, -1, %[t]\n\t"
          "s_cbranch_vccz 2f\n\t"
          "buffer_load_dwordx4 v[224:227], %[o], %[rs], 0 offen offset:0 sc1\n\t"
          "buffer_load_dwordx4 v[228:231], %[o], %[rs], 0 offen offset:1024 sc1\n\t"
          "buffer_load_dwordx4 v[232:235], %[o], %[rs], 0 offen offset:2048 sc1\n\t"
          "buffer_load_dwordx4 v[236:239], %[o], %[rs], 0 offen offset:3072 sc1\n\t"
          "s_waitcnt vmcnt(4)\n\t"
          "v_max3_u32 %[t], v240, v241, v242\n\t"
          "v_max3_u32 %[t], %[t], v243, v244\n\t"
          "v_max3_u32 %[t], %[t], v245, v246\n\t"
          "v_max3_u32 %[t], %[t], v247, v248\n\t"
          "v_max3_u32 %[t], %[t], v249, v250\n\t"
          "v_max3_u32 %[t], %[t], v251, v252\n\t"
          "v_max3_u32 %[t], %[t], v253, v254\n\t"
          "v_max_u32 %[t], %[t], v255\n\t"
          "v_cmp_eq_u32 vcc, -1, %[t]\n\t"
          "s_cbranch_vccz 3f\n\t"
          "s_add_u32 %[cnt], %[cnt], 1\n\t"
          "s_cmp_lt_u32 %[cnt], 0x10000\n\t"
          "s_cbranch_scc1 1b\n"
          "3:\n\t"
          "v_mov_b64 v[172:173], v[240:241]\n\t"
          "v_mov_b64 v[174:175], v[242:243]\n\t"
          "v_mov_b64 v[176:177], v[244:245]\n\t"
          "v_mov_b64 v[178:179], v[246:247]\n\t"
          "v_mov_b64 v[180:181], v[248:249]\n\t"
          "v_mov_b64 v[182:183], v[250:251]\n\t"
          "v_mov_b64 v[184:185], v[252:253]\n\t"
          "v_mov_b64 v[186:187], v[254:255]\n\t"
          "s_branch 4f\n"
          "2:\n\t"
          "v_mov_b64 v[172:173], v[224:225]\n\t"
          "v_mov_b64 v[174:175], v[226:227]\n\t"
          "v_mov_b64 v[176:177], v[228:229]\n\t"
          "v_mov_b64 v[178:179], v[230:231]\n\t"
          "v_mov_b64 v[180:181], v[232:233]\n\t"
          "v_mov_b64 v[182:183], v[234:235]\n\t"
          "v_mov_b64 v[184:185], v[236:237]\n\t"
          "v_mov_b64 v[186:187], v[238:239]\n\t"
          "4:"
          : "=&{v[172:175]}"(v[0]), "=&{v[176:179]}"(v[1]), "=&{v[180:183]}"(v[2]), "=&{v[184:187]}"(v[3]),
            "+{v[224:227]}"(qt[0]), "+{v[228:231]}"(qt[1]), "+{v[232:235]}"(qt[2]), "+{v[236:239]}"(qt[3]), "+{v[240:243]}"(qt[4]), "+{v[244:247]}"(qt[5]), "+{v[248:251]}"(qt[6]), "+{v[252:255]}"(qt[7]), [t] "=&v"(tmx), [cnt] "=&s"(cnt)
          : [o] "v"(base), [rs] "s"(din), [slp] "i"(SLP_D1), [slp0] "i"(SLP_RI), [doit] "s"(doit)
          : "vcc", "scc", "memory");
      load_kv(s != NSEG - 1 ? s + 1 : s);
      if (c >= (int)NRED && s != NSEG - 1) __builtin_amdgcn_s_sleep(SLP_NR);
      if (doit < 0) {
      if (cnt >= 0x10000u && l == 0) *err = 2;
      STAMP(5);
      float a8[8];
#pragma unroll
      for (int j = 0; j < 4; ++j) {
        a8[2 * j] = (bflo(v[0][j]) + bflo(v[1][j])) + (bflo(v[2][j]) + bflo(v[3][j]));
        a8[2 * j + 1] = (bfhi(v[0][j]) + bfhi(v[1][j])) + (bfhi(v[2][j]) + bfhi(v[3][j]));
      }
#pragma unroll
      for (int j = 0; j < 8; ++j) a8[j] = xr32_add(xr16_add(xr8_add(a8[j])));
      if (l < 8) {
        *(f32x4*)(smem + CH_SRED + (w * 8 + l) * 32) = (f32x4){a8[0], a8[1], a8[2], a8[3]};
        *(f32x4*)(smem + CH_SRED + (w * 8 + l) * 32 + 16) = (f32x4){a8[4], a8[5], a8[6], a8[7]};
      }
      __syncthreads();
      if (w == 0) {
        const int l8 = l & 7;
        const f32x4* sr = (const f32x4*)(smem + CH_SRED);
        f32x4 a0 = (sr[2 * l8] + sr[16 + 2 * l8]) + (sr[32 + 2 * l8] + sr[48 + 2 * l8]);
        f32x4 a1 = (sr[2 * l8 + 1] + sr[16 + 2 * l8 + 1]) + (sr[32 + 2 * l8 + 1] + sr[48 + 2 * l8 + 1]);
        u32x4 o = {min(pack2(a0.x, a0.y), 0xFFFFFFFEu), min(pack2(a0.z, a0.w), 0xFFFFFFFEu), min(pack2(a1.x, a1.y), 0xFFFFFFFEu), min(pack2(a1.z, a1.w), 0xFFFFFFFEu)};
        __builtin_amdgcn_raw_buffer_store_b128(o, rmem, (s + 1) * MEM_STEP + (l >> 3) * MEM_REP + c * 128 + l8 * 16, 0, 16);
      }
      STAMP(6);
      __builtin_amdgcn_s_sleep(SLP_RM);
      }
    }
  }
  asm volatile("s_waitcnt vmcnt(0)" ::"{v[224:227]}"(qt[0]), "{v[228:231]}"(qt[1]), "{v[232:235]}"(qt[2]), "{v[236:239]}"(qt[3]), "{v[240:243]}"(qt[4]), "{v[244:247]}"(qt[5]), "{v[248:251]}"(qt[6]), "{v[252:255]}"(qt[7]) : "memory");
  __syncthreads();
  if (tid == 0) __hip_atomic_fetch_add(mcnt + ((NSEG - 1) * 32 + h) * 16, 1u, __ATOMIC_RELAXED, __HIP_MEMORY_SCOPE_AGENT);
}

__device__ __forceinline__ int otid() { int t = threadIdx.x; asm volatile("" : "+v"(t)); return t; }
#define VMCNT(n) asm volatile("s_waitcnt vmcnt(%0)" ::"i"(n) : "memory")
#define RAWBAR() do { asm volatile("s_waitcnt lgkmcnt(0)" ::: "memory"); __builtin_amdgcn_s_barrier(); asm volatile("" ::: "memory"); } while (0)

__device__ __forceinline__ void block_top8(const float* __restrict__ r, int* sidx, float* sval, float* swv, int* swi) {
  const int tid = otid(), w = tid >> 6, l = tid & 63;
  float v[8];
#pragma unroll
  for (int j = 0; j < 8; ++j) v[j] = r[tid + 256 * j];
  for (int round = 0; round < CSEL; ++round) {
    float bv = v[0]; int bi = tid;
#pragma unroll
    for (int j = 1; j < 8; ++j) if (v[j] > bv) { bv = v[j]; bi = tid + 256 * j; }
#pragma unroll
    for (int o = 32; o > 0; o >>= 1) {
      float ov = __shfl_xor(bv, o); int oi = __shfl_xor(bi, o);
      if (ov > bv || (ov == bv && oi < bi)) { bv = ov; bi = oi; }
    }
    if (l == 0) { swv[w] = bv; swi[w] = bi; }
    __syncthreads();
    float fv = swv[0]; int fi = swi[0];
#pragma unroll
    for (int k = 1; k < 4; ++k) { float ov = swv[k]; int oi = swi[k]; if (ov > fv || (ov == fv && oi < fi)) { fv = ov; fi = oi; } }
    if (tid == 0) { sidx[round] = fi; sval[round] = fv; }
#pragma unroll
    for (int j = 0; j < 8; ++j) if (tid + 256 * j == fi) v[j] = -INFINITY;
    __syncthreads();
  }
}

__device__ __forceinline__ void worker_modA(const u16* __restrict__ wg, const u16* __restrict__ wu, char* __restrict__ ws, char* smem, const int blk) {
  float* xs = (float*)smem;
  float* red = (float*)(smem + 20480);
  int* sidx = (int*)(smem + 24576);
  float* sval = (float*)(smem + 24576 + 64);
  float* swv = (float*)(smem + 24576 + 128);
  int* swi = (int*)(smem + 24576 + 192);
  const int tid = otid(), w = tid >> 6, l = tid & 63;
  block_top8((const float*)(ws + OFF_R), sidx, sval, swv, swi);
  const u16* xn = (const u16*)(ws + OFF_XN);
  for (int i = tid; i < 8 * 640; i += 256) { int rr = i / 640, k = i % 640; xs[i] = bf2f(xn[(size_t)sidx[rr] * DM + k]); }
  const __amdgpu_buffer_rsrc_t rh = __builtin_amdgcn_make_buffer_rsrc(ws + OFF_HB, 0, (u32)(OFF_ERR - OFF_HB), 0x00020000);
  if (blk == 0 && tid < 8) {
    __builtin_amdgcn_raw_buffer_store_b32((u32)sidx[tid], rh, (u32)(OFF_SEL - OFF_HB) + tid * 4, 0, 16);
    __builtin_amdgcn_raw_buffer_store_b32(__float_as_uint(bfr(1.0f / (1.0f + __expf(-sval[tid])))), rh, (u32)(OFF_SEL - OFF_HB) + 32 + tid * 4, 0, 16);
  }
  __syncthreads();
  const int cp = tid & 7, kg = tid >> 3;
  const int n0 = blk * 16 + 2 * cp;
  float ag[8][2], au[8][2];
#pragma unroll
  for (int rr = 0; rr < 8; ++rr) { ag[rr][0] = ag[rr][1] = au[rr][0] = au[rr][1] = 0.f; }
  u32 g2[20], u2[20];
#pragma unroll
  for (int i = 0; i < 20; ++i) {
    g2[i] = *(const u32*)(wg + (size_t)(20 * kg + i) * FF + n0);
    u2[i] = *(const u32*)(wu + (size_t)(20 * kg + i) * FF + n0);
  }
#pragma unroll
  for (int i = 0; i < 20; ++i) {
    const float g0 = bflo(g2[i]), g1 = bfhi(g2[i]), u0 = bflo(u2[i]), u1 = bfhi(u2[i]);
#pragma unroll
    for (int rr = 0; rr < 8; ++rr) {
      const float xv = xs[rr * 640 + 20 * kg + i];
      ag[rr][0] += xv * g0; ag[rr][1] += xv * g1; au[rr][0] += xv * u0; au[rr][1] += xv * u1;
    }
  }
#pragma unroll
  for (int rr = 0; rr < 8; ++rr) {
#pragma unroll
    for (int e = 0; e < 2; ++e) {
      ag[rr][e] = xr32_add(xr16_add(xr8_add(ag[rr][e])));
      au[rr][e] = xr32_add(xr16_add(xr8_add(au[rr][e])));
    }
  }
  if (l < 8) {
#pragma unroll
    for (int rr = 0; rr < 8; ++rr) *(f32x4*)(red + (w * 8 + l) * 32 + rr * 4) = (f32x4){ag[rr][0], ag[rr][1], au[rr][0], au[rr][1]};
  }
  __syncthreads();
  if (tid < 64) {
    const int rr = tid >> 3, c2 = tid & 7;
    f32x4 a0 = *(const f32x4*)(red + (0 * 8 + c2) * 32 + rr * 4), a1 = *(const f32x4*)(red + (1 * 8 + c2) * 32 + rr * 4);
    f32x4 a2 = *(const f32x4*)(red + (2 * 8 + c2) * 32 + rr * 4), a3 = *(const f32x4*)(red + (3 * 8 + c2) * 32 + rr * 4);
    f32x4 t = (a0 + a1) + (a2 + a3);
    float hv[2];
#pragma unroll
    for (int e = 0; e < 2; ++e) {
      const float gsum = bfr(t[e]), usum = bfr(t[2 + e]);
      const float sg = bfr(gsum / (1.0f + __expf(-gsum)));
      hv[e] = sg * usum;
    }
    __builtin_amdgcn_raw_buffer_store_b32(pack2(hv[0], hv[1]), rh, (u32)((rr * FF + blk * 16 + 2 * c2) * 2), 0, 16);
  }
  asm volatile("s_waitcnt vmcnt(0)" ::: "memory");
  __syncthreads();
  if (tid == 0) __hip_atomic_fetch_add((u32*)(ws + OFF_HCNT), 1u, __ATOMIC_RELAXED, __HIP_MEMORY_SCOPE_AGENT);
}

__device__ __forceinline__ void worker_modB(const u16* __restrict__ wd, char* __restrict__ ws, char* smem, const int blk) {
  float* hs = (float*)smem;
  float* red = (float*)(smem + 4096);
  const int tid = otid(), w = tid >> 6, l = tid & 63;
  const int cb = blk % 5, kb = blk / 5;
  const int n0 = cb * 128 + 2 * l;
  u32 d2[27];
#pragma unroll
  for (int i = 0; i < 27; ++i) d2[i] = *(const u32*)(wd + (size_t)(kb * 108 + 27 * w + i) * DM + n0);
  wait_count((const u32*)(ws + OFF_HCNT), 108u, (u32*)(ws + OFF_ERR));
  const __amdgpu_buffer_rsrc_t rh = __builtin_amdgcn_make_buffer_rsrc(ws + OFF_HB, 0, (u32)(OFF_ERR - OFF_HB), 0x00020000);
  for (int i = tid; i < 8 * 54; i += 256) {
    const int rr = i / 54, k2 = i % 54;
    const u32 v = __builtin_amdgcn_raw_buffer_load_b32(rh, (u32)((rr * FF + kb * 108 + 2 * k2) * 2), 0, 16);
    hs[rr * 108 + 2 * k2] = bflo(v); hs[rr * 108 + 2 * k2 + 1] = bfhi(v);
  }
  __syncthreads();
  float a[8][2];
#pragma unroll
  for (int rr = 0; rr < 8; ++rr) a[rr][0] = a[rr][1] = 0.f;
#pragma unroll
  for (int i = 0; i < 27; ++i) {
    const float d0 = bflo(d2[i]), d1 = bfhi(d2[i]);
#pragma unroll
    for (int rr = 0; rr < 8; ++rr) { const float hv = hs[rr * 108 + 27 * w + i]; a[rr][0] += hv * d0; a[rr][1] += hv * d1; }
  }
#pragma unroll
  for (int rr = 0; rr < 8; ++rr) { red[(w * 8 + rr) * 128 + 2 * l] = a[rr][0]; red[(w * 8 + rr) * 128 + 2 * l + 1] = a[rr][1]; }
  __syncthreads();
  for (int i = tid; i < 8 * 128; i += 256) {
    const int rr = i >> 7, cc = i & 127;
    const float v = (red[rr * 128 + cc] + red[(8 + rr) * 128 + cc]) + (red[(16 + rr) * 128 + cc] + red[(24 + rr) * 128 + cc]);
    __builtin_amdgcn_raw_buffer_store_b32(__float_as_uint(v), rh, (u32)(OFF_YP - OFF_HB) + (u32)(((kb * 8 + rr) * DM + cb * 128 + cc) * 4), 0, 16);
  }
  asm volatile("s_waitcnt vmcnt(0)" ::: "memory");
  __syncthreads();
  if (tid == 0) __hip_atomic_fetch_add((u32*)(ws + OFF_YCNT), 1u, __ATOMIC_RELAXED, __HIP_MEMORY_SCOPE_AGENT);
}

__device__ __forceinline__ void worker_kv(char* __restrict__ ws, char* smem, const int mb, const int h) {
  const int tid = otid(), w = __builtin_amdgcn_readfirstlane(tid >> 6), l = tid & 63;
  const char* xnf = ws + OFF_XNF;
  const char* wf = ws + OFF_WF;
  const int wr = w >> 1, wc = w & 1;
  const int c = l & 15, g = l >> 4;
  f32x4 acc[4][8];
#pragma unroll
  for (int i = 0; i < 4; ++i)
#pragma unroll
    for (int j = 0; j < 8; ++j) acc[i][j] = (f32x4){0, 0, 0, 0};
  const char* src[6];
#pragma unroll
  for (int i = 0; i < 6; ++i) {
    const int f = 6 * w + i;
    src[i] = ((f < 8) ? xnf + (size_t)(mb * 8 + f) * 20480 : (f < 16 ? wf + (size_t)(256 + h * 8 + (f - 8)) * 20480 : wf + (size_t)(512 + h * 8 + (f - 16)) * 20480)) + l * 16;
  }
  auto stage = [&](int ks) {
#pragma unroll
    for (int i = 0; i < 6; ++i) glds16(src[i] + ks * 1024, smem + (ks % 5) * 24576 + (6 * w + i) * 1024 + l * 16);
  };
#pragma unroll
  for (int ks = 0; ks < 4; ++ks) stage(ks);
#pragma unroll
  for (int ks = 0; ks < 20; ++ks) {
    VMCNT(6 * (ks < 17 ? 3 : 19 - ks));
    RAWBAR();
    if (ks + 4 < 20) stage(ks + 4);
    const char* base = smem + (ks % 5) * 24576 + l * 16;
    bf16x8 a[4], bb[8];
#pragma unroll
    for (int i = 0; i < 4; ++i) a[i] = *(const bf16x8*)(base + (4 * wr + i) * 1024);
#pragma unroll
    for (int j = 0; j < 8; ++j) bb[j] = *(const bf16x8*)(base + (8 + 8 * wc + j) * 1024);
    if (wc == 0) {
#pragma unroll
      for (int i = 0; i < 4; ++i)
#pragma unroll
        for (int j = 0; j < 8; ++j) acc[i][j] = mfma32(bb[j], a[i], acc[i][j]);
    } else {
#pragma unroll
      for (int i = 0; i < 4; ++i)
#pragma unroll
        for (int j = 0; j < 8; ++j) acc[i][j] = mfma32(a[i], bb[j], acc[i][j]);
    }
  }
  const __amdgpu_buffer_rsrc_t rkv = __builtin_amdgcn_make_buffer_rsrc(ws + OFF_K, 0, (u32)(2 * SZ_Q), 0x00020000);
  if (wc == 0) {
#pragma unroll
    for (int i = 0; i < 4; ++i) {
      const int mt = mb * 8 + 4 * wr + i;
      const int tok = 32 * (mt >> 1) + p32(mt & 1, c);
#pragma unroll
      for (int u = 0; u < 4; ++u) {
        f32x4 a0 = acc[i][2 * u], a1 = acc[i][2 * u + 1];
        u32x4 v = {pack2(a0.x, a0.y), pack2(a0.z, a0.w), pack2(a1.x, a1.y), pack2(a1.z, a1.w)};
        __builtin_amdgcn_raw_buffer_store_b128(v, rkv, (u32)((tok * HD + h * 128 + 32 * u + 8 * g) * 2), 0, 16);
      }
    }
  } else {
#pragma unroll
    for (int ip = 0; ip < 2; ++ip) {
      const int sg = mb * 4 + 2 * wr + ip;
#pragma unroll
      for (int j = 0; j < 8; ++j) {
        const int e = 32 * (j >> 1) + p32(j & 1, c);
        f32x4 a0 = acc[2 * ip][j], a1 = acc[2 * ip + 1][j];
        u32x4 v = {pack2(a0.x, a0.y), pack2(a0.z, a0.w), pack2(a1.x, a1.y), pack2(a1.z, a1.w)};
        __builtin_amdgcn_raw_buffer_store_b128(v, rkv, (u32)(SZ_Q + (((sg * 32 + h) * 128 + e) * 32 + 8 * g) * 2), 0, 16);
      }
    }
  }
  asm volatile("s_waitcnt vmcnt(0)" ::: "memory");
  __syncthreads();
  if (tid == 0) __hip_atomic_fetch_add((u32*)(ws + OFF_KVC) + (mb * 32 + h) * 16, 1u, __ATOMIC_RELAXED, __HIP_MEMORY_SCOPE_AGENT);
}

__device__ __forceinline__ void worker_qattn(char* __restrict__ ws, char* smem, const int mb, const int h) {
  const int tid = otid(), w = __builtin_amdgcn_readfirstlane(tid >> 6), l = tid & 63;
  const char* xnf = ws + OFF_XNF;
  const char* wf = ws + OFF_WF;
  const int wr = w >> 1, wc = w & 1;
  const f32x4 z4 = {0, 0, 0, 0};
  const int c = l & 15, g = l >> 4;
  {
    f32x4 acc[4][4];
#pragma unroll
    for (int i = 0; i < 4; ++i)
#pragma unroll
      for (int j = 0; j < 4; ++j) acc[i][j] = z4;
    const char* src[4];
#pragma unroll
    for (int i = 0; i < 4; ++i) {
      const int f = 4 * w + i;
      src[i] = ((f < 8) ? xnf + (size_t)(mb * 8 + f) * 20480 : wf + (size_t)(h * 8 + (f - 8)) * 20480) + l * 16;
    }
    auto stage = [&](int ks) {
#pragma unroll
      for (int i = 0; i < 4; ++i) glds16(src[i] + ks * 1024, smem + (ks % 5) * 16384 + (4 * w + i) * 1024 + l * 16);
    };
#pragma unroll
    for (int ks = 0; ks < 4; ++ks) stage(ks);
#pragma unroll
    for (int ks = 0; ks < 20; ++ks) {
      VMCNT(4 * (ks < 17 ? 3 : 19 - ks));
      RAWBAR();
      if (ks + 4 < 20) stage(ks + 4);
      const char* base = smem + (ks % 5) * 16384 + l * 16;
      bf16x8 a[4], bb[4];
#pragma unroll
      for (int i = 0; i < 4; ++i) a[i] = *(const bf16x8*)(base + (4 * wr + i) * 1024);
#pragma unroll
      for (int j = 0; j < 4; ++j) bb[j] = *(const bf16x8*)(base + (8 + 4 * wc + j) * 1024);
#pragma unroll
      for (int i = 0; i < 4; ++i)
#pragma unroll
        for (int j = 0; j < 4; ++j) acc[i][j] = mfma32(bb[j], a[i], acc[i][j]);
    }
#pragma unroll
    for (int i = 0; i < 4; ++i)
#pragma unroll
      for (int u = 0; u < 2; ++u) {
        f32x4 a0 = acc[i][2 * u], a1 = acc[i][2 * u + 1];
        u32x4 v = {pack2(a0.x, a0.y), pack2(a0.z, a0.w), pack2(a1.x, a1.y), pack2(a1.z, a1.w)};
        *(u32x4*)(smem + 81920 + (((4 * wr + i) * 4 + 2 * wc + u) * 64 + l) * 16) = v;
      }
  }
  __syncthreads();
  const int s = mb * 4 + w;
  wait_count((const u32*)(ws + OFF_KVC) + (mb * 32 + h) * 16, 1u, (u32*)(ws + OFF_ERR));
  const __amdgpu_buffer_rsrc_t rkv = __builtin_amdgcn_make_buffer_rsrc(ws + OFF_K, 0, (u32)(2 * SZ_Q), 0x00020000);
  const __amdgpu_buffer_rsrc_t rkm = __builtin_amdgcn_make_buffer_rsrc(ws + OFF_KM, 0, (u32)(2 * SZ_KM), 0x00020000);
  const __amdgpu_buffer_rsrc_t rof = __builtin_amdgcn_make_buffer_rsrc(ws + OFF_OF, 0, (u32)SZ_Q, 0x00020000);
  const float scale = 0.08838834764831845f;
  f32x4 sM[2], sX[2][2];
#pragma unroll
  for (int T = 0; T < 2; ++T) { sM[T] = z4; sX[0][T] = z4; sX[1][T] = z4; }
  bf16x8 q[4][2];
#pragma unroll
  for (int ks = 0; ks < 4; ++ks) {
    bf16x8 kx[2];
#pragma unroll
    for (int T = 0; T < 2; ++T) {
      q[ks][T] = *(const bf16x8*)(smem + 81920 + (((2 * w + T) * 4 + ks) * 64 + l) * 16);
      kx[T] = as_bf8(__builtin_amdgcn_raw_buffer_load_b128(rkv, (u32)(((32 * s + p32(T, c)) * HD + h * 128 + 32 * ks + 8 * g) * 2), 0, 16));
    }
#pragma unroll
    for (int T = 0; T < 2; ++T) {
      sX[0][T] = mfma32(kx[0], q[ks][T], sX[0][T]);
      sX[1][T] = mfma32(kx[1], q[ks][T], sX[1][T]);
    }
  }
  bf16x8 vxa[4][2];
#pragma unroll
  for (int u = 0; u < 4; ++u)
#pragma unroll
    for (int E = 0; E < 2; ++E)
      vxa[u][E] = as_bf8(__builtin_amdgcn_raw_buffer_load_b128(rkv, (u32)(SZ_Q + (((s * 32 + h) * 128 + 32 * u + p32(E, c)) * 32 + 8 * g) * 2), 0, 16));
  wait_count((const u32*)(ws + OFF_MCNT) + (s * 32 + h) * 16, 4u, (u32*)(ws + OFF_ERR));
  u32x2 vma[4][2];
  {
    u32x4 km[4];
#pragma unroll
    for (int ks = 0; ks < 4; ++ks) km[ks] = __builtin_amdgcn_raw_buffer_load_b128(rkm, (u32)(((s * 8 + (c & 7)) * HD + h * 128 + 32 * ks + 8 * g) * 2), 0, 16);
#pragma unroll
    for (int u = 0; u < 4; ++u)
#pragma unroll
      for (int E = 0; E < 2; ++E)
        vma[u][E] = __builtin_amdgcn_raw_buffer_load_b64(rkm, (u32)(SZ_KM + (((s * 32 + h) * 128 + 32 * u + p32(E, c)) * 8 + 4 * (g & 1)) * 2), 0, 16);
#pragma unroll
    for (int ks = 0; ks < 4; ++ks)
#pragma unroll
      for (int T = 0; T < 2; ++T) sM[T] = mfma32(as_bf8(km[ks]), q[ks][T], sM[T]);
  }
  u32x4 px[2]; u32x2 pm[2];
#pragma unroll
  for (int T = 0; T < 2; ++T) {
    const int qi = p32(T, c);
    float sm_[4], sx[8];
#pragma unroll
    for (int i = 0; i < 4; ++i) {
      sm_[i] = g < 2 ? bfr(bfr(sM[T][i]) * scale) : -INFINITY;
      float a = bfr(bfr(sX[0][T][i]) * scale), b = bfr(bfr(sX[1][T][i]) * scale);
      sx[i] = (8 * g + i <= qi) ? a : -1e9f;
      sx[4 + i] = (8 * g + 4 + i <= qi) ? b : -1e9f;
    }
    float mx = fmaxf(fmaxf(fmaxf(sx[0], sx[1]), fmaxf(sx[2], sx[3])), fmaxf(fmaxf(sx[4], sx[5]), fmaxf(sx[6], sx[7])));
    mx = fmaxf(mx, fmaxf(fmaxf(sm_[0], sm_[1]), fmaxf(sm_[2], sm_[3])));
    mx = xr32_max(xr16_max(mx));
    float sum = 0.f;
#pragma unroll
    for (int i = 0; i < 8; ++i) { sx[i] = __expf(sx[i] - mx); sum += sx[i]; }
#pragma unroll
    for (int i = 0; i < 4; ++i) { sm_[i] = __expf(sm_[i] - mx); sum += sm_[i]; }
    sum = xr32_add(xr16_add(sum));
    const float inv = 1.0f / sum;
    px[T] = (u32x4){pack2(sx[0] * inv, sx[1] * inv), pack2(sx[2] * inv, sx[3] * inv), pack2(sx[4] * inv, sx[5] * inv), pack2(sx[6] * inv, sx[7] * inv)};
    pm[T] = (u32x2){pack2(sm_[0] * inv, sm_[1] * inv), pack2(sm_[2] * inv, sm_[3] * inv)};
  }
#pragma unroll
  for (int u = 0; u < 4; ++u) {
    f32x4 o[2][2];
#pragma unroll
    for (int E = 0; E < 2; ++E) {
      u32x2 vm = vma[u][E];
      if (g >= 2) vm = (u32x2){0, 0};
#pragma unroll
      for (int T = 0; T < 2; ++T) {
        o[E][T] = mfma32(vxa[u][E], as_bf8(px[T]), z4);
        o[E][T] = mfma16(as_bf4(vm), as_bf4(pm[T]), o[E][T]);
      }
    }
#pragma unroll
    for (int T = 0; T < 2; ++T) {
      u32x4 v = {pack2(o[0][T].x, o[0][T].y), pack2(o[0][T].z, o[0][T].w), pack2(o[1][T].x, o[1][T].y), pack2(o[1][T].z, o[1][T].w)};
      __builtin_amdgcn_raw_buffer_store_b128(v, rof, (u32)((((2 * s + T) * 128 + (4 * h + u)) * 64 + l) * 16), 0, 16);
    }
  }
  asm volatile("s_waitcnt vmcnt(0)" ::: "memory");
  if (l == 0) __hip_atomic_fetch_add((u32*)(ws + OFF_OSEG) + s * 16, 1u, __ATOMIC_RELAXED, __HIP_MEMORY_SCOPE_AGENT);
  __syncthreads();
}

__device__ __forceinline__ void final_store2(char* __restrict__ ws, u16* __restrict__ out, const int tok, const int n0, const f32x4 a0, const f32x4 a1,
                                             const u32x4 xv, const u32x4 nv, const int slot) {
  const float* rsel = (const float*)(ws + OFF_SEL) + 8;
  const float* yp = (const float*)(ws + OFF_YP);
  float a[8] = {a0.x, a0.y, a0.z, a0.w, a1.x, a1.y, a1.z, a1.w};
  float xf[8] = {bflo(xv.x), bfhi(xv.x), bflo(xv.y), bfhi(xv.y), bflo(xv.z), bfhi(xv.z), bflo(xv.w), bfhi(xv.w)};
  float nf[8] = {bflo(nv.x), bfhi(nv.x), bflo(nv.y), bfhi(nv.y), bflo(nv.z), bfhi(nv.z), bflo(nv.w), bfhi(nv.w)};
  if (slot >= 0) {
    const float rs = rsel[slot];
#pragma unroll
    for (int j = 0; j < 8; ++j) {
      float y = 0.f;
#pragma unroll
      for (int kb = 0; kb < 16; ++kb) y += yp[((size_t)kb * 8 + slot) * DM + n0 + j];
      y = bfr(bfr(y) * rs);
      nf[j] = bfr(nf[j] + y);
    }
  }
  float o[8];
#pragma unroll
  for (int j = 0; j < 8; ++j) o[j] = bfr(xf[j] + bfr(a[j])) + nf[j];
  u32x4 ov = {pack2(o[0], o[1]), pack2(o[2], o[3]), pack2(o[4], o[5]), pack2(o[6], o[7])};
  *(u32x4*)(out + (size_t)tok * DM + n0) = ov;
}

__device__ __forceinline__ void worker_wo_fine(const u16* __restrict__ x, char* __restrict__ ws, u16* __restrict__ out, char* smem, const int par, const int np,
                                               const int sg0, const int sgstep) {
  const int tid = otid(), w = __builtin_amdgcn_readfirstlane(tid >> 6), l = tid & 63;
  const int c = l & 15, g = l >> 4;
  const char* wof = ws + OFF_WOF + ((size_t)(2 * np) * 128 + 32 * w) * 1024 + l * 16;
  bf16x8 b0[32], b1[32];
  const int rot = (np * 8) / 5;
#pragma unroll
  for (int i = 0; i < 32; ++i) { const int kk = (i + rot) & 31; b0[i] = *(const bf16x8*)(wof + kk * 1024); b1[i] = *(const bf16x8*)(wof + 131072 + kk * 1024); }
  const __amdgpu_buffer_rsrc_t rof = __builtin_amdgcn_make_buffer_rsrc(ws + OFF_OF, 0, (u32)SZ_Q, 0x00020000);
  const u16* xn = (const u16*)(ws + OFF_XN);
  int sel[8];
  if (w == 0) {
    wait_count((const u32*)(ws + OFF_YCNT), 80u, (u32*)(ws + OFF_ERR));
    __builtin_amdgcn_fence(__ATOMIC_ACQUIRE, "agent");
#pragma unroll
    for (int k = 0; k < 8; ++k) sel[k] = __builtin_amdgcn_readfirstlane(((const int*)(ws + OFF_SEL))[k]);
  }
  int it = 0;
  for (int sg = sg0; sg < 64; sg += sgstep, ++it) {
    const int mt = 2 * sg + par;
    const int tok = 32 * sg + p32(par, c), n0 = 32 * np + 8 * g;
    u32x4 xv, nv;
    if (w == 0) { xv = *(const u32x4*)(x + (size_t)tok * DM + n0); nv = *(const u32x4*)(xn + (size_t)tok * DM + n0); }
    if (w == 1) {
      if (sg < 63) wait_count<100>((const u32*)(ws + OFF_OSEG) + sg * 16, 32u, (u32*)(ws + OFF_ERR));
      else wait_count<4>((const u32*)(ws + OFF_OSEG) + sg * 16, 32u, (u32*)(ws + OFF_ERR));
      __builtin_amdgcn_fence(__ATOMIC_ACQUIRE, "agent");
      asm volatile("s_waitcnt vmcnt(0)" ::: "memory");
    }
    __syncthreads();
    f32x4 acc0, acc1;
    const u32 abase = (u32)(((mt * 128 + 32 * w) * 64 + l) * 16);
    u32x4 a[32];
#pragma unroll
    for (int i = 0; i < 32; ++i) {
      a[i] = __builtin_amdgcn_raw_buffer_load_b128(rof, abase, ((i + rot) & 31) * 1024, 0);
    }
    __builtin_amdgcn_sched_barrier(0);
    MFMA_A0(acc0, b0[0], a[0]); MFMA_A0(acc1, b1[0], a[0]);
#pragma unroll
    for (int i = 1; i < 32; ++i) { MFMA_A(acc0, b0[i], a[i]); MFMA_A(acc1, b1[i], a[i]); }
    asm volatile("s_nop 7\n\ts_nop 7" : "+v"(acc0), "+v"(acc1));
    char* sb = smem + (it & 1) * 8192;
    if (w > 0) {
      *(f32x4*)(sb + (w * 2) * 1024 + l * 16) = acc0;
      *(f32x4*)(sb + (w * 2 + 1) * 1024 + l * 16) = acc1;
    }
    __syncthreads();
    if (w == 0) {
      const f32x4* p = (const f32x4*)(sb + l * 16);
      acc0 = (acc0 + p[2 * 64]) + (p[4 * 64] + p[6 * 64]);
      acc1 = (acc1 + p[3 * 64]) + (p[5 * 64] + p[7 * 64]);
      int slot = -1;
#pragma unroll
      for (int k = 0; k < 8; ++k) if (sel[k] == tok) slot = k;
      final_store2(ws, out, tok, n0, acc0, acc1, xv, nv, slot);
    }
  }
}

#define WSTAMP(i)
template <typename T> __device__ __forceinline__ T* opq(T* p) { asm volatile("" : "+s"(p)); return p; }

__global__ __launch_bounds__(256, 1) void main_kernel(const u16* __restrict__ x, const u16* __restrict__ wg, const u16* __restrict__ wu,
                                                      const u16* __restrict__ wd, char* __restrict__ ws, u16* __restrict__ out) {
  extern __shared__ __attribute__((aligned(16))) char smem[];
  const int b = blockIdx.x;
  if (b < 128) {
    chain_role(ws, smem, b);
  } else {
    const int j = b - 128;
    for (int i = 0; i < 4; ++i) {
      worker_kv(opq(ws), smem, 4 * i + (j >> 5), j & 31);
      WSTAMP(i);
      if (i == 0 && j < 108) worker_modA(opq(wg), opq(wu), opq(ws), smem, j);
      if (i == 1 && j < 80) worker_modB(opq(wd), opq(ws), smem, j);
    }
    if (j < 40) {
      worker_wo_fine(opq(x), opq(ws), out, smem, j / 20, j % 20, 0, 1);
    } else {
      for (int a = j - 40; a < 512; a += 88) { worker_qattn(opq(ws), smem, a >> 5, a & 31); WSTAMP(4 + a / 88); }
    }
    WSTAMP(15);
  }
}

extern "C" void kernel_launch(void* const* d_in, const int* in_sizes, int n_in, void* d_out, int out_size, void* d_ws, size_t ws_size,
                              hipStream_t stream) {
  const u16* x = (const u16*)d_in[0];
  const u16* mem0 = (const u16*)d_in[1];
  const u16* Wq = (const u16*)d_in[2];
  const u16* Wk = (const u16*)d_in[3];
  const u16* Wv = (const u16*)d_in[4];
  const u16* Wo = (const u16*)d_in[5];
  const u16* w_r = (const u16*)d_in[8];
  const u16* wg = (const u16*)d_in[9];
  const u16* wu = (const u16*)d_in[10];
  const u16* wd = (const u16*)d_in[11];
  char* ws = (char*)d_ws;
  u16* out = (u16*)d_out;
  (void)hipFuncSetAttribute((const void*)main_kernel, hipFuncAttributeMaxDynamicSharedMemorySize, CH_LDS);
  prep_kernel<<<PREP_BLOCKS, 256, 0, stream>>>(x, mem0, Wq, Wk, Wv, Wo, w_r, ws);
  main_kernel<<<256, 256, CH_LDS, stream>>>(x, wg, wu, wd, ws, out);
}
```

```cpp
#include <hip/hip_runtime.h>
#include <hip/hip_bf16.h>
#include <stdint.h>

constexpr int S_TOK = 2048, DM = 640, NH = 32, DH = 128, HD = 4096, NMEM = 8, SEG = 32, NSEG = 64;
constexpr int FF = 1728, CSEL = 8;
constexpr int KS_DM = DM / 32;
constexpr int KS_HD = HD / 32;

typedef unsigned int u32;
typedef unsigned short u16;
typedef __attribute__((ext_vector_type(4))) u32 u32x4;
typedef __attribute__((ext_vector_type(2))) u32 u32x2;
typedef __attribute__((ext_vector_type(4))) float f32x4;
typedef __attribute__((ext_vector_type(8))) short bf16x8;
typedef __attribute__((ext_vector_type(4))) short bf16x4;
typedef __attribute__((ext_vector_type(2))) __bf16 nbf16x2;

#define SENT 0xFFFFFFFFu

__device__ __forceinline__ float bf2f(u16 v) { return __uint_as_float(((u32)v) << 16); }
__device__ __forceinline__ float bflo(u32 v) { return __uint_as_float(v << 16); }
__device__ __forceinline__ float bfhi(u32 v) { return __uint_as_float(v & 0xffff0000u); }
__device__ __forceinline__ u32 pack2(float a, float b) {
  nbf16x2 v = {(__bf16)a, (__bf16)b};
  return __builtin_bit_cast(u32, v);
}
__device__ __forceinline__ float bfr(float a) { return (float)(__bf16)a; }
__device__ __forceinline__ bf16x8 as_bf8(u32x4 v) { return __builtin_bit_cast(bf16x8, v); }
__device__ __forceinline__ bf16x4 as_bf4(u32x2 v) { return __builtin_bit_cast(bf16x4, v); }
__device__ __forceinline__ f32x4 mfma32(bf16x8 a, bf16x8 b, f32x4 c) { return __builtin_amdgcn_mfma_f32_16x16x32_bf16(a, b, c, 0, 0, 0); }
__device__ __forceinline__ f32x4 mfma16(bf16x4 a, bf16x4 b, f32x4 c) {
  u32x2 a2 = __builtin_bit_cast(u32x2, a), b2 = __builtin_bit_cast(u32x2, b);
  u32x4 a4 = {a2.x, a2.y, 0u, 0u}, b4 = {b2.x, b2.y, 0u, 0u};
  return __builtin_amdgcn_mfma_f32_16x16x32_bf16(__builtin_bit_cast(bf16x8, a4), __builtin_bit_cast(bf16x8, b4), c, 0, 0, 0);
}
__device__ __forceinline__ float xr16_max(float v) { auto r = __builtin_amdgcn_permlane16_swap(__float_as_uint(v), __float_as_uint(v), false, false); return fmaxf(__uint_as_float(r[0]), __uint_as_float(r[1])); }
__device__ __forceinline__ float xr32_max(float v) { auto r = __builtin_amdgcn_permlane32_swap(__float_as_uint(v), __float_as_uint(v), false, false); return fmaxf(__uint_as_float(r[0]), __uint_as_float(r[1])); }
__device__ __forceinline__ float xr16_add(float v) { auto r = __builtin_amdgcn_permlane16_swap(__float_as_uint(v), __float_as_uint(v), false, false); return __uint_as_float(r[0]) + __uint_as_float(r[1]); }
__device__ __forceinline__ float xr32_add(float v) { auto r = __builtin_amdgcn_permlane32_swap(__float_as_uint(v), __float_as_uint(v), false, false); return __uint_as_float(r[0]) + __uint_as_float(r[1]); }
__device__ __forceinline__ float xr8_max(float v) { return fmaxf(v, __uint_as_float(__builtin_amdgcn_update_dpp(0u, __float_as_uint(v), 0x128  , 0xf, 0xf, false))); }
__device__ __forceinline__ float xr8_add(float v) { return v + __uint_as_float(__builtin_amdgcn_update_dpp(0u, __float_as_uint(v), 0x128  , 0xf, 0xf, false)); }
__device__ __forceinline__ int p32(int T, int r) { return 8 * (r >> 2) + 4 * T + (r & 3); }

__host__ __device__ __forceinline__ int piece_col(int p, int t) {
  const int w = p / 20, r = p % 20;
  const int blk = r < 16 ? 2 * w + (r >> 3) : 8 + (w >> 1), sel = r < 16 ? (r >> 2) & 1 : w & 1, g = r & 3;
  return 64 * blk + 16 * g + 4 * (t >> 1) + 2 * sel + (t & 1);
}

constexpr size_t SZ_XN = (size_t)S_TOK * DM * 2;
constexpr size_t OFF_XN = 0;
constexpr size_t OFF_XNF = OFF_XN + SZ_XN;
constexpr size_t OFF_R = OFF_XNF + SZ_XN;
constexpr size_t OFF_WF = OFF_R + 8192;
constexpr size_t SZ_WF = (size_t)768 * 20 * 1024;
constexpr size_t OFF_WOF = OFF_WF + SZ_WF;
constexpr size_t SZ_WOF = (size_t)40 * 128 * 1024;
constexpr size_t OFF_Q = OFF_WOF + SZ_WOF;
constexpr size_t SZ_Q = (size_t)S_TOK * HD * 2;
constexpr size_t OFF_K = OFF_Q + SZ_Q;
constexpr size_t OFF_VT = OFF_K + SZ_Q;
constexpr size_t OFF_KM = OFF_VT + SZ_Q;
constexpr size_t SZ_KM = (size_t)NSEG * NMEM * HD * 2;
constexpr size_t OFF_VMT = OFF_KM + SZ_KM;
constexpr size_t OFF_OF = OFF_VMT + SZ_KM;
constexpr size_t OFF_INBOX = OFF_OF + SZ_Q;
constexpr u32 NPROD = 128, NRED = 80;
constexpr u32 INBOX_SLOT = NRED * NPROD * 128;
constexpr size_t OFF_MEMB = OFF_INBOX + 2 * (size_t)INBOX_SLOT;
constexpr u32 MREP = 8;
constexpr u32 MEM_REP = NRED * 128;
constexpr u32 MEM_STEP = MREP * MEM_REP;
constexpr size_t SZ_MEMB = (size_t)65 * MEM_STEP;
constexpr size_t OFF_HB = OFF_MEMB + ((SZ_MEMB + 255) / 256) * 256;
constexpr size_t OFF_YP = OFF_HB + 32768;
constexpr size_t OFF_SEL = OFF_YP + (size_t)16 * 8 * 640 * 4;
constexpr size_t OFF_ERR = OFF_SEL + 256;
constexpr size_t OFF_MCNT = OFF_ERR + 256;
constexpr size_t OFF_OSEG = OFF_MCNT + (size_t)64 * 32 * 64;
constexpr size_t OFF_KVC = OFF_OSEG + 64 * 64;
constexpr size_t OFF_HCNT = OFF_KVC + (size_t)16 * 32 * 64;
constexpr size_t OFF_YCNT = OFF_HCNT + 256;
constexpr size_t OFF_FLAG_END = OFF_YCNT + 256;
constexpr size_t WS_NEED = OFF_FLAG_END;

constexpr int PREP_LN_BLOCKS = S_TOK / 4;
constexpr int PREP_WQKV_BLOCKS = 3 * 20 * 16;
constexpr int PREP_WO_BLOCKS = 128 * 3;
constexpr int PREP_INIT_BLOCKS = 64;
constexpr int PREP_BLOCKS = PREP_LN_BLOCKS + PREP_WQKV_BLOCKS + PREP_WO_BLOCKS + PREP_INIT_BLOCKS;

__device__ __forceinline__ void tile_weights(const u16* __restrict__ W, int N, int k0, int n0, int ncols, u16* __restrict__ dst,
                                             int nks_total, int ks, int nt0, u16* lds) {
  const int tid = threadIdx.x;
  const int chunks_per_row = ncols / 8;
  for (int i = tid; i < 32 * chunks_per_row; i += 256) {
    int row = i / chunks_per_row, ch = i % chunks_per_row;
    u32x4 v = *(const u32x4*)(W + (size_t)(k0 + row) * N + n0 + ch * 8);
    *(u32x4*)(lds + row * 264 + ch * 8) = v;
  }
  __syncthreads();
  const int ntiles = ncols / 16;
  for (int o = tid; o < ntiles * 64; o += 256) {
    int t = o >> 6, l = o & 63, r = l & 15, g = l >> 4;
    int col = 32 * (t >> 1) + p32(t & 1, r);
    u16 e[8];
#pragma unroll
    for (int j = 0; j < 8; ++j) e[j] = lds[(8 * g + j) * 264 + col];
    u32x4 v = {(u32)e[0] | ((u32)e[1] << 16), (u32)e[2] | ((u32)e[3] << 16), (u32)e[4] | ((u32)e[5] << 16), (u32)e[6] | ((u32)e[7] << 16)};
    *(u32x4*)(dst + (((size_t)(nt0 + t) * nks_total + ks) * 64 + l) * 8) = v;
  }
}

__global__ __launch_bounds__(256) void prep_kernel(const u16* __restrict__ x, const u16* __restrict__ mem0, const u16* __restrict__ Wq,
                                                   const u16* __restrict__ Wk, const u16* __restrict__ Wv, const u16* __restrict__ Wo,
                                                   const u16* __restrict__ w_r, char* __restrict__ ws) {
  __shared__ __attribute__((aligned(16))) u16 lds[32 * 264];
  const int b = blockIdx.x, tid = threadIdx.x;
  if (b < PREP_LN_BLOCKS) {
    const int w = tid >> 6, l = tid & 63;
    const int t = b * 4 + w;
    const u16* xr = x + (size_t)t * DM;
    u32x4 c0 = *(const u32x4*)(xr + l * 8);
    u32x4 c1 = {0, 0, 0, 0};
    if (l < 16) c1 = *(const u32x4*)(xr + 512 + l * 8);
    float v[16];
    v[0] = bflo(c0.x); v[1] = bfhi(c0.x); v[2] = bflo(c0.y); v[3] = bfhi(c0.y); v[4] = bflo(c0.z); v[5] = bfhi(c0.z); v[6] = bflo(c0.w); v[7] = bfhi(c0.w);
    v[8] = bflo(c1.x); v[9] = bfhi(c1.x); v[10] = bflo(c1.y); v[11] = bfhi(c1.y); v[12] = bflo(c1.z); v[13] = bfhi(c1.z); v[14] = bflo(c1.w); v[15] = bfhi(c1.w);
    float s = 0.f;
#pragma unroll
    for (int j = 0; j < 16; ++j) s += v[j];
#pragma unroll
    for (int o = 32; o > 0; o >>= 1) s += __shfl_xor(s, o);
    const float mean = s * (1.0f / DM);
    float q = 0.f;
#pragma unroll
    for (int j = 0; j < 8; ++j) { float d = v[j] - mean; q += d * d; }
    if (l < 16) {
#pragma unroll
      for (int j = 8; j < 16; ++j) { float d = v[j] - mean; q += d * d; }
    }
#pragma unroll
    for (int o = 32; o > 0; o >>= 1) q += __shfl_xor(q, o);
    const float var_b = bfr(q * (1.0f / DM));
    const float mean_b = bfr(mean);
    const float den = bfr(sqrtf(bfr(var_b + 1e-05f)));
    float y[16];
#pragma unroll
    for (int j = 0; j < 16; ++j) y[j] = bfr(bfr(v[j] - mean_b) / den);
    u32x4 w0 = *(const u32x4*)(w_r + l * 8);
    u32x4 w1 = {0, 0, 0, 0};
    if (l < 16) w1 = *(const u32x4*)(w_r + 512 + l * 8);
    float rs = y[0] * bflo(w0.x) + y[1] * bfhi(w0.x) + y[2] * bflo(w0.y) + y[3] * bfhi(w0.y) + y[4] * bflo(w0.z) + y[5] * bfhi(w0.z) + y[6] * bflo(w0.w) + y[7] * bfhi(w0.w);
    if (l < 16) rs += y[8] * bflo(w1.x) + y[9] * bfhi(w1.x) + y[10] * bflo(w1.y) + y[11] * bfhi(w1.y) + y[12] * bflo(w1.z) + y[13] * bfhi(w1.z) + y[14] * bflo(w1.w) + y[15] * bfhi(w1.w);
#pragma unroll
    for (int o = 32; o > 0; o >>= 1) rs += __shfl_xor(rs, o);
    if (l == 0) ((float*)(ws + OFF_R))[t] = bfr(rs);
    u16* xn = (u16*)(ws + OFF_XN);
    u16* xnf = (u16*)(ws + OFF_XNF);
    const int sg = t >> 5, tt = t & 31;
    const int mt = 2 * sg + ((tt >> 2) & 1), r = 4 * (tt >> 3) + (tt & 3);
    u32x4 o0 = {pack2(y[0], y[1]), pack2(y[2], y[3]), pack2(y[4], y[5]), pack2(y[6], y[7])};
    *(u32x4*)(xn + (size_t)t * DM + l * 8) = o0;
    { int c = l; *(u32x4*)(xnf + (((size_t)mt * 20 + (c >> 2)) * 64 + (c & 3) * 16 + r) * 8) = o0; }
    if (l < 16) {
      u32x4 o1 = {pack2(y[8], y[9]), pack2(y[10], y[11]), pack2(y[12], y[13]), pack2(y[14], y[15])};
      *(u32x4*)(xn + (size_t)t * DM + 512 + l * 8) = o1;
      int c = 64 + l;
      *(u32x4*)(xnf + (((size_t)mt * 20 + (c >> 2)) * 64 + (c & 3) * 16 + r) * 8) = o1;
    }
  } else if (b < PREP_LN_BLOCKS + PREP_WQKV_BLOCKS) {
    int i = b - PREP_LN_BLOCKS;
    int mat = i / 320, rem = i % 320, ks = rem / 16, nb = rem % 16;
    const u16* W = mat == 0 ? Wq : (mat == 1 ? Wk : Wv);
    tile_weights(W, HD, ks * 32, nb * 256, 256, (u16*)(ws + OFF_WF), 20, ks, mat * 256 + nb * 16, lds);
  } else if (b < PREP_LN_BLOCKS + PREP_WQKV_BLOCKS + PREP_WO_BLOCKS) {
    int i = b - PREP_LN_BLOCKS - PREP_WQKV_BLOCKS;
    int ks = i / 3, nb = i % 3;
    tile_weights(Wo, DM, ks * 32, nb * 256, nb == 2 ? 128 : 256, (u16*)(ws + OFF_WOF), 128, ks, nb * 16, lds);
  } else {
    int i = b - (PREP_LN_BLOCKS + PREP_WQKV_BLOCKS + PREP_WO_BLOCKS);
    u32x4 sv = {SENT, SENT, SENT, SENT};
    u32x4* p = (u32x4*)(ws + OFF_INBOX);
    const size_t n16 = (2 * (size_t)INBOX_SLOT) / 16;
    for (size_t j = (size_t)i * 256 + tid; j < n16; j += (size_t)PREP_INIT_BLOCKS * 256) p[j] = sv;
    u32x4* pm = (u32x4*)(ws + OFF_MEMB + MEM_STEP);
    const size_t m16 = (size_t)64 * MEM_STEP / 16;
    for (size_t j = (size_t)i * 256 + tid; j < m16; j += (size_t)PREP_INIT_BLOCKS * 256) pm[j] = sv;
    if (i == 0) {
      for (int j = tid; j < 640; j += 256) {
        int p_ = j >> 3, m = j & 7;
        u32x4 v = {*(const u32*)(mem0 + m * DM + piece_col(p_, 0)), *(const u32*)(mem0 + m * DM + piece_col(p_, 2)),
                   *(const u32*)(mem0 + m * DM + piece_col(p_, 4)), *(const u32*)(mem0 + m * DM + piece_col(p_, 6))};
        if (v.x == SENT) v.x = 0xFFC0FFC0u;
        if (v.y == SENT) v.y = 0xFFC0FFC0u;
        if (v.z == SENT) v.z = 0xFFC0FFC0u;
        if (v.w == SENT) v.w = 0xFFC0FFC0u;
        for (u32 rp = 0; rp < MREP; ++rp) *(u32x4*)(ws + OFF_MEMB + rp * MEM_REP + (size_t)j * 16) = v;
      }
      if (tid == 0) *(u32*)(ws + OFF_ERR) = 0;
    }
    if (i == 1) {
      u32x4* pf = (u32x4*)(ws + OFF_MCNT);
      for (int j = tid; j < (int)((OFF_FLAG_END - OFF_MCNT) / 16); j += 256) pf[j] = (u32x4){0, 0, 0, 0};
    }
  }
}

__device__ __forceinline__ void glds16(const void* g, void* l) {
  __builtin_amdgcn_global_load_lds((const __attribute__((address_space(1))) void*)g, (__attribute__((address_space(3))) void*)l, 16, 0, 0);
}

constexpr u32 SPIN_CAP = 1u << 16;
#define WO_SC1
#define SLP_NR 40
#define SLP_RI 6
#define SLP_D1 10
#define SLP_D2 10
#define WO_SLP 40
#define WO_FAST 63
#define SLP_RM 8
__device__ __forceinline__ bool has_sent(u32x4 v) { return (v.x == SENT) | (v.y == SENT) | (v.z == SENT) | (v.w == SENT); }
__device__ __forceinline__ u32 desent(float f) { u32 b = __float_as_uint(f); return b == SENT ? 0x7FC00000u : b; }

template <int SLP = 8>
__device__ __forceinline__ void wait_count(const u32* p, u32 want, u32* err) {
  u32 spins = 0;
  for (;;) {
    u32 v = __builtin_amdgcn_readfirstlane(__hip_atomic_load(p, __ATOMIC_RELAXED, __HIP_MEMORY_SCOPE_AGENT));
    if (v >= want) break;
    __builtin_amdgcn_s_sleep(SLP);
    if (++spins > (1u << 20)) { *err = 3; break; }
  }
}

__device__ __forceinline__ u32x4 mk_rsrc(const void* p, u32 n) {
  const unsigned long long a = (unsigned long long)p;
  return (u32x4){(u32)__builtin_amdgcn_readfirstlane((u32)a), (u32)__builtin_amdgcn_readfirstlane((u32)(a >> 32) & 0xffffu), n, 0x00020000u};
}

constexpr int CH_MEMF = 0, CH_PART = 20480, CH_WV = 40960, CH_WO = 81920, CH_SRED = 122880, CH_LDS = 123904;

#define MFMA_A(acc, wa, mb) asm volatile("v_mfma_f32_16x16x32_bf16 %0, %1, %2, %0" : "+v"(acc) : "a"(wa), "v"(mb))
#define MFMA_V(acc, wa, mb) asm volatile("v_mfma_f32_16x16x32_bf16 %0, %1, %2, %0" : "+v"(acc) : "v"(wa), "v"(mb))
#define MFMA_A0(acc, wa, mb) asm volatile("v_mfma_f32_16x16x32_bf16 %0, %1, %2, 0" : "=&v"(acc) : "a"(wa), "v"(mb))
#define MFMA_V0(acc, wa, mb) asm volatile("v_mfma_f32_16x16x32_bf16 %0, %1, %2, 0" : "=&v"(acc) : "v"(wa), "v"(mb))
#define MFMA_NV(acc, wa, mb) asm volatile("s_nop 1\n\tv_mfma_f32_16x16x32_bf16 %0, %1, %2, %0" : "+v"(acc) : "v"(wa), "v"(mb))
#define MFMA_NV0(acc, wa, mb) asm volatile("s_nop 1\n\tv_mfma_f32_16x16x32_bf16 %0, %1, %2, 0" : "=&v"(acc) : "v"(wa), "v"(mb))

#define SMF_A(acc, a, b) asm volatile("v_smfmac_f32_16x16x64_bf16 %0, %1, %2, %3" : "+v"(acc) : "v"(a), "a"(b), "v"(sidx))
#define SMF_V(acc, a, b) asm volatile("v_smfmac_f32_16x16x64_bf16 %0, %1, %2, %3" : "+v"(acc) : "v"(a), "v"(b), "v"(sidx))
#define MFMA16_N0(acc, a, b) asm volatile("s_nop 1\n\tv_mfma_f32_16x16x16_bf16 %0, %1, %2, 0" : "=&v"(acc) : "v"(a), "v"(b))
#define SMF_P(R, C, acc, a, b) asm volatile("v_smfmac_f32_16x16x64_bf16 %0, %1, %2, %3" : "+{" R "}"(acc) : "v"(a), C(b), "v"(sidx))
#define SQ0(C, a, b) SMF_P("v[208:211]", C, aq0, a, b)
#define SQ1(C, a, b) SMF_P("v[212:215]", C, aq1, a, b)
#define SK0(C, a, b) SMF_P("v[216:219]", C, ak0, a, b)
#define SK1(C, a, b) SMF_P("v[220:223]", C, ak1, a, b)
typedef __attribute__((ext_vector_type(8))) u32 u32x8;
typedef __attribute__((ext_vector_type(2))) float f32x2;
constexpr int PSTR = 4160;
__device__ __forceinline__ float hsum32(float a, float b) { auto r = __builtin_amdgcn_permlane32_swap(__float_as_uint(a), __float_as_uint(b), false, false); return __uint_as_float(r[0]) + __uint_as_float(r[1]); }
__device__ __forceinline__ u32 ror8(u32 v) { return (u32)__builtin_amdgcn_update_dpp(0u, v, 0x128  , 0xf, 0xf, false); }

#define STAMP(i)
#define CST(i)
__device__ __forceinline__ void chain_role(char* __restrict__ ws, char* smem, const int c) {
  const int tid = threadIdx.x, w = __builtin_amdgcn_readfirstlane(tid >> 6), l = tid & 63;
  const int h = c >> 2, gs = c & 3;
  const int m = l & 15, g = l >> 4;
  const char* wf = ws + OFF_WF;
  const char* wof = ws + OFF_WOF;
  auto ldblk = [&](int nt, int blk) -> u32x8 {
    const u32x4 lo = *(const u32x4*)(wf + (((size_t)nt * 20 + 2 * blk) * 64 + l) * 16), hi = *(const u32x4*)(wf + (((size_t)nt * 20 + 2 * blk + 1) * 64 + l) * 16);
    return (u32x8){lo.x, lo.y, lo.z, lo.w, hi.x, hi.y, hi.z, hi.w};
  };
  u32x8 wq[2][10], wk[2][10];
#pragma unroll
  for (int T = 0; T < 2; ++T)
#pragma unroll
    for (int i = 0; i < 10; ++i) {
      const int blk = i < 8 ? 2 * ((w + (i >> 1)) & 3) + (i & 1) : i;
      wq[T][i] = ldblk(2 * (4 * h + w) + T, blk);
      wk[T][i] = ldblk(256 + 2 * (4 * h + w) + T, blk);
    }
  for (int i = tid; i < 40 * 64; i += 256) {
    const int f = i >> 6, ll = i & 63;
    *(u32x4*)(smem + CH_WV + i * 16) = *(const u32x4*)(wf + (((size_t)(512 + 2 * (4 * h + gs) + f / 20) * 20 + f % 20) * 64 + ll) * 16);
  }
  for (int i = tid; i < 40 * 64; i += 256) {
    const int f = i >> 6, ll = i & 63, cc = ll & 15, gb = ll >> 4, w2 = f / 10, u = (f % 10) >> 1, e0 = 16 * (f & 1) + 4 * gb;
    u32x2 xx[2];
#pragma unroll
    for (int hb = 0; hb < 2; ++hb) {
      const int n = u < 4 ? piece_col(20 * w2 + 8 * (u >> 1) + 4 * hb + (cc >> 2), 4 * (u & 1) + (cc & 3)) : piece_col(20 * w2 + 16 + 2 * hb + (cc >> 3), cc & 7);
      const int nt = 2 * (n >> 5) + ((n >> 2) & 1), r2 = 4 * ((n & 31) >> 3) + (n & 3);
      xx[hb] = *(const u32x2*)(wof + (((size_t)nt * 128 + (4 * h + gs)) * 64 + r2 + 16 * (e0 >> 3)) * 16 + (e0 & 7) * 2);
    }
    *(u32x4*)(smem + CH_WO + i * 16) = (u32x4){xx[0].x, xx[1].x, xx[0].y, xx[1].y};
  }
  if (tid < 4) *(f32x4*)(smem + CH_PART + tid * PSTR + 4096) = (f32x4){-INFINITY, -INFINITY, -INFINITY, -INFINITY};
  __syncthreads();
  u32 sidx2 = (w & 1) ? 0xEEEEu : 0x4444u;
  u32 sidx = m < 8 ? 0x4444u : 0xEEEEu;
  asm volatile("" : "+v"(sidx), "+v"(sidx2));
  const u32 one_at = (m & 1) ? 0x3f800000u : 0x3f80u;
  const u32x2 idn = {(g == (m >> 2) && !(m & 2)) ? one_at : 0u, (g == (m >> 2) && (m & 2)) ? one_at : 0u};

  const __amdgpu_buffer_rsrc_t rin = __builtin_amdgcn_make_buffer_rsrc(ws + OFF_INBOX, 0, 2 * INBOX_SLOT, 0x00020000);
  const __amdgpu_buffer_rsrc_t rmem = __builtin_amdgcn_make_buffer_rsrc(ws + OFF_MEMB, 0, (u32)SZ_MEMB, 0x00020000);
  const u32x4 din = mk_rsrc(ws + OFF_INBOX, 2 * INBOX_SLOT), dmem = mk_rsrc(ws + OFF_MEMB, (u32)SZ_MEMB), dkv = mk_rsrc(ws + OFF_K, (u32)(2 * SZ_Q));
  const u32x4 dflag = mk_rsrc(ws + OFF_KVC, 16 * 32 * 64);
  const __amdgpu_buffer_rsrc_t rkm = __builtin_amdgcn_make_buffer_rsrc(ws + OFF_KM, 0, (u32)(2 * SZ_KM), 0x00020000);
  u32* mcnt = (u32*)(ws + OFF_MCNT);
  u32* err = (u32*)(ws + OFF_ERR);
  const float scale = 0.08838834764831845f;
  const char* lmem = smem + CH_MEMF + l * 16;

  u32x4 qt[4], mfo[3];
  u32 qf = 0;
#pragma unroll
  for (int i = 0; i < 4; ++i) qt[i] = (u32x4){0, 0, 0, 0};
#pragma unroll
  for (int i = 0; i < 3; ++i) mfo[i] = (u32x4){0, 0, 0, 0};
  u32x4 kv4[4];
  const u32 kvo0 = (u32)((p32(0, m) * HD + h * 128 + 32 * w + 8 * g) * 2), kvo2 = (u32)(SZ_Q + ((h * 128 + 32 * gs + p32(0, m)) * 32 + 8 * g) * 2);
  auto load_kv = [&](int sn) {
    const u32 so = (u32)sn * (32 * HD * 2);
    asm volatile("buffer_load_dwordx4 v[192:195], %[o0], %[rs], %[so] offen sc1\n\t"
                 "buffer_load_dwordx4 v[196:199], %[o0], %[rs], %[s1] offen sc1\n\t"
                 "buffer_load_dwordx4 v[200:203], %[o2], %[rs], %[so] offen sc1\n\t"
                 "buffer_load_dwordx4 v[204:207], %[o2], %[rs], %[so] offen offset:256 sc1"
                 : "=&{v[192:195]}"(kv4[0]), "=&{v[196:199]}"(kv4[1]), "=&{v[200:203]}"(kv4[2]), "=&{v[204:207]}"(kv4[3])
                 : [o0] "v"(kvo0), [o2] "v"(kvo2), [rs] "s"(dkv), [so] "s"(so), [s1] "s"(so + 4 * HD * 2)
                 : "memory");
  };
  const __amdgpu_buffer_rsrc_t rmc = __builtin_amdgcn_make_buffer_rsrc(ws + OFF_MCNT, 0, 64 * 32 * 64, 0x00020000);
  const u32 mco = tid == 0 ? (u32)h * 64 : 0x80000000u;
  const u32 rso = c < (int)NRED ? (u32)(c * NPROD * 128 + w * 4096 + l * 16) : 0x80000000u;
  wait_count((const u32*)(ws + OFF_KVC) + h * 16, 1u, err);
  load_kv(0);
  for (int s = 0; s < NSEG; ++s) {
    u32x8 wv[3][2];
#pragma unroll
    for (int j = 0; j < 3; ++j)
#pragma unroll
      for (int T = 0; T < 2; ++T) {
        const char* pw = smem + CH_WV + (T * 20 + 2 * (j < 2 ? 2 * w + j : 8 + (w >> 1))) * 1024 + l * 16;
        const u32x4 lo = *(const u32x4*)(pw), hi = *(const u32x4*)(pw + 1024);
        wv[j][T] = (u32x8){lo.x, lo.y, lo.z, lo.w, hi.x, hi.y, hi.z, hi.w};
      }
    f32x4 aq0, aq1, ak0, ak1, av0, av1;
    asm volatile("v_mov_b64 v[164:165], 0\n\tv_mov_b64 v[166:167], 0\n\tv_mov_b64 v[168:169], 0\n\tv_mov_b64 v[170:171], 0\n\t"
                 "v_mov_b64 v[208:209], 0\n\tv_mov_b64 v[210:211], 0\n\tv_mov_b64 v[212:213], 0\n\tv_mov_b64 v[214:215], 0\n\t"
                 "v_mov_b64 v[216:217], 0\n\tv_mov_b64 v[218:219], 0\n\tv_mov_b64 v[220:221], 0\n\tv_mov_b64 v[222:223], 0"
                 : "={v[208:211]}"(aq0), "={v[212:215]}"(aq1), "={v[216:219]}"(ak0), "={v[220:223]}"(ak1), "={v[164:167]}"(av0), "={v[168:171]}"(av1));
    {
      const u32 o = s * MEM_STEP + (c & (MREP - 1)) * MEM_REP + w * 2560 + (m >> 3) * 512 + g * 128 + (m & 7) * 16;
      const u32 o2 = m < 8 ? o : 0x80000000u;
      const u32 la = (u32)(uintptr_t)(__attribute__((address_space(3))) char*)smem + CH_MEMF + w * 2048 + l * 16;
      const u32 la2 = (u32)(uintptr_t)(__attribute__((address_space(3))) char*)smem + CH_MEMF + (m < 8 ? (8 + (w >> 1)) * 1024 + (g * 16 + m + 8 * (w & 1)) * 16 : 10240 + tid * 16);
      u32 tmx, cnt;
      unsigned long long m0, m1, m2, mo;
      const u32 fo = (u32)((min(s + 1, NSEG - 1) >> 2) * 32 + h) * 64;
      asm volatile(
          "buffer_load_dwordx4 v[240:243], %[o], %[rs], 0 offen sc1\n\t"
          "buffer_load_dwordx4 v[244:247], %[o], %[rs], 0 offen offset:1024 sc1\n\t"
          "buffer_load_dwordx4 v[248:251], %[o2], %[rs], 0 offen offset:2048 sc1\n\t"
          "buffer_load_dword v252, off, %[rf], %[fo] sc1\n\t"
          "s_mov_b32 %[cnt], 0\n\t"
          "s_mov_b64 %[m0], -1\n\t"
          "s_mov_b64 %[m1], -1\n\t"
          "s_mov_b64 %[m2], -1\n\t"
          "s_sleep %[slp]\n"
          "1:\n\t"
          "s_mov_b64 exec, %[m0]\n\t"
          "buffer_load_dwordx4 v[240:243], %[o], %[rs], 0 offen sc1\n\t"
          "s_mov_b64 exec, %[m1]\n\t"
          "buffer_load_dwordx4 v[244:247], %[o], %[rs], 0 offen offset:1024 sc1\n\t"
          "s_mov_b64 exec, %[m2]\n\t"
          "buffer_load_dwordx4 v[248:251], %[o2], %[rs], 0 offen offset:2048 sc1\n\t"
          "s_mov_b64 exec, -1\n\t"
          "buffer_load_dword v252, off, %[rf], %[fo] sc1\n\t"
          "s_waitcnt vmcnt(4)\n\t"
          "v_max3_u32 %[t], v240, v241, v242\n\t"
          "v_max_u32 %[t], %[t], v243\n\t"
          "v_cmp_eq_u32 %[m0], -1, %[t]\n\t"
          "v_max3_u32 %[t], v244, v245, v246\n\t"
          "v_max_u32 %[t], %[t], v247\n\t"
          "v_cmp_eq_u32 %[m1], -1, %[t]\n\t"
          "v_max3_u32 %[t], v248, v249, v250\n\t"
          "v_max_u32 %[t], %[t], v251\n\t"
          "v_cmp_eq_u32 %[m2], -1, %[t]\n\t"
          "v_cmp_eq_u32 vcc, 0, v252\n\t"
          "s_or_b64 %[mo], %[m0], %[m1]\n\t"
          "s_or_b64 vcc, vcc, %[m2]\n\t"
          "s_or_b64 %[mo], %[mo], vcc\n\t"
          "s_cbranch_scc0 2f\n\t"
          "s_add_u32 %[cnt], %[cnt], 1\n\t"
          "s_cmp_lt_u32 %[cnt], 0x20000\n\t"
          "s_cbranch_scc1 1b\n"
          "2:\n\t"
          "ds_write_b128 %[la], v[240:243] offset:0\n\t"
          "ds_write_b128 %[la], v[244:247] offset:1024\n\t"
          "ds_write_b128 %[la2], v[248:251]"
          : "+{v[240:243]}"(mfo[0]), "+{v[244:247]}"(mfo[1]), "+{v[248:251]}"(mfo[2]), "+{v252}"(qf), [t] "=&v"(tmx), [cnt] "=&s"(cnt),
            [m0] "=&s"(m0), [m1] "=&s"(m1), [m2] "=&s"(m2), [mo] "=&s"(mo),
            "+{v[192:195]}"(kv4[0]), "+{v[196:199]}"(kv4[1]), "+{v[200:203]}"(kv4[2]), "+{v[204:207]}"(kv4[3])
          : [o] "v"(o), [o2] "v"(o2), [rs] "s"(dmem), [la] "v"(la), [la2] "v"(la2), [slp] "i"(SLP_D2), [rf] "s"(dflag), [fo] "s"(fo)
          : "vcc", "scc", "memory");
      if (cnt >= 0x20000u && l == 0) *err = 1;
    }
    STAMP(0);
    u32x8 wo[5];
    u32x4 kp;
    u32x2 vm0, vm1;
    auto ldwo = [&](int u) {
      const u32x4 lo = *(const u32x4*)(smem + CH_WO + (10 * w + 2 * u) * 1024 + l * 16), hi = *(const u32x4*)(smem + CH_WO + (10 * w + 2 * u + 1) * 1024 + l * 16);
      wo[u] = (u32x8){lo.x, lo.y, lo.z, lo.w, hi.x, hi.y, hi.z, hi.w};
    };
    {
      u32x4 mf[8];
      SMF_P("v[164:167]", "v", av0, mfo[0], wv[0][0]); SMF_P("v[168:171]", "v", av1, mfo[0], wv[0][1]);
      SMF_P("v[164:167]", "v", av0, mfo[1], wv[1][0]); SMF_P("v[168:171]", "v", av1, mfo[1], wv[1][1]);
      { const u32 sidx = sidx2; SMF_P("v[164:167]", "v", av0, mfo[2], wv[2][0]); SMF_P("v[168:171]", "v", av1, mfo[2], wv[2][1]); }
      asm volatile("s_waitcnt lgkmcnt(0)" ::: "memory");
      __syncthreads();
      STAMP(1);
      CST(0);
#pragma unroll
      for (int i = 0; i < 3; ++i) {
        const char* lc = lmem + ((w + 1 + i) & 3) * 2048;
        mf[2 * i] = *(const u32x4*)(lc); mf[2 * i + 1] = *(const u32x4*)(lc + 1024);
      }
      mf[6] = *(const u32x4*)(lmem + 8 * 1024);
      mf[7] = *(const u32x4*)(lmem + 9 * 1024);
      __builtin_amdgcn_sched_barrier(0);
      SQ0("a", mfo[0], wq[0][0]); SQ1("a", mfo[0], wq[1][0]); SK0("a", mfo[0], wk[0][0]); SK1("a", mfo[0], wk[1][0]);
      const u32 pp = (u32)(uintptr_t)(__attribute__((address_space(3))) char*)smem + CH_PART + w * PSTR + l * 16;
      asm volatile(
          "v_smfmac_f32_16x16x64_bf16 v[208:211], %[m], %[b0], %[ix]\n\t"
          "v_permlane32_swap_b32 v164, v168\n\t"
          "v_permlane32_swap_b32 v165, v169\n\t"
          "v_smfmac_f32_16x16x64_bf16 v[212:215], %[m], %[b1], %[ix]\n\t"
          "v_permlane32_swap_b32 v166, v170\n\t"
          "v_permlane32_swap_b32 v167, v171\n\t"
          "v_smfmac_f32_16x16x64_bf16 v[216:219], %[m], %[b2], %[ix]\n\t"
          "v_add_f32 v164, v164, v168\n\t"
          "v_add_f32 v165, v165, v169\n\t"
          "v_smfmac_f32_16x16x64_bf16 v[220:223], %[m], %[b3], %[ix]\n\t"
          "v_add_f32 v166, v166, v170\n\t"
          "v_add_f32 v167, v167, v171\n\t"
          "ds_write_b128 %[pp], v[164:167] offset:3072"
          : "+{v[208:211]}"(aq0), "+{v[212:215]}"(aq1), "+{v[216:219]}"(ak0), "+{v[220:223]}"(ak1), "+{v[164:167]}"(av0), "+{v[168:171]}"(av1)
          : [m] "v"(mfo[1]), [b0] "a"(wq[0][1]), [b1] "a"(wq[1][1]), [b2] "a"(wk[0][1]), [b3] "a"(wk[1][1]), [ix] "v"(sidx), [pp] "v"(pp)
          : "memory");
      (void)__builtin_amdgcn_raw_ptr_buffer_atomic_add_i32(1, rmc, s > 0 ? mco : 0x80000000u, (u32)(s - 1) * (32 * 64), 16);
      __builtin_amdgcn_sched_barrier(0);
      {
        u32x4 sv = {SENT, SENT, SENT, SENT};
        asm volatile("" : "+v"(sv));
        const u32 ro = s > 0 ? rso : 0x80000000u;
        const u32 rb = ((s - 1) & 1) * INBOX_SLOT;
#pragma unroll
        for (int i = 0; i < 4; ++i) {
          SQ0("a", mf[i], wq[0][2 + i]); SQ1("a", mf[i], wq[1][2 + i]); SK0("a", mf[i], wk[0][2 + i]); SK1("a", mf[i], wk[1][2 + i]);
          __builtin_amdgcn_raw_buffer_store_b128(sv, rin, ro, rb + i * 1024, 16);
          if (i < 3) ldwo(i);
          __builtin_amdgcn_sched_barrier(0);
        }
      }
      CST(1);
      SQ0("a", mf[4], wq[0][6]); SQ1("a", mf[4], wq[1][6]); SQ0("a", mf[5], wq[0][7]); SQ1("a", mf[5], wq[1][7]);
      SQ0("v", mf[6], wq[0][8]); SQ1("v", mf[6], wq[1][8]); SQ0("v", mf[7], wq[0][9]); SQ1("v", mf[7], wq[1][9]);
      SK0("a", mf[4], wk[0][6]); SK1("a", mf[4], wk[1][6]);
      const u32 px = m < 8 ? pp + 1024 : (u32)(uintptr_t)(__attribute__((address_space(3))) char*)smem + CH_MEMF + 10240 + tid * 16;
      asm volatile(
          "v_smfmac_f32_16x16x64_bf16 v[216:219], %[m5], %[k05], %[ix]\n\t"
          "v_permlane32_swap_b32 v208, v212\n\t"
          "v_permlane32_swap_b32 v209, v213\n\t"
          "v_smfmac_f32_16x16x64_bf16 v[220:223], %[m5], %[k15], %[ix]\n\t"
          "v_permlane32_swap_b32 v210, v214\n\t"
          "v_permlane32_swap_b32 v211, v215\n\t"
          "v_smfmac_f32_16x16x64_bf16 v[216:219], %[m6], %[k06], %[ix]\n\t"
          "v_add_f32 v208, v208, v212\n\t"
          "v_add_f32 v209, v209, v213\n\t"
          "v_smfmac_f32_16x16x64_bf16 v[220:223], %[m6], %[k16], %[ix]\n\t"
          "v_add_f32 v210, v210, v214\n\t"
          "v_add_f32 v211, v211, v215\n\t"
          "v_smfmac_f32_16x16x64_bf16 v[216:219], %[m7], %[k07], %[ix]\n\t"
          "v_cvt_pk_bf16_f32 v208, v208, v209\n\t"
          "v_cvt_pk_bf16_f32 v209, v210, v211\n\t"
          "v_smfmac_f32_16x16x64_bf16 v[220:223], %[m7], %[k17], %[ix]\n\t"
          "s_nop 0\n\t"
          "v_mfma_f32_16x16x16_bf16 v[212:215], v[208:209], %[idn], 0\n\t"
          "s_nop 5\n\t"
          "v_permlane32_swap_b32 v216, v220\n\t"
          "v_permlane32_swap_b32 v217, v221\n\t"
          "v_permlane32_swap_b32 v218, v222\n\t"
          "v_permlane32_swap_b32 v219, v223\n\t"
          "v_cvt_pk_bf16_f32 v184, v212, v213\n\t"
          "v_cvt_pk_bf16_f32 v185, v214, v215\n\t"
          "v_add_f32 v216, v216, v220\n\t"
          "v_add_f32 v217, v217, v221\n\t"
          "v_mov_b32_dpp v186, v184 row_ror:8 row_mask:0xf bank_mask:0xf\n\t"
          "v_mov_b32_dpp v187, v185 row_ror:8 row_mask:0xf bank_mask:0xf\n\t"
          "v_add_f32 v218, v218, v222\n\t"
          "v_add_f32 v219, v219, v223\n\t"
          "v_mfma_f32_16x16x32_bf16 v[172:175], v[192:195], v[184:187], 0\n\t"
          "v_cvt_pk_bf16_f32 v216, v216, v217\n\t"
          "v_cvt_pk_bf16_f32 v217, v218, v219\n\t"
          "v_mfma_f32_16x16x32_bf16 v[176:179], v[196:199], v[184:187], 0\n\t"
          "s_nop 0\n\t"
          "v_mfma_f32_16x16x16_bf16 v[220:223], v[216:217], %[idn], 0\n\t"
          "s_nop 2\n\t"
          "ds_write_b128 %[px], v[172:175]\n\t"
          "s_nop 1\n\t"
          "ds_write_b128 %[px], v[176:179] offset:128\n\t"
          "s_nop 0\n\t"
          "v_cvt_pk_bf16_f32 v188, v220, v221\n\t"
          "v_cvt_pk_bf16_f32 v189, v222, v223\n\t"
          "s_nop 1\n\t"
          "v_mov_b32_dpp v190, v188 row_ror:8 row_mask:0xf bank_mask:0xf\n\t"
          "v_mov_b32_dpp v191, v189 row_ror:8 row_mask:0xf bank_mask:0xf\n\t"
          "s_nop 1\n\t"
          "v_mfma_f32_16x16x32_bf16 v[180:183], v[188:191], v[184:187], 0\n\t"
          "s_nop 7\n\t"
          "ds_write_b128 %[pp], v[180:183]\n\t"
          "s_waitcnt lgkmcnt(0)"
          : "+{v[208:211]}"(aq0), "+{v[212:215]}"(aq1), "+{v[216:219]}"(ak0), "+{v[220:223]}"(ak1), "=&{v[188:191]}"(kp)
          : [m5] "v"(mf[5]), [m6] "v"(mf[6]), [m7] "v"(mf[7]), [k05] "a"(wk[0][7]), [k15] "a"(wk[1][7]), [k06] "v"(wk[0][8]), [k16] "v"(wk[1][8]),
            [k07] "v"(wk[0][9]), [k17] "v"(wk[1][9]), [ix] "v"(sidx), [idn] "v"(idn), "{v[192:195]}"(kv4[0]), "{v[196:199]}"(kv4[1]),
            [pp] "v"(pp), [px] "v"(px)
          : "v172", "v173", "v174", "v175", "v176", "v177", "v178", "v179", "v180", "v181", "v182", "v183",
            "v184", "v185", "v186", "v187", "memory");
    }
    CST(2);
    STAMP(2);
    __syncthreads();
    STAMP(3);
    CST(3);
    {
      const char* pa = smem + CH_PART + 1024 + l * 16;
      const char* pc = smem + CH_PART + (((m < 8) & (g < 2)) ? l * 16 : 4096);
      const char* pb = smem + CH_PART + 3072 + l * 16;
      const f32x4 a0 = *(const f32x4*)(pa), a1 = *(const f32x4*)(pa + PSTR), a2 = *(const f32x4*)(pa + 2 * PSTR), a3 = *(const f32x4*)(pa + 3 * PSTR);
      const f32x4 c0 = *(const f32x4*)(pc), c1 = *(const f32x4*)(pc + PSTR), c2 = *(const f32x4*)(pc + 2 * PSTR), c3 = *(const f32x4*)(pc + 3 * PSTR);
      const f32x4 b0 = *(const f32x4*)(pb), b1 = *(const f32x4*)(pb + PSTR);
      __builtin_amdgcn_sched_barrier(0);
      const f32x4 ta = (a0 + a1) + (a2 + a3);
      const f32x4 b2 = *(const f32x4*)(pb + 2 * PSTR), b3 = *(const f32x4*)(pb + 3 * PSTR);
      __builtin_amdgcn_sched_barrier(0);
      const f32x4 tc = (c0 + c1) + (c2 + c3);
      ldwo(3); ldwo(4);
      __builtin_amdgcn_sched_barrier(0);
      CST(4);
      float s0 = ta.x, s1 = ta.y, s2 = ta.z, s3 = ta.w, s4 = tc.x, s5 = tc.y, s6 = tc.z, s7 = tc.w;
      float mxl, tu;
      f32x2 tl = b0.xy, th = b0.zw, ul = b2.xy, uh = b2.zw;
      asm volatile(
          "v_max3_f32 %[t], %[s0], %[s1], %[s2]\n\t"
          "v_max3_f32 %[u], %[s3], %[s4], %[s5]\n\t"
          "v_max3_f32 %[t], %[t], %[s6], %[s7]\n\t"
          "v_max_f32 %[t], %[t], %[u]\n\t"
          "v_pk_add_f32 %[tl], %[tl], %[b1l]\n\t"
          "v_pk_add_f32 %[th], %[th], %[b1h]\n\t"
          "v_max_f32_dpp %[t], %[t], %[t] row_ror:8 row_mask:0xf bank_mask:0xf\n\t"
          "v_mov_b32 %[u], %[t]\n\t"
          "v_pk_add_f32 %[ul], %[ul], %[b3l]\n\t"
          "v_pk_add_f32 %[uh], %[uh], %[b3h]\n\t"
          "v_permlane16_swap_b32 %[t], %[u]\n\t"
          "v_max_f32 %[t], %[t], %[u]\n\t"
          "v_mov_b32 %[u], %[t]\n\t"
          "v_pk_add_f32 %[tl], %[tl], %[ul]\n\t"
          "v_pk_add_f32 %[th], %[th], %[uh]\n\t"
          "v_permlane32_swap_b32 %[t], %[u]\n\t"
          "v_max_f32 %[t], %[t], %[u]\n\t"
          "v_mul_f32 %[t], 0xbe0293ee, %[t]"
          : [t] "=&v"(mxl), [u] "=&v"(tu), [tl] "+v"(tl), [th] "+v"(th), [ul] "+v"(ul), [uh] "+v"(uh)
          : [s0] "v"(s0), [s1] "v"(s1), [s2] "v"(s2), [s3] "v"(s3), [s4] "v"(s4), [s5] "v"(s5), [s6] "v"(s6), [s7] "v"(s7),
            [b1l] "v"(b1.xy), [b1h] "v"(b1.zw), [b3l] "v"(b3.xy), [b3h] "v"(b3.zw));
      CST(5);
      const bool pub = s != NSEG - 1;
      const u32 ib = (u32)(s & 1) * INBOX_SLOT + (u32)c * 128 + (4 * (g & 1) + (m & 3)) * 16;
      const u32 ob = pub ? ib + (u32)(20 * w + 4 * (g >> 1) + (m >> 2)) * (NPROD * 128) : 0x80000000u;
      const u32 ob2 = pub ? ib + (u32)(20 * w + 16 + 2 * (g >> 1) + (m >> 3)) * (NPROD * 128) + ((m >> 2) & 1) * 8 : 0x80000000u;
      asm volatile(
          "v_fmamk_f32 %[s0], %[s0], 0x3e0293ee, %[mx]\n\t"
          "v_fmamk_f32 %[s1], %[s1], 0x3e0293ee, %[mx]\n\t"
          "v_exp_f32 %[s0], %[s0]\n\t"
          "v_fmamk_f32 %[s2], %[s2], 0x3e0293ee, %[mx]\n\t"
          "v_exp_f32 %[s1], %[s1]\n\t"
          "v_fmamk_f32 %[s3], %[s3], 0x3e0293ee, %[mx]\n\t"
          "v_exp_f32 %[s2], %[s2]\n\t"
          "v_exp_f32 %[s3], %[s3]\n\t"
          "v_fmamk_f32 %[s4], %[s4], 0x3e0293ee, %[mx]\n\t"
          "v_cvt_pk_bf16_f32 v172, %[s0], %[s1]\n\t"
          "v_cvt_pk_bf16_f32 v173, %[s2], %[s3]\n\t"
          "v_fmamk_f32 %[s5], %[s5], 0x3e0293ee, %[mx]\n\t"
          "v_fmamk_f32 %[s6], %[s6], 0x3e0293ee, %[mx]\n\t"
          "v_mov_b32_dpp v174, v172 row_ror:8 row_mask:0xf bank_mask:0xf\n\t"
          "v_mov_b32_dpp v175, v173 row_ror:8 row_mask:0xf bank_mask:0xf\n\t"
          "v_fmamk_f32 %[s7], %[s7], 0x3e0293ee, %[mx]\n\t"
          "v_exp_f32 %[s4], %[s4]\n\t"
          "v_mfma_f32_16x16x32_bf16 v[208:211], v[200:203], v[172:175], 0\n\t"
          "v_exp_f32 %[s5], %[s5]\n\t"
          "v_mfma_f32_16x16x32_bf16 v[212:215], v[204:207], v[172:175], 0\n\t"
          "v_exp_f32 %[s6], %[s6]\n\t"
          "v_exp_f32 %[s7], %[s7]\n\t"
          "v_cvt_pk_bf16_f32 v178, %[t0], %[t1]\n\t"
          "v_cvt_pk_bf16_f32 v179, %[t2], %[t3]\n\t"
          "v_cvt_pk_bf16_f32 v176, %[s4], %[s5]\n\t"
          "v_cvt_pk_bf16_f32 v177, %[s6], %[s7]\n\t"
          "v_add_f32 v182, %[s0], %[s1]\n\t"
          "v_permlane32_swap_b32 v178, v172\n\t"
          "v_permlane32_swap_b32 v179, v173\n\t"
          "v_add_f32 v183, %[s2], %[s3]\n\t"
          "v_add_f32 v182, v182, v183\n\t"
          "v_mfma_f32_16x16x16_bf16 v[208:211], v[178:179], v[176:177], v[208:211]\n\t"
          "v_add_f32 v183, %[s4], %[s5]\n\t"
          "v_add_f32 %[s6], %[s6], %[s7]\n\t"
          "v_mfma_f32_16x16x16_bf16 v[212:215], v[172:173], v[176:177], v[212:215]\n\t"
          "v_add_f32 v183, v183, %[s6]\n\t"
          "v_add_f32 v182, v182, v183\n\t"
          "v_mov_b64 v[184:185], 0\n\t"
          "v_mov_b64 v[186:187], 0\n\t"
          "v_add_f32_dpp v182, v182, v182 row_ror:8 row_mask:0xf bank_mask:0xf\n\t"
          "v_mov_b32 v183, v182\n\t"
          "v_mov_b64 v[164:165], 0\n\t"
          "v_mov_b64 v[166:167], 0\n\t"
          "v_permlane16_swap_b32 v182, v183\n\t"
          "v_add_f32 v182, v182, v183\n\t"
          "v_mov_b32 v183, v182\n\t"
          "v_mov_b64 v[220:221], 0\n\t"
          "v_mov_b64 v[222:223], 0\n\t"
          "v_permlane32_swap_b32 v182, v183\n\t"
          "v_add_f32 v182, v182, v183\n\t"
          "v_rcp_f32 v182, v182\n\t"
          "v_mov_b64 v[192:193], 0\n\t"
          "v_mul_f32 v208, v208, v182\n\t"
          "v_mul_f32 v209, v209, v182\n\t"
          "v_mul_f32 v210, v210, v182\n\t"
          "v_mul_f32 v211, v211, v182\n\t"
          "v_cvt_pk_bf16_f32 v216, v208, v209\n\t"
          "v_cvt_pk_bf16_f32 v217, v210, v211\n\t"
          "v_mul_f32 v212, v212, v182\n\t"
          "v_mul_f32 v213, v213, v182\n\t"
          "v_mul_f32 v214, v214, v182\n\t"
          "v_mul_f32 v215, v215, v182\n\t"
          "v_cvt_pk_bf16_f32 v218, v212, v213\n\t"
          "v_cvt_pk_bf16_f32 v219, v214, v215\n\t"
          "v_mov_b32_dpp v216, v216 row_ror:8 row_mask:0xf bank_mask:0xc\n\t"
          "v_mov_b32_dpp v217, v217 row_ror:8 row_mask:0xf bank_mask:0xc\n\t"
          "v_mov_b32_dpp v218, v218 row_ror:8 row_mask:0xf bank_mask:0xc\n\t"
          "v_mov_b32_dpp v219, v219 row_ror:8 row_mask:0xf bank_mask:0xc\n\t"
          "v_mov_b64 v[194:195], 0\n\t"
          "v_mov_b64 v[196:197], 0\n\t"
          "v_smfmac_f32_16x16x64_bf16 v[184:187], v[216:219], %[w0], %[ix]\n\t"
          "v_mov_b64 v[198:199], 0\n\t"
          "v_smfmac_f32_16x16x64_bf16 v[164:167], v[216:219], %[w1], %[ix]\n\t"
          "v_smfmac_f32_16x16x64_bf16 v[220:223], v[216:219], %[w2], %[ix]\n\t"
          "v_smfmac_f32_16x16x64_bf16 v[192:195], v[216:219], %[w3], %[ix]\n\t"
          "v_smfmac_f32_16x16x64_bf16 v[196:199], v[216:219], %[w4], %[ix]\n\t"
          "v_cvt_pk_bf16_f32 v208, v184, v185\n\t"
          "v_cvt_pk_bf16_f32 v209, v186, v187\n\t"
          "v_cvt_pk_bf16_f32 v210, v164, v165\n\t"
          "v_cvt_pk_bf16_f32 v211, v166, v167\n\t"
          "buffer_store_dwordx4 v[208:211], %[ob], %[ri], 0 offen sc1\n\t"
          "v_cvt_pk_bf16_f32 v212, v220, v221\n\t"
          "v_cvt_pk_bf16_f32 v213, v222, v223\n\t"
          "v_cvt_pk_bf16_f32 v214, v192, v193\n\t"
          "v_cvt_pk_bf16_f32 v215, v194, v195\n\t"
          "buffer_store_dwordx4 v[212:215], %[ob], %[ri], %[so] offen sc1\n\t"
          "v_cvt_pk_bf16_f32 v216, v196, v197\n\t"
          "v_cvt_pk_bf16_f32 v217, v198, v199\n\t"
          "buffer_store_dwordx2 v[216:217], %[ob2], %[ri], 0 offen sc1\n\t"
          "s_nop 1"
          : [s0] "+v"(s0), [s1] "+v"(s1), [s2] "+v"(s2), [s3] "+v"(s3), [s4] "+v"(s4), [s5] "+v"(s5), [s6] "+v"(s6), [s7] "+v"(s7), "=&{v[178:179]}"(vm0), "=&{v[172:173]}"(vm1)
          : [mx] "v"(mxl), [t0] "v"(tl.x), [t1] "v"(tl.y), [t2] "v"(th.x), [t3] "v"(th.y), "{v[200:203]}"(kv4[2]), "{v[204:207]}"(kv4[3]),
            [w0] "v"(wo[0]), [w1] "v"(wo[1]), [w2] "v"(wo[2]), [w3] "v"(wo[3]), [w4] "v"(wo[4]), [ix] "v"(sidx),
            [ob] "v"(ob), [ob2] "v"(ob2), [ri] "s"(din), [so] "s"(8 * NPROD * 128)
          : "v192", "v193", "v194", "v195", "v196", "v197", "v198", "v199", "v174", "v175", "v176", "v177", "v182", "v183",
            "v184", "v185", "v186", "v187", "v164", "v165", "v166", "v167", "v208", "v209", "v210", "v211", "v212", "v213", "v214", "v215",
            "v216", "v217", "v218", "v219", "v220", "v221", "v222", "v223", "memory");
    }
    const int slot = s & 1;
    CST(6);
    STAMP(4);
    if (w == gs && m < 8) __builtin_amdgcn_raw_buffer_store_b128(kp, rkm, (u32)(((s * 8 + m) * HD + h * 128 + 32 * w + 8 * g) * 2), 0, 16);
    if (w == ((gs + 1) & 3) && g < 2) __builtin_amdgcn_raw_buffer_store_b64(vm0, rkm, (u32)(SZ_KM + (((s * 32 + h) * 128 + 32 * gs + p32(0, m)) * 8 + 4 * g) * 2), 0, 16);
    if (w == ((gs + 2) & 3) && g < 2) __builtin_amdgcn_raw_buffer_store_b64(vm1, rkm, (u32)(SZ_KM + (((s * 32 + h) * 128 + 32 * gs + p32(1, m)) * 8 + 4 * g) * 2), 0, 16);
    const int doit = (c - (int)NRED) & (s - (NSEG - 1));
    {
      const u32 base = slot * INBOX_SLOT + (c < (int)NRED ? c : 0) * NPROD * 128 + w * 4096 + l * 16;
      int mm = m;
      asm volatile("" : "+v"(mm));
      const u32 one2 = (mm & 1) ? 0x3f800000u : 0x3f80u;
      const u32x4 sel = {(mm >> 1) == 0 ? one2 : 0u, (mm >> 1) == 1 ? one2 : 0u, (mm >> 1) == 2 ? one2 : 0u, (mm >> 1) == 3 ? one2 : 0u};
      f32x4 rd;
      u32 tmx, cnt;
      unsigned long long m0, m1, m2, m3, mo;
      asm volatile(
          "s_mov_b32 %[cnt], 0\n\t"
          "s_cmp_ge_i32 %[doit], 0\n\t"
          "s_cbranch_scc1 4f\n\t"
          "s_sleep %[slp0]\n\t"
          "buffer_load_dwordx4 v[224:227], %[o], %[rs], 0 offen offset:0 sc1\n\t"
          "buffer_load_dwordx4 v[228:231], %[o], %[rs], 0 offen offset:1024 sc1\n\t"
          "buffer_load_dwordx4 v[232:235], %[o], %[rs], 0 offen offset:2048 sc1\n\t"
          "buffer_load_dwordx4 v[236:239], %[o], %[rs], 0 offen offset:3072 sc1\n\t"
          "s_mov_b64 %[m0], -1\n\t"
          "s_mov_b64 %[m1], -1\n\t"
          "s_mov_b64 %[m2], -1\n\t"
          "s_mov_b64 %[m3], -1\n\t"
          "s_sleep %[slp]\n"
          "1:\n\t"
          "s_mov_b64 exec, %[m0]\n\t"
          "buffer_load_dwordx4 v[224:227], %[o], %[rs], 0 offen offset:0 sc1\n\t"
          "s_mov_b64 exec, %[m1]\n\t"
          "buffer_load_dwordx4 v[228:231], %[o], %[rs], 0 offen offset:1024 sc1\n\t"
          "s_mov_b64 exec, %[m2]\n\t"
          "buffer_load_dwordx4 v[232:235], %[o], %[rs], 0 offen offset:2048 sc1\n\t"
          "s_mov_b64 exec, %[m3]\n\t"
          "buffer_load_dwordx4 v[236:239], %[o], %[rs], 0 offen offset:3072 sc1\n\t"
          "s_mov_b64 exec, -1\n\t"
          "s_waitcnt vmcnt(4)\n\t"
          "v_max3_u32 %[t], v224, v225, v226\n\t"
          "v_max_u32 %[t], %[t], v227\n\t"
          "v_cmp_eq_u32 %[m0], -1, %[t]\n\t"
          "v_max3_u32 %[t], v228, v229, v230\n\t"
          "v_max_u32 %[t], %[t], v231\n\t"
          "v_cmp_eq_u32 %[m1], -1, %[t]\n\t"
          "v_max3_u32 %[t], v232, v233, v234\n\t"
          "v_max_u32 %[t], %[t], v235\n\t"
          "v_cmp_eq_u32 %[m2], -1, %[t]\n\t"
          "v_max3_u32 %[t], v236, v237, v238\n\t"
          "v_max_u32 %[t], %[t], v239\n\t"
          "v_cmp_eq_u32 %[m3], -1, %[t]\n\t"
          "s_or_b64 %[mo], %[m0], %[m1]\n\t"
          "s_or_b64 vcc, %[m2], %[m3]\n\t"
          "s_or_b64 %[mo], %[mo], vcc\n\t"
          "s_cbranch_scc0 2f\n\t"
          "s_add_u32 %[cnt], %[cnt], 1\n\t"
          "s_cmp_lt_u32 %[cnt], 0x20000\n\t"
          "s_cbranch_scc1 1b\n"
          "2:\n\t"
          "v_mfma_f32_16x16x32_bf16 %[d], v[224:227], %[sel], 0\n\t"
          "v_mfma_f32_16x16x32_bf16 %[d], v[228:231], %[sel], %[d]\n\t"
          "v_mfma_f32_16x16x32_bf16 %[d], v[232:235], %[sel], %[d]\n\t"
          "v_mfma_f32_16x16x32_bf16 %[d], v[236:239], %[sel], %[d]\n\t"
          "4:"
          : [d] "=&v"(rd), "+{v[224:227]}"(qt[0]), "+{v[228:231]}"(qt[1]), "+{v[232:235]}"(qt[2]), "+{v[236:239]}"(qt[3]), [t] "=&v"(tmx), [cnt] "=&s"(cnt),
            [m0] "=&s"(m0), [m1] "=&s"(m1), [m2] "=&s"(m2), [m3] "=&s"(m3), [mo] "=&s"(mo)
          : [o] "v"(base), [rs] "s"(din), [slp] "i"(SLP_D1), [slp0] "i"(SLP_RI), [doit] "s"(doit), [sel] "v"(sel)
          : "vcc", "scc", "memory");
      load_kv(s != NSEG - 1 ? s + 1 : s);
      if (c >= (int)NRED && s != NSEG - 1) __builtin_amdgcn_s_sleep(SLP_NR);
      if (doit < 0) {
      if (cnt >= 0x20000u && l == 0) *err = 2;
      STAMP(5);
      asm volatile("s_nop 4" : "+v"(rd));
      {
        const f32x2 r2 = {hsum32(rd.x, rd.z), hsum32(rd.y, rd.w)};
        if (m < 8) *(f32x2*)(smem + CH_SRED + (w * 8 + 4 * (g & 1) + (m & 3)) * 32 + (m >> 2) * 16 + (g >> 1) * 8) = r2;
      }
      __syncthreads();
      if (w == 0) {
        const int l8 = l & 7;
        const f32x4* sr = (const f32x4*)(smem + CH_SRED);
        f32x4 a0 = (sr[2 * l8] + sr[16 + 2 * l8]) + (sr[32 + 2 * l8] + sr[48 + 2 * l8]);
        f32x4 a1 = (sr[2 * l8 + 1] + sr[16 + 2 * l8 + 1]) + (sr[32 + 2 * l8 + 1] + sr[48 + 2 * l8 + 1]);
        u32x4 o = {pack2(a0.x, a0.y), pack2(a0.z, a0.w), pack2(a1.x, a1.y), pack2(a1.z, a1.w)};
        __builtin_amdgcn_raw_buffer_store_b128(o, rmem, (s + 1) * MEM_STEP + (l >> 3) * MEM_REP + c * 128 + l8 * 16, 0, 16);
      }
      STAMP(6);
      __builtin_amdgcn_s_sleep(SLP_RM);
      }
    }
  }
  asm volatile("s_waitcnt vmcnt(0)" ::"{v[224:227]}"(qt[0]), "{v[228:231]}"(qt[1]), "{v[232:235]}"(qt[2]), "{v[236:239]}"(qt[3]), "{v[240:243]}"(mfo[0]), "{v[244:247]}"(mfo[1]), "{v[248:251]}"(mfo[2]), "{v252}"(qf) : "memory");
  __syncthreads();
  if (tid == 0) __hip_atomic_fetch_add(mcnt + ((NSEG - 1) * 32 + h) * 16, 1u, __ATOMIC_RELAXED, __HIP_MEMORY_SCOPE_AGENT);
}

__device__ __forceinline__ int otid() { int t = threadIdx.x; asm volatile("" : "+v"(t)); return t; }
#define VMCNT(n) asm volatile("s_waitcnt vmcnt(%0)" ::"i"(n) : "memory")
#define RAWBAR() do { asm volatile("s_waitcnt lgkmcnt(0)" ::: "memory"); __builtin_amdgcn_s_barrier(); asm volatile("" ::: "memory"); } while (0)

__device__ __forceinline__ void block_top8(const float* __restrict__ r, int* sidx, float* sval, float* swv, int* swi) {
  const int tid = otid(), w = tid >> 6, l = tid & 63;
  float v[8];
#pragma unroll
  for (int j = 0; j < 8; ++j) v[j] = r[tid + 256 * j];
  for (int round = 0; round < CSEL; ++round) {
    float bv = v[0]; int bi = tid;
#pragma unroll
    for (int j = 1; j < 8; ++j) if (v[j] > bv) { bv = v[j]; bi = tid + 256 * j; }
#pragma unroll
    for (int o = 32; o > 0; o >>= 1) {
      float ov = __shfl_xor(bv, o); int oi = __shfl_xor(bi, o);
      if (ov > bv || (ov == bv && oi < bi)) { bv = ov; bi = oi; }
    }
    if (l == 0) { swv[w] = bv; swi[w] = bi; }
    __syncthreads();
    float fv = swv[0]; int fi = swi[0];
#pragma unroll
    for (int k = 1; k < 4; ++k) { float ov = swv[k]; int oi = swi[k]; if (ov > fv || (ov == fv && oi < fi)) { fv = ov; fi = oi; } }
    if (tid == 0) { sidx[round] = fi; sval[round] = fv; }
#pragma unroll
    for (int j = 0; j < 8; ++j) if (tid + 256 * j == fi) v[j] = -INFINITY;
    __syncthreads();
  }
}

__device__ __forceinline__ void worker_modA(const u16* __restrict__ wg, const u16* __restrict__ wu, char* __restrict__ ws, char* smem, const int blk) {
  float* xs = (float*)smem;
  float* red = (float*)(smem + 20480);
  int* sidx = (int*)(smem + 24576);
  float* sval = (float*)(smem + 24576 + 64);
  float* swv = (float*)(smem + 24576 + 128);
  int* swi = (int*)(smem + 24576 + 192);
  const int tid = otid(), w = tid >> 6, l = tid & 63;
  block_top8((const float*)(ws + OFF_R), sidx, sval, swv, swi);
  const u16* xn = (const u16*)(ws + OFF_XN);
  for (int i = tid; i < 8 * 640; i += 256) { int rr = i / 640, k = i % 640; xs[i] = bf2f(xn[(size_t)sidx[rr] * DM + k]); }
  const __amdgpu_buffer_rsrc_t rh = __builtin_amdgcn_make_buffer_rsrc(ws + OFF_HB, 0, (u32)(OFF_ERR - OFF_HB), 0x00020000);
  if (blk == 0 && tid < 8) {
    __builtin_amdgcn_raw_buffer_store_b32((u32)sidx[tid], rh, (u32)(OFF_SEL - OFF_HB) + tid * 4, 0, 16);
    __builtin_amdgcn_raw_buffer_store_b32(__float_as_uint(bfr(1.0f / (1.0f + __expf(-sval[tid])))), rh, (u32)(OFF_SEL - OFF_HB) + 32 + tid * 4, 0, 16);
  }
  __syncthreads();
  const int cp = tid & 7, kg = tid >> 3;
  const int n0 = blk * 16 + 2 * cp;
  float ag[8][2], au[8][2];
#pragma unroll
  for (int rr = 0; rr < 8; ++rr) { ag[rr][0] = ag[rr][1] = au[rr][0] = au[rr][1] = 0.f; }
  u32 g2[20], u2[20];
#pragma unroll
  for (int i = 0; i < 20; ++i) {
    g2[i] = *(const u32*)(wg + (size_t)(20 * kg + i) * FF + n0);
    u2[i] = *(const u32*)(wu + (size_t)(20 * kg + i) * FF + n0);
  }
#pragma unroll
  for (int i = 0; i < 20; ++i) {
    const float g0 = bflo(g2[i]), g1 = bfhi(g2[i]), u0 = bflo(u2[i]), u1 = bfhi(u2[i]);
#pragma unroll
    for (int rr = 0; rr < 8; ++rr) {
      const float xv = xs[rr * 640 + 20 * kg + i];
      ag[rr][0] += xv * g0; ag[rr][1] += xv * g1; au[rr][0] += xv * u0; au[rr][1] += xv * u1;
    }
  }
#pragma unroll
  for (int rr = 0; rr < 8; ++rr) {
#pragma unroll
    for (int e = 0; e < 2; ++e) {
      ag[rr][e] = xr32_add(xr16_add(xr8_add(ag[rr][e])));
      au[rr][e] = xr32_add(xr16_add(xr8_add(au[rr][e])));
    }
  }
  if (l < 8) {
#pragma unroll
    for (int rr = 0; rr < 8; ++rr) *(f32x4*)(red + (w * 8 + l) * 32 + rr * 4) = (f32x4){ag[rr][0], ag[rr][1], au[rr][0], au[rr][1]};
  }
  __syncthreads();
  if (tid < 64) {
    const int rr = tid >> 3, c2 = tid & 7;
    f32x4 a0 = *(const f32x4*)(red + (0 * 8 + c2) * 32 + rr * 4), a1 = *(const f32x4*)(red + (1 * 8 + c2) * 32 + rr * 4);
    f32x4 a2 = *(const f32x4*)(red + (2 * 8 + c2) * 32 + rr * 4), a3 = *(const f32x4*)(red + (3 * 8 + c2) * 32 + rr * 4);
    f32x4 t = (a0 + a1) + (a2 + a3);
    float hv[2];
#pragma unroll
    for (int e = 0; e < 2; ++e) {
      const float gsum = bfr(t[e]), usum = bfr(t[2 + e]);
      const float sg = bfr(gsum / (1.0f + __expf(-gsum)));
      hv[e] = sg * usum;
    }
    __builtin_amdgcn_raw_buffer_store_b32(pack2(hv[0], hv[1]), rh, (u32)((rr * FF + blk * 16 + 2 * c2) * 2), 0, 16);
  }
  asm volatile("s_waitcnt vmcnt(0)" ::: "memory");
  __syncthreads();
  if (tid == 0) __hip_atomic_fetch_add((u32*)(ws + OFF_HCNT), 1u, __ATOMIC_RELAXED, __HIP_MEMORY_SCOPE_AGENT);
}

__device__ __forceinline__ void worker_modB(const u16* __restrict__ wd, char* __restrict__ ws, char* smem, const int blk) {
  float* hs = (float*)smem;
  float* red = (float*)(smem + 4096);
  const int tid = otid(), w = tid >> 6, l = tid & 63;
  const int cb = blk % 5, kb = blk / 5;
  const int n0 = cb * 128 + 2 * l;
  u32 d2[27];
#pragma unroll
  for (int i = 0; i < 27; ++i) d2[i] = *(const u32*)(wd + (size_t)(kb * 108 + 27 * w + i) * DM + n0);
  wait_count((const u32*)(ws + OFF_HCNT), 108u, (u32*)(ws + OFF_ERR));
  const __amdgpu_buffer_rsrc_t rh = __builtin_amdgcn_make_buffer_rsrc(ws + OFF_HB, 0, (u32)(OFF_ERR - OFF_HB), 0x00020000);
  for (int i = tid; i < 8 * 54; i += 256) {
    const int rr = i / 54, k2 = i % 54;
    const u32 v = __builtin_amdgcn_raw_buffer_load_b32(rh, (u32)((rr * FF + kb * 108 + 2 * k2) * 2), 0, 16);
    hs[rr * 108 + 2 * k2] = bflo(v); hs[rr * 108 + 2 * k2 + 1] = bfhi(v);
  }
  __syncthreads();
  float a[8][2];
#pragma unroll
  for (int rr = 0; rr < 8; ++rr) a[rr][0] = a[rr][1] = 0.f;
#pragma unroll
  for (int i = 0; i < 27; ++i) {
    const float d0 = bflo(d2[i]), d1 = bfhi(d2[i]);
#pragma unroll
    for (int rr = 0; rr < 8; ++rr) { const float hv = hs[rr * 108 + 27 * w + i]; a[rr][0] += hv * d0; a[rr][1] += hv * d1; }
  }
#pragma unroll
  for (int rr = 0; rr < 8; ++rr) { red[(w * 8 + rr) * 128 + 2 * l] = a[rr][0]; red[(w * 8 + rr) * 128 + 2 * l + 1] = a[rr][1]; }
  __syncthreads();
  for (int i = tid; i < 8 * 128; i += 256) {
    const int rr = i >> 7, cc = i & 127;
    const float v = (red[rr * 128 + cc] + red[(8 + rr) * 128 + cc]) + (red[(16 + rr) * 128 + cc] + red[(24 + rr) * 128 + cc]);
    __builtin_amdgcn_raw_buffer_store_b32(__float_as_uint(v), rh, (u32)(OFF_YP - OFF_HB) + (u32)(((kb * 8 + rr) * DM + cb * 128 + cc) * 4), 0, 16);
  }
  asm volatile("s_waitcnt vmcnt(0)" ::: "memory");
  __syncthreads();
  if (tid == 0) __hip_atomic_fetch_add((u32*)(ws + OFF_YCNT), 1u, __ATOMIC_RELAXED, __HIP_MEMORY_SCOPE_AGENT);
}

__device__ __forceinline__ void worker_kv(char* __restrict__ ws, char* smem, const int mb, const int h) {
  const int tid = otid(), w = __builtin_amdgcn_readfirstlane(tid >> 6), l = tid & 63;
  const char* xnf = ws + OFF_XNF;
  const char* wf = ws + OFF_WF;
  const int wr = w >> 1, wc = w & 1;
  const int c = l & 15, g = l >> 4;
  f32x4 acc[4][8];
#pragma unroll
  for (int i = 0; i < 4; ++i)
#pragma unroll
    for (int j = 0; j < 8; ++j) acc[i][j] = (f32x4){0, 0, 0, 0};
  const char* src[6];
#pragma unroll
  for (int i = 0; i < 6; ++i) {
    const int f = 6 * w + i;
    src[i] = ((f < 8) ? xnf + (size_t)(mb * 8 + f) * 20480 : (f < 16 ? wf + (size_t)(256 + h * 8 + (f - 8)) * 20480 : wf + (size_t)(512 + h * 8 + (f - 16)) * 20480)) + l * 16;
  }
  auto stage = [&](int ks) {
#pragma unroll
    for (int i = 0; i < 6; ++i) glds16(src[i] + ks * 1024, smem + (ks % 5) * 24576 + (6 * w + i) * 1024 + l * 16);
  };
#pragma unroll
  for (int ks = 0; ks < 4; ++ks) stage(ks);
#pragma unroll
  for (int ks = 0; ks < 20; ++ks) {
    VMCNT(6 * (ks < 17 ? 3 : 19 - ks));
    RAWBAR();
    if (ks + 4 < 20) stage(ks + 4);
    const char* base = smem + (ks % 5) * 24576 + l * 16;
    bf16x8 a[4], bb[8];
#pragma unroll
    for (int i = 0; i < 4; ++i) a[i] = *(const bf16x8*)(base + (4 * wr + i) * 1024);
#pragma unroll
    for (int j = 0; j < 8; ++j) bb[j] = *(const bf16x8*)(base + (8 + 8 * wc + j) * 1024);
    if (wc == 0) {
#pragma unroll
      for (int i = 0; i < 4; ++i)
#pragma unroll
        for (int j = 0; j < 8; ++j) acc[i][j] = mfma32(bb[j], a[i], acc[i][j]);
    } else {
#pragma unroll
      for (int i = 0; i < 4; ++i)
#pragma unroll
        for (int j = 0; j < 8; ++j) acc[i][j] = mfma32(a[i], bb[j], acc[i][j]);
    }
  }
  const __amdgpu_buffer_rsrc_t rkv = __builtin_amdgcn_make_buffer_rsrc(ws + OFF_K, 0, (u32)(2 * SZ_Q), 0x00020000);
  if (wc == 0) {
#pragma unroll
    for (int i = 0; i < 4; ++i) {
      const int mt = mb * 8 + 4 * wr + i;
      const int tok = 32 * (mt >> 1) + p32(mt & 1, c);
#pragma unroll
      for (int u = 0; u < 4; ++u) {
        f32x4 a0 = acc[i][2 * u], a1 = acc[i][2 * u + 1];
        u32x4 v = {pack2(a0.x, a0.y), pack2(a0.z, a0.w), pack2(a1.x, a1.y), pack2(a1.z, a1.w)};
        __builtin_amdgcn_raw_buffer_store_b128(v, rkv, (u32)((tok * HD + h * 128 + 32 * u + 8 * g) * 2), 0, 16);
      }
    }
  } else {
#pragma unroll
    for (int ip = 0; ip < 2; ++ip) {
      const int sg = mb * 4 + 2 * wr + ip;
#pragma unroll
      for (int j = 0; j < 8; ++j) {
        const int e = 32 * (j >> 1) + p32(j & 1, c);
        f32x4 a0 = acc[2 * ip][j], a1 = acc[2 * ip + 1][j];
        u32x4 v = {pack2(a0.x, a0.y), pack2(a0.z, a0.w), pack2(a1.x, a1.y), pack2(a1.z, a1.w)};
        __builtin_amdgcn_raw_buffer_store_b128(v, rkv, (u32)(SZ_Q + (((sg * 32 + h) * 128 + e) * 32 + 8 * g) * 2), 0, 16);
      }
    }
  }
  asm volatile("s_waitcnt vmcnt(0)" ::: "memory");
  __syncthreads();
  if (tid == 0) __hip_atomic_fetch_add((u32*)(ws + OFF_KVC) + (mb * 32 + h) * 16, 1u, __ATOMIC_RELAXED, __HIP_MEMORY_SCOPE_AGENT);
}

__device__ __forceinline__ void worker_qattn(char* __restrict__ ws, char* smem, const int mb, const int h) {
  const int tid = otid(), w = __builtin_amdgcn_readfirstlane(tid >> 6), l = tid & 63;
  const char* xnf = ws + OFF_XNF;
  const char* wf = ws + OFF_WF;
  const int wr = w >> 1, wc = w & 1;
  const f32x4 z4 = {0, 0, 0, 0};
  const int c = l & 15, g = l >> 4;
  {
    f32x4 acc[4][4];
#pragma unroll
    for (int i = 0; i < 4; ++i)
#pragma unroll
      for (int j = 0; j < 4; ++j) acc[i][j] = z4;
    const char* src[4];
#pragma unroll
    for (int i = 0; i < 4; ++i) {
      const int f = 4 * w + i;
      src[i] = ((f < 8) ? xnf + (size_t)(mb * 8 + f) * 20480 : wf + (size_t)(h * 8 + (f - 8)) * 20480) + l * 16;
    }
    auto stage = [&](int ks) {
#pragma unroll
      for (int i = 0; i < 4; ++i) glds16(src[i] + ks * 1024, smem + (ks % 5) * 16384 + (4 * w + i) * 1024 + l * 16);
    };
#pragma unroll
    for (int ks = 0; ks < 4; ++ks) stage(ks);
#pragma unroll
    for (int ks = 0; ks < 20; ++ks) {
      VMCNT(4 * (ks < 17 ? 3 : 19 - ks));
      RAWBAR();
      if (ks + 4 < 20) stage(ks + 4);
      const char* base = smem + (ks % 5) * 16384 + l * 16;
      bf16x8 a[4], bb[4];
#pragma unroll
      for (int i = 0; i < 4; ++i) a[i] = *(const bf16x8*)(base + (4 * wr + i) * 1024);
#pragma unroll
      for (int j = 0; j < 4; ++j) bb[j] = *(const bf16x8*)(base + (8 + 4 * wc + j) * 1024);
#pragma unroll
      for (int i = 0; i < 4; ++i)
#pragma unroll
        for (int j = 0; j < 4; ++j) acc[i][j] = mfma32(bb[j], a[i], acc[i][j]);
    }
#pragma unroll
    for (int i = 0; i < 4; ++i)
#pragma unroll
      for (int u = 0; u < 2; ++u) {
        f32x4 a0 = acc[i][2 * u], a1 = acc[i][2 * u + 1];
        u32x4 v = {pack2(a0.x, a0.y), pack2(a0.z, a0.w), pack2(a1.x, a1.y), pack2(a1.z, a1.w)};
        *(u32x4*)(smem + 81920 + (((4 * wr + i) * 4 + 2 * wc + u) * 64 + l) * 16) = v;
      }
  }
  __syncthreads();
  const int s = mb * 4 + w;
  wait_count((const u32*)(ws + OFF_KVC) + (mb * 32 + h) * 16, 1u, (u32*)(ws + OFF_ERR));
  const __amdgpu_buffer_rsrc_t rkv = __builtin_amdgcn_make_buffer_rsrc(ws + OFF_K, 0, (u32)(2 * SZ_Q), 0x00020000);
  const __amdgpu_buffer_rsrc_t rkm = __builtin_amdgcn_make_buffer_rsrc(ws + OFF_KM, 0, (u32)(2 * SZ_KM), 0x00020000);
  const __amdgpu_buffer_rsrc_t rof = __builtin_amdgcn_make_buffer_rsrc(ws + OFF_OF, 0, (u32)SZ_Q, 0x00020000);
  const float scale = 0.08838834764831845f;
  f32x4 sM[2], sX[2][2];
#pragma unroll
  for (int T = 0; T < 2; ++T) { sM[T] = z4; sX[0][T] = z4; sX[1][T] = z4; }
  bf16x8 q[4][2];
#pragma unroll
  for (int ks = 0; ks < 4; ++ks) {
    bf16x8 kx[2];
#pragma unroll
    for (int T = 0; T < 2; ++T) {
      q[ks][T] = *(const bf16x8*)(smem + 81920 + (((2 * w + T) * 4 + ks) * 64 + l) * 16);
      kx[T] = as_bf8(__builtin_amdgcn_raw_buffer_load_b128(rkv, (u32)(((32 * s + p32(T, c)) * HD + h * 128 + 32 * ks + 8 * g) * 2), 0, 16));
    }
#pragma unroll
    for (int T = 0; T < 2; ++T) {
      sX[0][T] = mfma32(kx[0], q[ks][T], sX[0][T]);
      sX[1][T] = mfma32(kx[1], q[ks][T], sX[1][T]);
    }
  }
  bf16x8 vxa[4][2];
#pragma unroll
  for (int u = 0; u < 4; ++u)
#pragma unroll
    for (int E = 0; E < 2; ++E)
      vxa[u][E] = as_bf8(__builtin_amdgcn_raw_buffer_load_b128(rkv, (u32)(SZ_Q + (((s * 32 + h) * 128 + 32 * u + p32(E, c)) * 32 + 8 * g) * 2), 0, 16));
  wait_count((const u32*)(ws + OFF_MCNT) + (s * 32 + h) * 16, 4u, (u32*)(ws + OFF_ERR));
  u32x2 vma[4][2];
  {
    u32x4 km[4];
#pragma unroll
    for (int ks = 0; ks < 4; ++ks) km[ks] = __builtin_amdgcn_raw_buffer_load_b128(rkm, (u32)(((s * 8 + (c & 7)) * HD + h * 128 + 32 * ks + 8 * g) * 2), 0, 16);
#pragma unroll
    for (int u = 0; u < 4; ++u)
#pragma unroll
      for (int E = 0; E < 2; ++E)
        vma[u][E] = __builtin_amdgcn_raw_buffer_load_b64(rkm, (u32)(SZ_KM + (((s * 32 + h) * 128 + 32 * u + p32(E, c)) * 8 + 4 * (g & 1)) * 2), 0, 16);
#pragma unroll
    for (int ks = 0; ks < 4; ++ks)
#pragma unroll
      for (int T = 0; T < 2; ++T) sM[T] = mfma32(as_bf8(km[ks]), q[ks][T], sM[T]);
  }
  u32x4 px[2]; u32x2 pm[2];
#pragma unroll
  for (int T = 0; T < 2; ++T) {
    const int qi = p32(T, c);
    float sm_[4], sx[8];
#pragma unroll
    for (int i = 0; i < 4; ++i) {
      sm_[i] = g < 2 ? bfr(bfr(sM[T][i]) * scale) : -INFINITY;
      float a = bfr(bfr(sX[0][T][i]) * scale), b = bfr(bfr(sX[1][T][i]) * scale);
      sx[i] = (8 * g + i <= qi) ? a : -1e9f;
      sx[4 + i] = (8 * g + 4 + i <= qi) ? b : -1e9f;
    }
    float mx = fmaxf(fmaxf(fmaxf(sx[0], sx[1]), fmaxf(sx[2], sx[3])), fmaxf(fmaxf(sx[4], sx[5]), fmaxf(sx[6], sx[7])));
    mx = fmaxf(mx, fmaxf(fmaxf(sm_[0], sm_[1]), fmaxf(sm_[2], sm_[3])));
    mx = xr32_max(xr16_max(mx));
    float sum = 0.f;
#pragma unroll
    for (int i = 0; i < 8; ++i) { sx[i] = __expf(sx[i] - mx); sum += sx[i]; }
#pragma unroll
    for (int i = 0; i < 4; ++i) { sm_[i] = __expf(sm_[i] - mx); sum += sm_[i]; }
    sum = xr32_add(xr16_add(sum));
    const float inv = 1.0f / sum;
    px[T] = (u32x4){pack2(sx[0] * inv, sx[1] * inv), pack2(sx[2] * inv, sx[3] * inv), pack2(sx[4] * inv, sx[5] * inv), pack2(sx[6] * inv, sx[7] * inv)};
    pm[T] = (u32x2){pack2(sm_[0] * inv, sm_[1] * inv), pack2(sm_[2] * inv, sm_[3] * inv)};
  }
#pragma unroll
  for (int u = 0; u < 4; ++u) {
    f32x4 o[2][2];
#pragma unroll
    for (int E = 0; E < 2; ++E) {
      u32x2 vm = vma[u][E];
      if (g >= 2) vm = (u32x2){0, 0};
#pragma unroll
      for (int T = 0; T < 2; ++T) {
        o[E][T] = mfma32(vxa[u][E], as_bf8(px[T]), z4);
        o[E][T] = mfma16(as_bf4(vm), as_bf4(pm[T]), o[E][T]);
      }
    }
#pragma unroll
    for (int T = 0; T < 2; ++T) {
      u32x4 v = {pack2(o[0][T].x, o[0][T].y), pack2(o[0][T].z, o[0][T].w), pack2(o[1][T].x, o[1][T].y), pack2(o[1][T].z, o[1][T].w)};
      __builtin_amdgcn_raw_buffer_store_b128(v, rof, (u32)((((2 * s + T) * 128 + (4 * h + u)) * 64 + l) * 16), 0, 16);
    }
  }
  asm volatile("s_waitcnt vmcnt(0)" ::: "memory");
  if (l == 0) __hip_atomic_fetch_add((u32*)(ws + OFF_OSEG) + s * 16, 1u, __ATOMIC_RELAXED, __HIP_MEMORY_SCOPE_AGENT);
  __syncthreads();
}

__device__ __forceinline__ void final_store2(char* __restrict__ ws, u16* __restrict__ out, const int tok, const int n0, const f32x4 a0, const f32x4 a1,
                                             const u32x4 xv, const u32x4 nv, const int slot) {
  const float* rsel = (const float*)(ws + OFF_SEL) + 8;
  const float* yp = (const float*)(ws + OFF_YP);
  float a[8] = {a0.x, a0.y, a0.z, a0.w, a1.x, a1.y, a1.z, a1.w};
  float xf[8] = {bflo(xv.x), bfhi(xv.x), bflo(xv.y), bfhi(xv.y), bflo(xv.z), bfhi(xv.z), bflo(xv.w), bfhi(xv.w)};
  float nf[8] = {bflo(nv.x), bfhi(nv.x), bflo(nv.y), bfhi(nv.y), bflo(nv.z), bfhi(nv.z), bflo(nv.w), bfhi(nv.w)};
  if (slot >= 0) {
    const float rs = rsel[slot];
#pragma unroll
    for (int j = 0; j < 8; ++j) {
      float y = 0.f;
#pragma unroll
      for (int kb = 0; kb < 16; ++kb) y += yp[((size_t)kb * 8 + slot) * DM + n0 + j];
      y = bfr(bfr(y) * rs);
      nf[j] = bfr(nf[j] + y);
    }
  }
  float o[8];
#pragma unroll
  for (int j = 0; j < 8; ++j) o[j] = bfr(xf[j] + bfr(a[j])) + nf[j];
  u32x4 ov = {pack2(o[0], o[1]), pack2(o[2], o[3]), pack2(o[4], o[5]), pack2(o[6], o[7])};
  *(u32x4*)(out + (size_t)tok * DM + n0) = ov;
}

__device__ __forceinline__ void worker_wo_fine(const u16* __restrict__ x, char* __restrict__ ws, u16* __restrict__ out, char* smem, const int par, const int np,
                                               const int sg0, const int sgstep) {
  const int tid = otid(), w = __builtin_amdgcn_readfirstlane(tid >> 6), l = tid & 63;
  const int c = l & 15, g = l >> 4;
  const char* wof = ws + OFF_WOF + ((size_t)(2 * np) * 128 + 32 * w) * 1024 + l * 16;
  bf16x8 b0[32], b1[32];
  const int rot = (np * 8) / 5;
#pragma unroll
  for (int i = 0; i < 32; ++i) { const int kk = (i + rot) & 31; b0[i] = *(const bf16x8*)(wof + kk * 1024); b1[i] = *(const bf16x8*)(wof + 131072 + kk * 1024); }
  const __amdgpu_buffer_rsrc_t rof = __builtin_amdgcn_make_buffer_rsrc(ws + OFF_OF, 0, (u32)SZ_Q, 0x00020000);
  const u16* xn = (const u16*)(ws + OFF_XN);
  int sel[8];
  if (w == 0) {
    wait_count((const u32*)(ws + OFF_YCNT), 80u, (u32*)(ws + OFF_ERR));
    __builtin_amdgcn_fence(__ATOMIC_ACQUIRE, "agent");
#pragma unroll
    for (int k = 0; k < 8; ++k) sel[k] = __builtin_amdgcn_readfirstlane(((const int*)(ws + OFF_SEL))[k]);
  }
  int it = 0, ready = -1;
  for (int sg = sg0; sg < 64; sg += sgstep, ++it) {
    const int mt = 2 * sg + par;
    const int tok = 32 * sg + p32(par, c), n0 = 32 * np + 8 * g;
    u32x4 xv, nv;
    if (w == 0) { xv = *(const u32x4*)(x + (size_t)tok * DM + n0); nv = *(const u32x4*)(xn + (size_t)tok * DM + n0); }
    if (w == 1 && sg > ready) {
      if (sg < WO_FAST) wait_count<WO_SLP>((const u32*)(ws + OFF_OSEG) + sg * 16, 32u, (u32*)(ws + OFF_ERR));
      else wait_count<4>((const u32*)(ws + OFF_OSEG) + sg * 16, 32u, (u32*)(ws + OFF_ERR));
      const u32 cv = __hip_atomic_load((const u32*)(ws + OFF_OSEG) + l * 16, __ATOMIC_RELAXED, __HIP_MEMORY_SCOPE_AGENT);
      const unsigned long long nok = ~(__builtin_amdgcn_ballot_w64(cv >= 32u) >> sg) & ~1ull;
      ready = sg + (nok ? __builtin_ctzll(nok) : 64) - 1;
    }
    __syncthreads();
    f32x4 acc0, acc1;
    const u32 abase = (u32)(((mt * 128 + 32 * w) * 64 + l) * 16);
    u32x4 a[32];
#pragma unroll
    for (int i = 0; i < 32; ++i) {
      a[i] = __builtin_amdgcn_raw_buffer_load_b128(rof, abase, ((i + rot) & 31) * 1024, 16);
    }
    __builtin_amdgcn_sched_barrier(0);
    MFMA_A0(acc0, b0[0], a[0]); MFMA_A0(acc1, b1[0], a[0]);
#pragma unroll
    for (int i = 1; i < 32; ++i) { MFMA_A(acc0, b0[i], a[i]); MFMA_A(acc1, b1[i], a[i]); }
    asm volatile("s_nop 7\n\ts_nop 7" : "+v"(acc0), "+v"(acc1));
    char* sb = smem + (it & 1) * 8192;
    if (w > 0) {
      *(f32x4*)(sb + (w * 2) * 1024 + l * 16) = acc0;
      *(f32x4*)(sb + (w * 2 + 1) * 1024 + l * 16) = acc1;
    }
    __syncthreads();
    if (w == 0) {
      const f32x4* p = (const f32x4*)(sb + l * 16);
      acc0 = (acc0 + p[2 * 64]) + (p[4 * 64] + p[6 * 64]);
      acc1 = (acc1 + p[3 * 64]) + (p[5 * 64] + p[7 * 64]);
      int slot = -1;
#pragma unroll
      for (int k = 0; k < 8; ++k) if (sel[k] == tok) slot = k;
      final_store2(ws, out, tok, n0, acc0, acc1, xv, nv, slot);
    }
  }
}

#define WSTAMP(i)
template <typename T> __device__ __forceinline__ T* opq(T* p) { asm volatile("" : "+s"(p)); return p; }

__global__ __launch_bounds__(256, 1) void main_kernel(const u16* __restrict__ x, const u16* __restrict__ wg, const u16* __restrict__ wu,
                                                      const u16* __restrict__ wd, char* __restrict__ ws, u16* __restrict__ out) {
  extern __shared__ __attribute__((aligned(16))) char smem[];
  const int b = blockIdx.x;
  if (b < 128) {
    chain_role(ws, smem, b);
  } else {
    const int j = b - 128;
    for (int i = 0; i < 4; ++i) {
      worker_kv(opq(ws), smem, 4 * i + (j >> 5), j & 31);
      WSTAMP(i);
      if (i == 0 && j < 108) worker_modA(opq(wg), opq(wu), opq(ws), smem, j);
      if (i == 1 && j < 80) worker_modB(opq(wd), opq(ws), smem, j);
    }
    if (j < 40) {
      worker_wo_fine(opq(x), opq(ws), out, smem, j / 20, j % 20, 0, 1);
    } else {
      for (int a = j - 40; a < 512; a += 88) { worker_qattn(opq(ws), smem, a >> 5, a & 31); WSTAMP(4 + a / 88); }
    }
    WSTAMP(15);
  }
}

extern "C" void kernel_launch(void* const* d_in, const int* in_sizes, int n_in, void* d_out, int out_size, void* d_ws, size_t ws_size,
                              hipStream_t stream) {
  const u16* x = (const u16*)d_in[0];
  const u16* mem0 = (const u16*)d_in[1];
  const u16* Wq = (const u16*)d_in[2];
  const u16* Wk = (const u16*)d_in[3];
  const u16* Wv = (const u16*)d_in[4];
  const u16* Wo = (const u16*)d_in[5];
  const u16* w_r = (const u16*)d_in[8];
  const u16* wg = (const u16*)d_in[9];
  const u16* wu = (const u16*)d_in[10];
  const u16* wd = (const u16*)d_in[11];
  char* ws = (char*)d_ws;
  u16* out = (u16*)d_out;
  (void)hipFuncSetAttribute((const void*)main_kernel, hipFuncAttributeMaxDynamicSharedMemorySize, CH_LDS);
  prep_kernel<<<PREP_BLOCKS, 256, 0, stream>>>(x, mem0, Wq, Wk, Wv, Wo, w_r, ws);
  main_kernel<<<256, 256, CH_LDS, stream>>>(x, wg, wu, wd, ws, out);
}
```

```cpp
#include <hip/hip_runtime.h>
#include <hip/hip_bf16.h>
#include <stdint.h>

constexpr int S_TOK = 2048, DM = 640, NH = 32, DH = 128, HD = 4096, NMEM = 8, SEG = 32, NSEG = 64;
constexpr int FF = 1728, CSEL = 8;
constexpr int KS_DM = DM / 32;
constexpr int KS_HD = HD / 32;

typedef unsigned int u32;
typedef unsigned short u16;
typedef __attribute__((ext_vector_type(4))) u32 u32x4;
typedef __attribute__((ext_vector_type(2))) u32 u32x2;
typedef __attribute__((ext_vector_type(4))) float f32x4;
typedef __attribute__((ext_vector_type(8))) short bf16x8;
typedef __attribute__((ext_vector_type(4))) short bf16x4;
typedef __attribute__((ext_vector_type(2))) __bf16 nbf16x2;

#define SENT 0xFFFFFFFFu

__device__ __forceinline__ float bf2f(u16 v) { return __uint_as_float(((u32)v) << 16); }
__device__ __forceinline__ float bflo(u32 v) { return __uint_as_float(v << 16); }
__device__ __forceinline__ float bfhi(u32 v) { return __uint_as_float(v & 0xffff0000u); }
__device__ __forceinline__ u32 pack2(float a, float b) {
  nbf16x2 v = {(__bf16)a, (__bf16)b};
  return __builtin_bit_cast(u32, v);
}
__device__ __forceinline__ float bfr(float a) { return (float)(__bf16)a; }
__device__ __forceinline__ bf16x8 as_bf8(u32x4 v) { return __builtin_bit_cast(bf16x8, v); }
__device__ __forceinline__ bf16x4 as_bf4(u32x2 v) { return __builtin_bit_cast(bf16x4, v); }
__device__ __forceinline__ f32x4 mfma32(bf16x8 a, bf16x8 b, f32x4 c) { return __builtin_amdgcn_mfma_f32_16x16x32_bf16(a, b, c, 0, 0, 0); }
__device__ __forceinline__ f32x4 mfma16(bf16x4 a, bf16x4 b, f32x4 c) {
  u32x2 a2 = __builtin_bit_cast(u32x2, a), b2 = __builtin_bit_cast(u32x2, b);
  u32x4 a4 = {a2.x, a2.y, 0u, 0u}, b4 = {b2.x, b2.y, 0u, 0u};
  return __builtin_amdgcn_mfma_f32_16x16x32_bf16(__builtin_bit_cast(bf16x8, a4), __builtin_bit_cast(bf16x8, b4), c, 0, 0, 0);
}
__device__ __forceinline__ float xr16_max(float v) { auto r = __builtin_amdgcn_permlane16_swap(__float_as_uint(v), __float_as_uint(v), false, false); return fmaxf(__uint_as_float(r[0]), __uint_as_float(r[1])); }
__device__ __forceinline__ float xr32_max(float v) { auto r = __builtin_amdgcn_permlane32_swap(__float_as_uint(v), __float_as_uint(v), false, false); return fmaxf(__uint_as_float(r[0]), __uint_as_float(r[1])); }
__device__ __forceinline__ float xr16_add(float v) { auto r = __builtin_amdgcn_permlane16_swap(__float_as_uint(v), __float_as_uint(v), false, false); return __uint_as_float(r[0]) + __uint_as_float(r[1]); }
__device__ __forceinline__ float xr32_add(float v) { auto r = __builtin_amdgcn_permlane32_swap(__float_as_uint(v), __float_as_uint(v), false, false); return __uint_as_float(r[0]) + __uint_as_float(r[1]); }
__device__ __forceinline__ float xr8_max(float v) { return fmaxf(v, __uint_as_float(__builtin_amdgcn_update_dpp(0u, __float_as_uint(v), 0x128  , 0xf, 0xf, false))); }
__device__ __forceinline__ float xr8_add(float v) { return v + __uint_as_float(__builtin_amdgcn_update_dpp(0u, __float_as_uint(v), 0x128  , 0xf, 0xf, false)); }
__device__ __forceinline__ int p32(int T, int r) { return 8 * (r >> 2) + 4 * T + (r & 3); }

__device__ __forceinline__ u32x4 sel_op(bool on, int pos) {
  const u32 one = on ? ((pos & 1) ? 0x3f800000u : 0x3f80u) : 0u;
  const int d = pos >> 1;
  return (u32x4){d == 0 ? one : 0u, d == 1 ? one : 0u, d == 2 ? one : 0u, d == 3 ? one : 0u};
}
__device__ __forceinline__ void tr_half2(u32x4 a, u32x4 s, u32x4 b, u32x4 t, u32x2& x, u32x2& y) {
  f32x4 d, e;
  asm volatile("s_nop 1\n\tv_mfma_f32_16x16x32_bf16 %0, %2, %3, 0\n\tv_mfma_f32_16x16x32_bf16 %1, %4, %5, 0\n\ts_nop 7\n\ts_nop 3"
               : "=&v"(d), "=&v"(e) : "v"(a), "v"(s), "v"(b), "v"(t));
  x = (u32x2){pack2(d.x, d.y), pack2(d.z, d.w)};
  y = (u32x2){pack2(e.x, e.y), pack2(e.z, e.w)};
}
__device__ __forceinline__ void tr_frag2(u32x4 a0, u32x4 a1, u32x4 s0, u32x4 s1, u32x4& f0, u32x4& f1) {
  f32x4 d0, d1, d2, d3;
  asm volatile("s_nop 1\n\tv_mfma_f32_16x16x32_bf16 %0, %4, %6, 0\n\tv_mfma_f32_16x16x32_bf16 %1, %5, %6, 0\n\t"
               "v_mfma_f32_16x16x32_bf16 %2, %4, %7, 0\n\tv_mfma_f32_16x16x32_bf16 %3, %5, %7, 0\n\ts_nop 7\n\ts_nop 3"
               : "=&v"(d0), "=&v"(d1), "=&v"(d2), "=&v"(d3) : "v"(a0), "v"(a1), "v"(s0), "v"(s1));
  f0 = (u32x4){pack2(d0.x, d0.y), pack2(d0.z, d0.w), pack2(d1.x, d1.y), pack2(d1.z, d1.w)};
  f1 = (u32x4){pack2(d2.x, d2.y), pack2(d2.z, d2.w), pack2(d3.x, d3.y), pack2(d3.z, d3.w)};
}

__host__ __device__ __forceinline__ int piece_col(int p, int t) {
  const int w = p / 20, r = p % 20;
  const int blk = r < 16 ? 2 * w + (r >> 3) : 8 + (w >> 1), sel = r < 16 ? (r >> 2) & 1 : w & 1, g = r & 3;
  return 64 * blk + 16 * g + 4 * (t >> 1) + 2 * sel + (t & 1);
}

constexpr size_t SZ_XN = (size_t)S_TOK * DM * 2;
constexpr size_t OFF_XN = 0;
constexpr size_t OFF_XNF = OFF_XN + SZ_XN;
constexpr size_t OFF_R = OFF_XNF + SZ_XN;
constexpr size_t OFF_WF = OFF_R + 8192;
constexpr size_t SZ_WF = (size_t)768 * 20 * 1024;
constexpr size_t OFF_WOF = OFF_WF + SZ_WF;
constexpr size_t SZ_WOF = (size_t)40 * 128 * 1024;
constexpr size_t OFF_Q = OFF_WOF + SZ_WOF;
constexpr size_t SZ_Q = (size_t)S_TOK * HD * 2;
constexpr size_t OFF_K = OFF_Q + SZ_Q;
constexpr size_t OFF_VT = OFF_K + SZ_Q;
constexpr size_t OFF_KM = OFF_VT + SZ_Q;
constexpr size_t SZ_KM = (size_t)NSEG * NMEM * HD * 2;
constexpr size_t OFF_VMT = OFF_KM + SZ_KM;
constexpr size_t OFF_OF = OFF_VMT + SZ_KM;
constexpr size_t OFF_INBOX = OFF_OF + SZ_Q;
constexpr u32 NPROD = 128, NRED = 80;
constexpr u32 INBOX_SLOT = NRED * NPROD * 128;
constexpr size_t OFF_MEMB = OFF_INBOX + 2 * (size_t)INBOX_SLOT;
constexpr u32 MREP = 8;
constexpr u32 MEM_REP = NRED * 128;
constexpr u32 MEM_STEP = MREP * MEM_REP;
constexpr size_t SZ_MEMB = (size_t)65 * MEM_STEP;
constexpr size_t OFF_HB = OFF_MEMB + ((SZ_MEMB + 255) / 256) * 256;
constexpr size_t OFF_YP = OFF_HB + 32768;
constexpr size_t OFF_SEL = OFF_YP + (size_t)16 * 8 * 640 * 4;
constexpr size_t OFF_ERR = OFF_SEL + 256;
constexpr size_t OFF_MCNT = OFF_ERR + 256;
constexpr size_t OFF_OSEG = OFF_MCNT + (size_t)64 * 32 * 64;
constexpr size_t OFF_KVC = OFF_OSEG + 64 * 64;
constexpr size_t OFF_HCNT = OFF_KVC + (size_t)64 * 32 * 64;
constexpr size_t OFF_YCNT = OFF_HCNT + 256;
constexpr size_t OFF_FLAG_END = OFF_YCNT + 256;
constexpr size_t WS_NEED = OFF_FLAG_END;

constexpr int PREP_LN_BLOCKS = S_TOK / 4;
constexpr int PREP_INIT_BLOCKS = 128;
constexpr int PREP_BLOCKS = PREP_LN_BLOCKS + PREP_INIT_BLOCKS;

__global__ __launch_bounds__(256) void prep_kernel(const u16* __restrict__ x, const u16* __restrict__ mem0,
                                                   const u16* __restrict__ w_r, char* __restrict__ ws) {
  const int b = blockIdx.x, tid = threadIdx.x;
  if (b < PREP_LN_BLOCKS) {
    const int w = tid >> 6, l = tid & 63;
    const int t = b * 4 + w;
    const u16* xr = x + (size_t)t * DM;
    u32x4 c0 = *(const u32x4*)(xr + l * 8);
    u32x4 c1 = {0, 0, 0, 0};
    if (l < 16) c1 = *(const u32x4*)(xr + 512 + l * 8);
    float v[16];
    v[0] = bflo(c0.x); v[1] = bfhi(c0.x); v[2] = bflo(c0.y); v[3] = bfhi(c0.y); v[4] = bflo(c0.z); v[5] = bfhi(c0.z); v[6] = bflo(c0.w); v[7] = bfhi(c0.w);
    v[8] = bflo(c1.x); v[9] = bfhi(c1.x); v[10] = bflo(c1.y); v[11] = bfhi(c1.y); v[12] = bflo(c1.z); v[13] = bfhi(c1.z); v[14] = bflo(c1.w); v[15] = bfhi(c1.w);
    float s = 0.f;
#pragma unroll
    for (int j = 0; j < 16; ++j) s += v[j];
#pragma unroll
    for (int o = 32; o > 0; o >>= 1) s += __shfl_xor(s, o);
    const float mean = s * (1.0f / DM);
    float q = 0.f;
#pragma unroll
    for (int j = 0; j < 8; ++j) { float d = v[j] - mean; q += d * d; }
    if (l < 16) {
#pragma unroll
      for (int j = 8; j < 16; ++j) { float d = v[j] - mean; q += d * d; }
    }
#pragma unroll
    for (int o = 32; o > 0; o >>= 1) q += __shfl_xor(q, o);
    const float var_b = bfr(q * (1.0f / DM));
    const float mean_b = bfr(mean);
    const float den = bfr(sqrtf(bfr(var_b + 1e-05f)));
    float y[16];
#pragma unroll
    for (int j = 0; j < 16; ++j) y[j] = bfr(bfr(v[j] - mean_b) / den);
    u32x4 w0 = *(const u32x4*)(w_r + l * 8);
    u32x4 w1 = {0, 0, 0, 0};
    if (l < 16) w1 = *(const u32x4*)(w_r + 512 + l * 8);
    float rs = y[0] * bflo(w0.x) + y[1] * bfhi(w0.x) + y[2] * bflo(w0.y) + y[3] * bfhi(w0.y) + y[4] * bflo(w0.z) + y[5] * bfhi(w0.z) + y[6] * bflo(w0.w) + y[7] * bfhi(w0.w);
    if (l < 16) rs += y[8] * bflo(w1.x) + y[9] * bfhi(w1.x) + y[10] * bflo(w1.y) + y[11] * bfhi(w1.y) + y[12] * bflo(w1.z) + y[13] * bfhi(w1.z) + y[14] * bflo(w1.w) + y[15] * bfhi(w1.w);
#pragma unroll
    for (int o = 32; o > 0; o >>= 1) rs += __shfl_xor(rs, o);
    if (l == 0) ((float*)(ws + OFF_R))[t] = bfr(rs);
    u16* xn = (u16*)(ws + OFF_XN);
    u16* xnf = (u16*)(ws + OFF_XNF);
    const int sg = t >> 5, tt = t & 31;
    const int mt = 2 * sg + ((tt >> 2) & 1), r = 4 * (tt >> 3) + (tt & 3);
    u32x4 o0 = {pack2(y[0], y[1]), pack2(y[2], y[3]), pack2(y[4], y[5]), pack2(y[6], y[7])};
    *(u32x4*)(xn + (size_t)t * DM + l * 8) = o0;
    { int c = l; *(u32x4*)(xnf + (((size_t)mt * 20 + (c >> 2)) * 64 + (c & 3) * 16 + r) * 8) = o0; }
    if (l < 16) {
      u32x4 o1 = {pack2(y[8], y[9]), pack2(y[10], y[11]), pack2(y[12], y[13]), pack2(y[14], y[15])};
      *(u32x4*)(xn + (size_t)t * DM + 512 + l * 8) = o1;
      int c = 64 + l;
      *(u32x4*)(xnf + (((size_t)mt * 20 + (c >> 2)) * 64 + (c & 3) * 16 + r) * 8) = o1;
    }
  } else {
    int i = b - PREP_LN_BLOCKS;
    u32x4 sv = {SENT, SENT, SENT, SENT};
    u32x4* p = (u32x4*)(ws + OFF_INBOX);
    const size_t n16 = (2 * (size_t)INBOX_SLOT) / 16;
    for (size_t j = (size_t)i * 256 + tid; j < n16; j += (size_t)PREP_INIT_BLOCKS * 256) p[j] = sv;
    u32x4* pm = (u32x4*)(ws + OFF_MEMB + MEM_STEP);
    const size_t m16 = (size_t)64 * MEM_STEP / 16;
    for (size_t j = (size_t)i * 256 + tid; j < m16; j += (size_t)PREP_INIT_BLOCKS * 256) pm[j] = sv;
    if (i == 0) {
      for (int j = tid; j < 640; j += 256) {
        int p_ = j >> 3, m = j & 7;
        u32x4 v = {*(const u32*)(mem0 + m * DM + piece_col(p_, 0)), *(const u32*)(mem0 + m * DM + piece_col(p_, 2)),
                   *(const u32*)(mem0 + m * DM + piece_col(p_, 4)), *(const u32*)(mem0 + m * DM + piece_col(p_, 6))};
        if (v.x == SENT) v.x = 0xFFC0FFC0u;
        if (v.y == SENT) v.y = 0xFFC0FFC0u;
        if (v.z == SENT) v.z = 0xFFC0FFC0u;
        if (v.w == SENT) v.w = 0xFFC0FFC0u;
        for (u32 rp = 0; rp < MREP; ++rp) *(u32x4*)(ws + OFF_MEMB + rp * MEM_REP + (size_t)j * 16) = v;
      }
      if (tid == 0) *(u32*)(ws + OFF_ERR) = 0;
    }
    {
      u32x4* pf = (u32x4*)(ws + OFF_MCNT);
      for (int j = i * 256 + tid; j < (int)((OFF_FLAG_END - OFF_MCNT) / 16); j += PREP_INIT_BLOCKS * 256) pf[j] = (u32x4){0, 0, 0, 0};
    }
  }
}

__device__ __forceinline__ void glds16(const void* g, void* l) {
  __builtin_amdgcn_global_load_lds((const __attribute__((address_space(1))) void*)g, (__attribute__((address_space(3))) void*)l, 16, 0, 0);
}

constexpr u32 SPIN_CAP = 1u << 16;
#define HMAP 0
#define KV_DELAY 0
#define WO_SC1
#define SLP_NR 40
#define SLP_RI 6
#define SLP_D1 10
#define SLP_D2 10
#define WO_SLP 40
#define WO_FAST 63
#define SLP_RM 8
__device__ __forceinline__ bool has_sent(u32x4 v) { return (v.x == SENT) | (v.y == SENT) | (v.z == SENT) | (v.w == SENT); }
__device__ __forceinline__ u32 desent(float f) { u32 b = __float_as_uint(f); return b == SENT ? 0x7FC00000u : b; }

template <int SLP = 8>
__device__ __forceinline__ void wait_count(const u32* p, u32 want, u32* err) {
  u32 spins = 0;
  for (;;) {
    u32 v = __builtin_amdgcn_readfirstlane(__hip_atomic_load(p, __ATOMIC_RELAXED, __HIP_MEMORY_SCOPE_AGENT));
    if (v >= want) break;
    __builtin_amdgcn_s_sleep(SLP);
    if (++spins > (1u << 20)) { *err = 3; break; }
  }
}

__device__ __forceinline__ u32x4 mk_rsrc(const void* p, u32 n) {
  const unsigned long long a = (unsigned long long)p;
  return (u32x4){(u32)__builtin_amdgcn_readfirstlane((u32)a), (u32)__builtin_amdgcn_readfirstlane((u32)(a >> 32) & 0xffffu), n, 0x00020000u};
}

constexpr int CH_MEMF = 0, CH_PART = 20480, CH_WV = 40960, CH_WO = 81920, CH_SRED = 122880, CH_LDS = 123904;

#define MFMA_A(acc, wa, mb) asm volatile("v_mfma_f32_16x16x32_bf16 %0, %1, %2, %0" : "+v"(acc) : "a"(wa), "v"(mb))
#define MFMA_V(acc, wa, mb) asm volatile("v_mfma_f32_16x16x32_bf16 %0, %1, %2, %0" : "+v"(acc) : "v"(wa), "v"(mb))
#define MFMA_A0(acc, wa, mb) asm volatile("v_mfma_f32_16x16x32_bf16 %0, %1, %2, 0" : "=&v"(acc) : "a"(wa), "v"(mb))
#define MFMA_V0(acc, wa, mb) asm volatile("v_mfma_f32_16x16x32_bf16 %0, %1, %2, 0" : "=&v"(acc) : "v"(wa), "v"(mb))
#define MFMA_NV(acc, wa, mb) asm volatile("s_nop 1\n\tv_mfma_f32_16x16x32_bf16 %0, %1, %2, %0" : "+v"(acc) : "v"(wa), "v"(mb))
#define MFMA_NV0(acc, wa, mb) asm volatile("s_nop 1\n\tv_mfma_f32_16x16x32_bf16 %0, %1, %2, 0" : "=&v"(acc) : "v"(wa), "v"(mb))

#define SMF_A(acc, a, b) asm volatile("v_smfmac_f32_16x16x64_bf16 %0, %1, %2, %3" : "+v"(acc) : "v"(a), "a"(b), "v"(sidx))
#define SMF_V(acc, a, b) asm volatile("v_smfmac_f32_16x16x64_bf16 %0, %1, %2, %3" : "+v"(acc) : "v"(a), "v"(b), "v"(sidx))
#define MFMA16_N0(acc, a, b) asm volatile("s_nop 1\n\tv_mfma_f32_16x16x16_bf16 %0, %1, %2, 0" : "=&v"(acc) : "v"(a), "v"(b))
#define SMF_P(R, C, acc, a, b) asm volatile("v_smfmac_f32_16x16x64_bf16 %0, %1, %2, %3" : "+{" R "}"(acc) : "v"(a), C(b), "v"(sidx))
#define SQ0(C, a, b) SMF_P("v[208:211]", C, aq0, a, b)
#define SQ1(C, a, b) SMF_P("v[212:215]", C, aq1, a, b)
#define SK0(C, a, b) SMF_P("v[216:219]", C, ak0, a, b)
#define SK1(C, a, b) SMF_P("v[220:223]", C, ak1, a, b)
typedef __attribute__((ext_vector_type(8))) u32 u32x8;
typedef __attribute__((ext_vector_type(2))) float f32x2;
constexpr int PSTR = 4160;
__device__ __forceinline__ float hsum32(float a, float b) { auto r = __builtin_amdgcn_permlane32_swap(__float_as_uint(a), __float_as_uint(b), false, false); return __uint_as_float(r[0]) + __uint_as_float(r[1]); }
__device__ __forceinline__ u32 ror8(u32 v) { return (u32)__builtin_amdgcn_update_dpp(0u, v, 0x128  , 0xf, 0xf, false); }

#define STAMP(i)
#define CST(i)
__device__ __forceinline__ void chain_role(const u16* __restrict__ Wq, const u16* __restrict__ Wk, const u16* __restrict__ Wv, const u16* __restrict__ Wo,
                                           char* __restrict__ ws, char* smem, const int c) {
  const int tid = threadIdx.x, w = __builtin_amdgcn_readfirstlane(tid >> 6), l = tid & 63;
  const int h = (c & 7) | ((c >> 5) << 3), gs = (c >> 3) & 3;
  const int m = l & 15, g = l >> 4;
  u32x8 wq[2][10], wk[2][10];
  {
    int l = tid & 63;
    asm volatile("" : "+v"(l));
    const int m = l & 15, g = l >> 4;
    const u32x4 S0 = sel_op(g == (m >> 2), m & 3), S1 = sel_op(g == (m >> 2), 4 + (m & 3));
    const int lo_ = p32(0, m) * HD + h * 128 + 8 * g;
    u32x4 rv[10], ro[12], rb[5][8];
    u32 pfd = 0;
    {
      const int o = ((160 * gs + 40 * w + (l >> 2)) * HD + h * 128 + 32 * (l & 3)) * 2;
#pragma unroll
      for (int i = 0; i < 3; ++i) {
        const u32 oo = i < 2 || l < 32 ? (u32)(o + i * 16 * HD * 2) : 0u;
        asm volatile("global_load_dword %0, %1, %2\n\tglobal_load_dword %0, %1, %3\n\tglobal_load_dword %0, %1, %4" : "+v"(pfd) : "v"(oo), "s"(Wq), "s"(Wk), "s"(Wv) : "memory");
      }
    }
#pragma unroll
    for (int i = 0; i < 5; ++i) {
      const u16* p = Wv + lo_ + 32 * gs + (5 * w + i) * 32 * HD;
      rv[2 * i] = *(const u32x4*)(p); rv[2 * i + 1] = *(const u32x4*)(p + 4 * HD);
    }
#pragma unroll
    for (int f = 0; f < 10; ++f) {
      const int u = f >> 1;
      const u16* p = Wo + (h * 128 + 32 * gs + 16 * (f & 1) + m) * DM;
      if (u < 4) ro[f] = *(const u32x4*)(p + 64 * (2 * w + (u >> 1)) + 16 * g + 8 * (u & 1));
      else { ro[f] = *(const u32x4*)(p + 64 * (8 + (w >> 1)) + 8 * g); ro[f + 2] = *(const u32x4*)(p + 64 * (8 + (w >> 1)) + 32 + 8 * g); }
    }
    auto ldb = [&](int i) {
      const int blk = i < 8 ? 2 * ((w + (i >> 1)) & 3) + (i & 1) : i;
      const int o = lo_ + 32 * w + blk * 64 * HD;
      u32x4* r = rb[i % 5];
      r[0] = *(const u32x4*)(Wq + o); r[1] = *(const u32x4*)(Wq + o + 4 * HD); r[2] = *(const u32x4*)(Wq + o + 32 * HD); r[3] = *(const u32x4*)(Wq + o + 36 * HD);
      r[4] = *(const u32x4*)(Wk + o); r[5] = *(const u32x4*)(Wk + o + 4 * HD); r[6] = *(const u32x4*)(Wk + o + 32 * HD); r[7] = *(const u32x4*)(Wk + o + 36 * HD);
    };
    __builtin_amdgcn_sched_barrier(0);
#pragma unroll
    for (int i = 0; i < 5; ++i) {
      u32x4 f0, f1;
      tr_frag2(rv[2 * i], rv[2 * i + 1], S0, S1, f0, f1);
      *(u32x4*)(smem + CH_WV + (5 * w + i) * 1024 + l * 16) = f0;
      *(u32x4*)(smem + CH_WV + (20 + 5 * w + i) * 1024 + l * 16) = f1;
    }
    __builtin_amdgcn_sched_barrier(0);
    asm volatile("" ::"v"(pfd));
    ldb(0); ldb(1); ldb(2);
    ldb(3);
    __builtin_amdgcn_sched_barrier(0);
    {
      const u32x4 P0 = sel_op(g == (m >> 2), 4 * ((m >> 1) & 1) + (m & 1)), P1 = sel_op(g == (m >> 2), 4 * ((m >> 1) & 1) + 2 + (m & 1));
      const u32x4 P4 = sel_op(g == 2 * (m >> 3) + ((m & 7) >> 2), 4 * ((m >> 1) & 1) + 2 * (w & 1) + (m & 1));
#pragma unroll
      for (int f = 0; f < 10; ++f) {
        u32x2 x0, x1;
        tr_half2(ro[f], f < 8 ? P0 : P4, ro[f < 8 ? f : f + 2], f < 8 ? P1 : P4, x0, x1);
        *(u32x4*)(smem + CH_WO + (10 * w + f) * 1024 + l * 16) = (u32x4){x0.x, x1.x, x0.y, x1.y};
      }
    }
    __builtin_amdgcn_sched_barrier(0);
    ldb(4);
#pragma unroll
    for (int i = 0; i < 10; ++i) {
      __builtin_amdgcn_sched_barrier(0);
      const u32x4* r = rb[i % 5];
      auto j8 = [](u32x4 lo, u32x4 hi) { return (u32x8){lo.x, lo.y, lo.z, lo.w, hi.x, hi.y, hi.z, hi.w}; };
      u32x4 f[8];
      tr_frag2(r[0], r[1], S0, S1, f[0], f[1]); tr_frag2(r[2], r[3], S0, S1, f[2], f[3]);
      tr_frag2(r[4], r[5], S0, S1, f[4], f[5]); tr_frag2(r[6], r[7], S0, S1, f[6], f[7]);
      wq[0][i] = j8(f[0], f[2]); wq[1][i] = j8(f[1], f[3]); wk[0][i] = j8(f[4], f[6]); wk[1][i] = j8(f[5], f[7]);
      if (i < 8) asm volatile("" : "+a"(wq[0][i]), "+a"(wq[1][i]), "+a"(wk[0][i]), "+a"(wk[1][i]));
      else asm volatile("" : "+v"(wq[0][i]), "+v"(wq[1][i]), "+v"(wk[0][i]), "+v"(wk[1][i]));
      __builtin_amdgcn_sched_barrier(0);
      if (i + 5 < 10) ldb(i + 5);
    }
    __builtin_amdgcn_sched_barrier(0);
  }
  if (tid < 4) *(f32x4*)(smem + CH_PART + tid * PSTR + 4096) = (f32x4){-INFINITY, -INFINITY, -INFINITY, -INFINITY};
  __syncthreads();
  u32 sidx2 = (w & 1) ? 0xEEEEu : 0x4444u;
  u32 sidx = m < 8 ? 0x4444u : 0xEEEEu;
  asm volatile("" : "+v"(sidx), "+v"(sidx2));
  const u32 one_at = (m & 1) ? 0x3f800000u : 0x3f80u;
  const u32x2 idn = {(g == (m >> 2) && !(m & 2)) ? one_at : 0u, (g == (m >> 2) && (m & 2)) ? one_at : 0u};

  const __amdgpu_buffer_rsrc_t rin = __builtin_amdgcn_make_buffer_rsrc(ws + OFF_INBOX, 0, 2 * INBOX_SLOT, 0x00020000);
  const __amdgpu_buffer_rsrc_t rmem = __builtin_amdgcn_make_buffer_rsrc(ws + OFF_MEMB, 0, (u32)SZ_MEMB, 0x00020000);
  const u32x4 din = mk_rsrc(ws + OFF_INBOX, 2 * INBOX_SLOT), dmem = mk_rsrc(ws + OFF_MEMB, (u32)SZ_MEMB), dkv = mk_rsrc(ws + OFF_K, (u32)(2 * SZ_Q));
  const u32x4 dflag = mk_rsrc(ws + OFF_KVC, 64 * 32 * 64);
  const __amdgpu_buffer_rsrc_t rkm = __builtin_amdgcn_make_buffer_rsrc(ws + OFF_KM, 0, (u32)(2 * SZ_KM), 0x00020000);
  u32* mcnt = (u32*)(ws + OFF_MCNT);
  u32* err = (u32*)(ws + OFF_ERR);
  const float scale = 0.08838834764831845f;
  const char* lmem = smem + CH_MEMF + l * 16;

  u32x4 qt[4], mfo[3];
  u32 qf = 0;
#pragma unroll
  for (int i = 0; i < 4; ++i) qt[i] = (u32x4){0, 0, 0, 0};
#pragma unroll
  for (int i = 0; i < 3; ++i) mfo[i] = (u32x4){0, 0, 0, 0};
  u32x4 kv4[4];
  const u32 kvo0 = (u32)((p32(0, m) * HD + h * 128 + 32 * w + 8 * g) * 2), kvo2 = (u32)(SZ_Q + ((h * 128 + 32 * gs + p32(0, m)) * 32 + 8 * g) * 2);
  auto load_kv = [&](int sn) {
    const u32 so = (u32)sn * (32 * HD * 2);
    asm volatile("buffer_load_dwordx4 v[192:195], %[o0], %[rs], %[so] offen sc1\n\t"
                 "buffer_load_dwordx4 v[196:199], %[o0], %[rs], %[s1] offen sc1\n\t"
                 "buffer_load_dwordx4 v[200:203], %[o2], %[rs], %[so] offen sc1\n\t"
                 "buffer_load_dwordx4 v[204:207], %[o2], %[rs], %[so] offen offset:256 sc1"
                 : "=&{v[192:195]}"(kv4[0]), "=&{v[196:199]}"(kv4[1]), "=&{v[200:203]}"(kv4[2]), "=&{v[204:207]}"(kv4[3])
                 : [o0] "v"(kvo0), [o2] "v"(kvo2), [rs] "s"(dkv), [so] "s"(so), [s1] "s"(so + 4 * HD * 2)
                 : "memory");
  };
  const __amdgpu_buffer_rsrc_t rmc = __builtin_amdgcn_make_buffer_rsrc(ws + OFF_MCNT, 0, 64 * 32 * 64, 0x00020000);
  const u32 mco = tid == 0 ? (u32)h * 64 : 0x80000000u;
  const u32 rso = c < (int)NRED ? (u32)(c * NPROD * 128 + w * 4096 + l * 16) : 0x80000000u;
  wait_count((const u32*)(ws + OFF_KVC) + h * 16, 1u, err);
  load_kv(0);
  for (int s = 0; s < NSEG; ++s) {
    u32x8 wv[3][2];
#pragma unroll
    for (int j = 0; j < 3; ++j)
#pragma unroll
      for (int T = 0; T < 2; ++T) {
        const char* pw = smem + CH_WV + (T * 20 + 2 * (j < 2 ? 2 * w + j : 8 + (w >> 1))) * 1024 + l * 16;
        const u32x4 lo = *(const u32x4*)(pw), hi = *(const u32x4*)(pw + 1024);
        wv[j][T] = (u32x8){lo.x, lo.y, lo.z, lo.w, hi.x, hi.y, hi.z, hi.w};
      }
    f32x4 aq0, aq1, ak0, ak1, av0, av1;
    asm volatile("v_mov_b64 v[164:165], 0\n\tv_mov_b64 v[166:167], 0\n\tv_mov_b64 v[168:169], 0\n\tv_mov_b64 v[170:171], 0\n\t"
                 "v_mov_b64 v[208:209], 0\n\tv_mov_b64 v[210:211], 0\n\tv_mov_b64 v[212:213], 0\n\tv_mov_b64 v[214:215], 0\n\t"
                 "v_mov_b64 v[216:217], 0\n\tv_mov_b64 v[218:219], 0\n\tv_mov_b64 v[220:221], 0\n\tv_mov_b64 v[222:223], 0"
                 : "={v[208:211]}"(aq0), "={v[212:215]}"(aq1), "={v[216:219]}"(ak0), "={v[220:223]}"(ak1), "={v[164:167]}"(av0), "={v[168:171]}"(av1));
    {
      const u32 o = s * MEM_STEP + (c & (MREP - 1)) * MEM_REP + w * 2560 + (m >> 3) * 512 + g * 128 + (m & 7) * 16;
      const u32 o2 = m < 8 ? o : 0x80000000u;
      const u32 la = (u32)(uintptr_t)(__attribute__((address_space(3))) char*)smem + CH_MEMF + w * 2048 + l * 16;
      const u32 la2 = (u32)(uintptr_t)(__attribute__((address_space(3))) char*)smem + CH_MEMF + (m < 8 ? (8 + (w >> 1)) * 1024 + (g * 16 + m + 8 * (w & 1)) * 16 : 10240 + tid * 16);
      u32 tmx, cnt;
      unsigned long long m0, m1, m2, mo;
      const u32 fo = (u32)(min(s + 1, NSEG - 1) * 32 + h) * 64;
      asm volatile(
          "buffer_load_dwordx4 v[240:243], %[o], %[rs], 0 offen sc1\n\t"
          "buffer_load_dwordx4 v[244:247], %[o], %[rs], 0 offen offset:1024 sc1\n\t"
          "buffer_load_dwordx4 v[248:251], %[o2], %[rs], 0 offen offset:2048 sc1\n\t"
          "buffer_load_dword v252, off, %[rf], %[fo] sc1\n\t"
          "s_mov_b32 %[cnt], 0\n\t"
          "s_mov_b64 %[m0], -1\n\t"
          "s_mov_b64 %[m1], -1\n\t"
          "s_mov_b64 %[m2], -1\n\t"
          "s_sleep %[slp]\n"
          "1:\n\t"
          "s_mov_b64 exec, %[m0]\n\t"
          "buffer_load_dwordx4 v[240:243], %[o], %[rs], 0 offen sc1\n\t"
          "s_mov_b64 exec, %[m1]\n\t"
          "buffer_load_dwordx4 v[244:247], %[o], %[rs], 0 offen offset:1024 sc1\n\t"
          "s_mov_b64 exec, %[m2]\n\t"
          "buffer_load_dwordx4 v[248:251], %[o2], %[rs], 0 offen offset:2048 sc1\n\t"
          "s_mov_b64 exec, -1\n\t"
          "buffer_load_dword v252, off, %[rf], %[fo] sc1\n\t"
          "s_waitcnt vmcnt(4)\n\t"
          "v_max3_u32 %[t], v240, v241, v242\n\t"
          "v_max_u32 %[t], %[t], v243\n\t"
          "v_cmp_eq_u32 %[m0], -1, %[t]\n\t"
          "v_max3_u32 %[t], v244, v245, v246\n\t"
          "v_max_u32 %[t], %[t], v247\n\t"
          "v_cmp_eq_u32 %[m1], -1, %[t]\n\t"
          "v_max3_u32 %[t], v248, v249, v250\n\t"
          "v_max_u32 %[t], %[t], v251\n\t"
          "v_cmp_eq_u32 %[m2], -1, %[t]\n\t"
          "v_cmp_eq_u32 vcc, 0, v252\n\t"
          "s_or_b64 %[mo], %[m0], %[m1]\n\t"
          "s_or_b64 vcc, vcc, %[m2]\n\t"
          "s_or_b64 %[mo], %[mo], vcc\n\t"
          "s_cbranch_scc0 2f\n\t"
          "s_add_u32 %[cnt], %[cnt], 1\n\t"
          "s_cmp_lt_u32 %[cnt], 0x20000\n\t"
          "s_cbranch_scc1 1b\n"
          "2:\n\t"
          "ds_write_b128 %[la], v[240:243] offset:0\n\t"
          "ds_write_b128 %[la], v[244:247] offset:1024\n\t"
          "ds_write_b128 %[la2], v[248:251]"
          : "+{v[240:243]}"(mfo[0]), "+{v[244:247]}"(mfo[1]), "+{v[248:251]}"(mfo[2]), "+{v252}"(qf), [t] "=&v"(tmx), [cnt] "=&s"(cnt),
            [m0] "=&s"(m0), [m1] "=&s"(m1), [m2] "=&s"(m2), [mo] "=&s"(mo),
            "+{v[192:195]}"(kv4[0]), "+{v[196:199]}"(kv4[1]), "+{v[200:203]}"(kv4[2]), "+{v[204:207]}"(kv4[3])
          : [o] "v"(o), [o2] "v"(o2), [rs] "s"(dmem), [la] "v"(la), [la2] "v"(la2), [slp] "i"(SLP_D2), [rf] "s"(dflag), [fo] "s"(fo)
          : "vcc", "scc", "memory");
      if (cnt >= 0x20000u && l == 0) *err = 1;
    }
    STAMP(0);
    u32x8 wo[5];
    u32x4 kp;
    u32x2 vm0, vm1;
    auto ldwo = [&](int u) {
      const u32x4 lo = *(const u32x4*)(smem + CH_WO + (10 * w + 2 * u) * 1024 + l * 16), hi = *(const u32x4*)(smem + CH_WO + (10 * w + 2 * u + 1) * 1024 + l * 16);
      wo[u] = (u32x8){lo.x, lo.y, lo.z, lo.w, hi.x, hi.y, hi.z, hi.w};
    };
    {
      u32x4 mf[8];
      SMF_P("v[164:167]", "v", av0, mfo[0], wv[0][0]); SMF_P("v[168:171]", "v", av1, mfo[0], wv[0][1]);
      SMF_P("v[164:167]", "v", av0, mfo[1], wv[1][0]); SMF_P("v[168:171]", "v", av1, mfo[1], wv[1][1]);
      { const u32 sidx = sidx2; SMF_P("v[164:167]", "v", av0, mfo[2], wv[2][0]); SMF_P("v[168:171]", "v", av1, mfo[2], wv[2][1]); }
      asm volatile("s_waitcnt lgkmcnt(0)" ::: "memory");
      __syncthreads();
      STAMP(1);
      CST(0);
#pragma unroll
      for (int i = 0; i < 3; ++i) {
        const char* lc = lmem + ((w + 1 + i) & 3) * 2048;
        mf[2 * i] = *(const u32x4*)(lc); mf[2 * i + 1] = *(const u32x4*)(lc + 1024);
      }
      mf[6] = *(const u32x4*)(lmem + 8 * 1024);
      mf[7] = *(const u32x4*)(lmem + 9 * 1024);
      __builtin_amdgcn_sched_barrier(0);
      SQ0("a", mfo[0], wq[0][0]); SQ1("a", mfo[0], wq[1][0]); SK0("a", mfo[0], wk[0][0]); SK1("a", mfo[0], wk[1][0]);
      const u32 pp = (u32)(uintptr_t)(__attribute__((address_space(3))) char*)smem + CH_PART + w * PSTR + l * 16;
      asm volatile(
          "v_smfmac_f32_16x16x64_bf16 v[208:211], %[m], %[b0], %[ix]\n\t"
          "v_permlane32_swap_b32 v164, v168\n\t"
          "v_permlane32_swap_b32 v165, v169\n\t"
          "v_smfmac_f32_16x16x64_bf16 v[212:215], %[m], %[b1], %[ix]\n\t"
          "v_permlane32_swap_b32 v166, v170\n\t"
          "v_permlane32_swap_b32 v167, v171\n\t"
          "v_smfmac_f32_16x16x64_bf16 v[216:219], %[m], %[b2], %[ix]\n\t"
          "v_add_f32 v164, v164, v168\n\t"
          "v_add_f32 v165, v165, v169\n\t"
          "v_smfmac_f32_16x16x64_bf16 v[220:223], %[m], %[b3], %[ix]\n\t"
          "v_add_f32 v166, v166, v170\n\t"
          "v_add_f32 v167, v167, v171\n\t"
          "ds_write_b128 %[pp], v[164:167] offset:3072"
          : "+{v[208:211]}"(aq0), "+{v[212:215]}"(aq1), "+{v[216:219]}"(ak0), "+{v[220:223]}"(ak1), "+{v[164:167]}"(av0), "+{v[168:171]}"(av1)
          : [m] "v"(mfo[1]), [b0] "a"(wq[0][1]), [b1] "a"(wq[1][1]), [b2] "a"(wk[0][1]), [b3] "a"(wk[1][1]), [ix] "v"(sidx), [pp] "v"(pp)
          : "memory");
      (void)__builtin_amdgcn_raw_ptr_buffer_atomic_add_i32(1, rmc, s > 0 ? mco : 0x80000000u, (u32)(s - 1) * (32 * 64), 16);
      __builtin_amdgcn_sched_barrier(0);
      {
        u32x4 sv = {SENT, SENT, SENT, SENT};
        asm volatile("" : "+v"(sv));
        const u32 ro = s > 0 ? rso : 0x80000000u;
        const u32 rb = ((s - 1) & 1) * INBOX_SLOT;
#pragma unroll
        for (int i = 0; i < 4; ++i) {
          SQ0("a", mf[i], wq[0][2 + i]); SQ1("a", mf[i], wq[1][2 + i]); SK0("a", mf[i], wk[0][2 + i]); SK1("a", mf[i], wk[1][2 + i]);
          __builtin_amdgcn_raw_buffer_store_b128(sv, rin, ro, rb + i * 1024, 16);
          if (i < 3) ldwo(i);
          __builtin_amdgcn_sched_barrier(0);
        }
      }
      CST(1);
      SQ0("a", mf[4], wq[0][6]); SQ1("a", mf[4], wq[1][6]); SQ0("a", mf[5], wq[0][7]); SQ1("a", mf[5], wq[1][7]);
      SQ0("v", mf[6], wq[0][8]); SQ1("v", mf[6], wq[1][8]); SQ0("v", mf[7], wq[0][9]); SQ1("v", mf[7], wq[1][9]);
      SK0("a", mf[4], wk[0][6]); SK1("a", mf[4], wk[1][6]);
      const u32 px = m < 8 ? pp + 1024 : (u32)(uintptr_t)(__attribute__((address_space(3))) char*)smem + CH_MEMF + 10240 + tid * 16;
      asm volatile(
          "v_smfmac_f32_16x16x64_bf16 v[216:219], %[m5], %[k05], %[ix]\n\t"
          "v_permlane32_swap_b32 v208, v212\n\t"
          "v_permlane32_swap_b32 v209, v213\n\t"
          "v_smfmac_f32_16x16x64_bf16 v[220:223], %[m5], %[k15], %[ix]\n\t"
          "v_permlane32_swap_b32 v210, v214\n\t"
          "v_permlane32_swap_b32 v211, v215\n\t"
          "v_smfmac_f32_16x16x64_bf16 v[216:219], %[m6], %[k06], %[ix]\n\t"
          "v_add_f32 v208, v208, v212\n\t"
          "v_add_f32 v209, v209, v213\n\t"
          "v_smfmac_f32_16x16x64_bf16 v[220:223], %[m6], %[k16], %[ix]\n\t"
          "v_add_f32 v210, v210, v214\n\t"
          "v_add_f32 v211, v211, v215\n\t"
          "v_smfmac_f32_16x16x64_bf16 v[216:219], %[m7], %[k07], %[ix]\n\t"
          "v_cvt_pk_bf16_f32 v208, v208, v209\n\t"
          "v_cvt_pk_bf16_f32 v209, v210, v211\n\t"
          "v_smfmac_f32_16x16x64_bf16 v[220:223], %[m7], %[k17], %[ix]\n\t"
          "s_nop 0\n\t"
          "v_mfma_f32_16x16x16_bf16 v[212:215], v[208:209], %[idn], 0\n\t"
          "s_nop 5\n\t"
          "v_permlane32_swap_b32 v216, v220\n\t"
          "v_permlane32_swap_b32 v217, v221\n\t"
          "v_permlane32_swap_b32 v218, v222\n\t"
          "v_permlane32_swap_b32 v219, v223\n\t"
          "v_cvt_pk_bf16_f32 v184, v212, v213\n\t"
          "v_cvt_pk_bf16_f32 v185, v214, v215\n\t"
          "v_add_f32 v216, v216, v220\n\t"
          "v_add_f32 v217, v217, v221\n\t"
          "v_mov_b32_dpp v186, v184 row_ror:8 row_mask:0xf bank_mask:0xf\n\t"
          "v_mov_b32_dpp v187, v185 row_ror:8 row_mask:0xf bank_mask:0xf\n\t"
          "v_add_f32 v218, v218, v222\n\t"
          "v_add_f32 v219, v219, v223\n\t"
          "v_mfma_f32_16x16x32_bf16 v[172:175], v[192:195], v[184:187], 0\n\t"
          "v_cvt_pk_bf16_f32 v216, v216, v217\n\t"
          "v_cvt_pk_bf16_f32 v217, v218, v219\n\t"
          "v_mfma_f32_16x16x32_bf16 v[176:179], v[196:199], v[184:187], 0\n\t"
          "s_nop 0\n\t"
          "v_mfma_f32_16x16x16_bf16 v[220:223], v[216:217], %[idn], 0\n\t"
          "s_nop 2\n\t"
          "ds_write_b128 %[px], v[172:175]\n\t"
          "s_nop 1\n\t"
          "ds_write_b128 %[px], v[176:179] offset:128\n\t"
          "s_nop 0\n\t"
          "v_cvt_pk_bf16_f32 v188, v220, v221\n\t"
          "v_cvt_pk_bf16_f32 v189, v222, v223\n\t"
          "s_nop 1\n\t"
          "v_mov_b32_dpp v190, v188 row_ror:8 row_mask:0xf bank_mask:0xf\n\t"
          "v_mov_b32_dpp v191, v189 row_ror:8 row_mask:0xf bank_mask:0xf\n\t"
          "s_nop 1\n\t"
          "v_mfma_f32_16x16x32_bf16 v[180:183], v[188:191], v[184:187], 0\n\t"
          "s_nop 7\n\t"
          "ds_write_b128 %[pp], v[180:183]\n\t"
          "s_waitcnt lgkmcnt(0)"
          : "+{v[208:211]}"(aq0), "+{v[212:215]}"(aq1), "+{v[216:219]}"(ak0), "+{v[220:223]}"(ak1), "=&{v[188:191]}"(kp)
          : [m5] "v"(mf[5]), [m6] "v"(mf[6]), [m7] "v"(mf[7]), [k05] "a"(wk[0][7]), [k15] "a"(wk[1][7]), [k06] "v"(wk[0][8]), [k16] "v"(wk[1][8]),
            [k07] "v"(wk[0][9]), [k17] "v"(wk[1][9]), [ix] "v"(sidx), [idn] "v"(idn), "{v[192:195]}"(kv4[0]), "{v[196:199]}"(kv4[1]),
            [pp] "v"(pp), [px] "v"(px)
          : "v172", "v173", "v174", "v175", "v176", "v177", "v178", "v179", "v180", "v181", "v182", "v183",
            "v184", "v185", "v186", "v187", "memory");
    }
    CST(2);
    STAMP(2);
    __syncthreads();
    STAMP(3);
    CST(3);
    {
      const char* pa = smem + CH_PART + 1024 + l * 16;
      const char* pc = smem + CH_PART + (((m < 8) & (g < 2)) ? l * 16 : 4096);
      const char* pb = smem + CH_PART + 3072 + l * 16;
      const f32x4 a0 = *(const f32x4*)(pa), a1 = *(const f32x4*)(pa + PSTR), a2 = *(const f32x4*)(pa + 2 * PSTR), a3 = *(const f32x4*)(pa + 3 * PSTR);
      const f32x4 c0 = *(const f32x4*)(pc), c1 = *(const f32x4*)(pc + PSTR), c2 = *(const f32x4*)(pc + 2 * PSTR), c3 = *(const f32x4*)(pc + 3 * PSTR);
      const f32x4 b0 = *(const f32x4*)(pb), b1 = *(const f32x4*)(pb + PSTR);
      __builtin_amdgcn_sched_barrier(0);
      const f32x4 ta = (a0 + a1) + (a2 + a3);
      const f32x4 b2 = *(const f32x4*)(pb + 2 * PSTR), b3 = *(const f32x4*)(pb + 3 * PSTR);
      __builtin_amdgcn_sched_barrier(0);
      const f32x4 tc = (c0 + c1) + (c2 + c3);
      ldwo(3); ldwo(4);
      __builtin_amdgcn_sched_barrier(0);
      CST(4);
      float s0 = ta.x, s1 = ta.y, s2 = ta.z, s3 = ta.w, s4 = tc.x, s5 = tc.y, s6 = tc.z, s7 = tc.w;
      float mxl, tu;
      f32x2 tl = b0.xy, th = b0.zw, ul = b2.xy, uh = b2.zw;
      asm volatile(
          "v_max3_f32 %[t], %[s0], %[s1], %[s2]\n\t"
          "v_max3_f32 %[u], %[s3], %[s4], %[s5]\n\t"
          "v_max3_f32 %[t], %[t], %[s6], %[s7]\n\t"
          "v_max_f32 %[t], %[t], %[u]\n\t"
          "v_pk_add_f32 %[tl], %[tl], %[b1l]\n\t"
          "v_pk_add_f32 %[th], %[th], %[b1h]\n\t"
          "v_max_f32_dpp %[t], %[t], %[t] row_ror:8 row_mask:0xf bank_mask:0xf\n\t"
          "v_mov_b32 %[u], %[t]\n\t"
          "v_pk_add_f32 %[ul], %[ul], %[b3l]\n\t"
          "v_pk_add_f32 %[uh], %[uh], %[b3h]\n\t"
          "v_permlane16_swap_b32 %[t], %[u]\n\t"
          "v_max_f32 %[t], %[t], %[u]\n\t"
          "v_mov_b32 %[u], %[t]\n\t"
          "v_pk_add_f32 %[tl], %[tl], %[ul]\n\t"
          "v_pk_add_f32 %[th], %[th], %[uh]\n\t"
          "v_permlane32_swap_b32 %[t], %[u]\n\t"
          "v_max_f32 %[t], %[t], %[u]\n\t"
          "v_mul_f32 %[t], 0xbe0293ee, %[t]"
          : [t] "=&v"(mxl), [u] "=&v"(tu), [tl] "+v"(tl), [th] "+v"(th), [ul] "+v"(ul), [uh] "+v"(uh)
          : [s0] "v"(s0), [s1] "v"(s1), [s2] "v"(s2), [s3] "v"(s3), [s4] "v"(s4), [s5] "v"(s5), [s6] "v"(s6), [s7] "v"(s7),
            [b1l] "v"(b1.xy), [b1h] "v"(b1.zw), [b3l] "v"(b3.xy), [b3h] "v"(b3.zw));
      CST(5);
      const bool pub = s != NSEG - 1;
      const u32 ib = (u32)(s & 1) * INBOX_SLOT + (u32)c * 128 + (4 * (g & 1) + (m & 3)) * 16;
      const u32 ob = pub ? ib + (u32)(20 * w + 4 * (g >> 1) + (m >> 2)) * (NPROD * 128) : 0x80000000u;
      const u32 ob2 = pub ? ib + (u32)(20 * w + 16 + 2 * (g >> 1) + (m >> 3)) * (NPROD * 128) + ((m >> 2) & 1) * 8 : 0x80000000u;
      asm volatile(
          "v_fmamk_f32 %[s0], %[s0], 0x3e0293ee, %[mx]\n\t"
          "v_fmamk_f32 %[s1], %[s1], 0x3e0293ee, %[mx]\n\t"
          "v_exp_f32 %[s0], %[s0]\n\t"
          "v_fmamk_f32 %[s2], %[s2], 0x3e0293ee, %[mx]\n\t"
          "v_exp_f32 %[s1], %[s1]\n\t"
          "v_fmamk_f32 %[s3], %[s3], 0x3e0293ee, %[mx]\n\t"
          "v_exp_f32 %[s2], %[s2]\n\t"
          "v_exp_f32 %[s3], %[s3]\n\t"
          "v_fmamk_f32 %[s4], %[s4], 0x3e0293ee, %[mx]\n\t"
          "v_cvt_pk_bf16_f32 v172, %[s0], %[s1]\n\t"
          "v_cvt_pk_bf16_f32 v173, %[s2], %[s3]\n\t"
          "v_fmamk_f32 %[s5], %[s5], 0x3e0293ee, %[mx]\n\t"
          "v_fmamk_f32 %[s6], %[s6], 0x3e0293ee, %[mx]\n\t"
          "v_mov_b32_dpp v174, v172 row_ror:8 row_mask:0xf bank_mask:0xf\n\t"
          "v_mov_b32_dpp v175, v173 row_ror:8 row_mask:0xf bank_mask:0xf\n\t"
          "v_fmamk_f32 %[s7], %[s7], 0x3e0293ee, %[mx]\n\t"
          "v_exp_f32 %[s4], %[s4]\n\t"
          "v_mfma_f32_16x16x32_bf16 v[208:211], v[200:203], v[172:175], 0\n\t"
          "v_exp_f32 %[s5], %[s5]\n\t"
          "v_mfma_f32_16x16x32_bf16 v[212:215], v[204:207], v[172:175], 0\n\t"
          "v_exp_f32 %[s6], %[s6]\n\t"
          "v_exp_f32 %[s7], %[s7]\n\t"
          "v_cvt_pk_bf16_f32 v178, %[t0], %[t1]\n\t"
          "v_cvt_pk_bf16_f32 v179, %[t2], %[t3]\n\t"
          "v_cvt_pk_bf16_f32 v176, %[s4], %[s5]\n\t"
          "v_cvt_pk_bf16_f32 v177, %[s6], %[s7]\n\t"
          "v_add_f32 v182, %[s0], %[s1]\n\t"
          "v_permlane32_swap_b32 v178, v172\n\t"
          "v_permlane32_swap_b32 v179, v173\n\t"
          "v_add_f32 v183, %[s2], %[s3]\n\t"
          "v_add_f32 v182, v182, v183\n\t"
          "v_mfma_f32_16x16x16_bf16 v[208:211], v[178:179], v[176:177], v[208:211]\n\t"
          "v_add_f32 v183, %[s4], %[s5]\n\t"
          "v_add_f32 %[s6], %[s6], %[s7]\n\t"
          "v_mfma_f32_16x16x16_bf16 v[212:215], v[172:173], v[176:177], v[212:215]\n\t"
          "v_add_f32 v183, v183, %[s6]\n\t"
          "v_add_f32 v182, v182, v183\n\t"
          "v_mov_b64 v[184:185], 0\n\t"
          "v_mov_b64 v[186:187], 0\n\t"
          "v_add_f32_dpp v182, v182, v182 row_ror:8 row_mask:0xf bank_mask:0xf\n\t"
          "v_mov_b32 v183, v182\n\t"
          "v_mov_b64 v[164:165], 0\n\t"
          "v_mov_b64 v[166:167], 0\n\t"
          "v_permlane16_swap_b32 v182, v183\n\t"
          "v_add_f32 v182, v182, v183\n\t"
          "v_mov_b32 v183, v182\n\t"
          "v_mov_b64 v[220:221], 0\n\t"
          "v_mov_b64 v[222:223], 0\n\t"
          "v_permlane32_swap_b32 v182, v183\n\t"
          "v_add_f32 v182, v182, v183\n\t"
          "v_rcp_f32 v182, v182\n\t"
          "v_mov_b64 v[192:193], 0\n\t"
          "v_mul_f32 v208, v208, v182\n\t"
          "v_mul_f32 v209, v209, v182\n\t"
          "v_mul_f32 v210, v210, v182\n\t"
          "v_mul_f32 v211, v211, v182\n\t"
          "v_cvt_pk_bf16_f32 v216, v208, v209\n\t"
          "v_cvt_pk_bf16_f32 v217, v210, v211\n\t"
          "v_mul_f32 v212, v212, v182\n\t"
          "v_mul_f32 v213, v213, v182\n\t"
          "v_mul_f32 v214, v214, v182\n\t"
          "v_mul_f32 v215, v215, v182\n\t"
          "v_cvt_pk_bf16_f32 v218, v212, v213\n\t"
          "v_cvt_pk_bf16_f32 v219, v214, v215\n\t"
          "v_mov_b32_dpp v216, v216 row_ror:8 row_mask:0xf bank_mask:0xc\n\t"
          "v_mov_b32_dpp v217, v217 row_ror:8 row_mask:0xf bank_mask:0xc\n\t"
          "v_mov_b32_dpp v218, v218 row_ror:8 row_mask:0xf bank_mask:0xc\n\t"
          "v_mov_b32_dpp v219, v219 row_ror:8 row_mask:0xf bank_mask:0xc\n\t"
          "v_mov_b64 v[194:195], 0\n\t"
          "v_mov_b64 v[196:197], 0\n\t"
          "v_smfmac_f32_16x16x64_bf16 v[184:187], v[216:219], %[w0], %[ix]\n\t"
          "v_mov_b64 v[198:199], 0\n\t"
          "v_smfmac_f32_16x16x64_bf16 v[164:167], v[216:219], %[w1], %[ix]\n\t"
          "v_smfmac_f32_16x16x64_bf16 v[220:223], v[216:219], %[w2], %[ix]\n\t"
          "v_smfmac_f32_16x16x64_bf16 v[192:195], v[216:219], %[w3], %[ix]\n\t"
          "v_smfmac_f32_16x16x64_bf16 v[196:199], v[216:219], %[w4], %[ix]\n\t"
          "v_cvt_pk_bf16_f32 v208, v184, v185\n\t"
          "v_cvt_pk_bf16_f32 v209, v186, v187\n\t"
          "v_cvt_pk_bf16_f32 v210, v164, v165\n\t"
          "v_cvt_pk_bf16_f32 v211, v166, v167\n\t"
          "buffer_store_dwordx4 v[208:211], %[ob], %[ri], 0 offen sc1\n\t"
          "v_cvt_pk_bf16_f32 v212, v220, v221\n\t"
          "v_cvt_pk_bf16_f32 v213, v222, v223\n\t"
          "v_cvt_pk_bf16_f32 v214, v192, v193\n\t"
          "v_cvt_pk_bf16_f32 v215, v194, v195\n\t"
          "buffer_store_dwordx4 v[212:215], %[ob], %[ri], %[so] offen sc1\n\t"
          "v_cvt_pk_bf16_f32 v216, v196, v197\n\t"
          "v_cvt_pk_bf16_f32 v217, v198, v199\n\t"
          "buffer_store_dwordx2 v[216:217], %[ob2], %[ri], 0 offen sc1\n\t"
          "s_nop 1"
          : [s0] "+v"(s0), [s1] "+v"(s1), [s2] "+v"(s2), [s3] "+v"(s3), [s4] "+v"(s4), [s5] "+v"(s5), [s6] "+v"(s6), [s7] "+v"(s7), "=&{v[178:179]}"(vm0), "=&{v[172:173]}"(vm1)
          : [mx] "v"(mxl), [t0] "v"(tl.x), [t1] "v"(tl.y), [t2] "v"(th.x), [t3] "v"(th.y), "{v[200:203]}"(kv4[2]), "{v[204:207]}"(kv4[3]),
            [w0] "v"(wo[0]), [w1] "v"(wo[1]), [w2] "v"(wo[2]), [w3] "v"(wo[3]), [w4] "v"(wo[4]), [ix] "v"(sidx),
            [ob] "v"(ob), [ob2] "v"(ob2), [ri] "s"(din), [so] "s"(8 * NPROD * 128)
          : "v192", "v193", "v194", "v195", "v196", "v197", "v198", "v199", "v174", "v175", "v176", "v177", "v182", "v183",
            "v184", "v185", "v186", "v187", "v164", "v165", "v166", "v167", "v208", "v209", "v210", "v211", "v212", "v213", "v214", "v215",
            "v216", "v217", "v218", "v219", "v220", "v221", "v222", "v223", "memory");
    }
    const int slot = s & 1;
    CST(6);
    STAMP(4);
    if (w == gs && m < 8) __builtin_amdgcn_raw_buffer_store_b128(kp, rkm, (u32)(((s * 8 + m) * HD + h * 128 + 32 * w + 8 * g) * 2), 0, 16);
    if (w == ((gs + 1) & 3) && g < 2) __builtin_amdgcn_raw_buffer_store_b64(vm0, rkm, (u32)(SZ_KM + (((s * 32 + h) * 128 + 32 * gs + p32(0, m)) * 8 + 4 * g) * 2), 0, 16);
    if (w == ((gs + 2) & 3) && g < 2) __builtin_amdgcn_raw_buffer_store_b64(vm1, rkm, (u32)(SZ_KM + (((s * 32 + h) * 128 + 32 * gs + p32(1, m)) * 8 + 4 * g) * 2), 0, 16);
    const int doit = (c - (int)NRED) & (s - (NSEG - 1));
    {
      const u32 base = slot * INBOX_SLOT + (c < (int)NRED ? c : 0) * NPROD * 128 + w * 4096 + l * 16;
      int mm = m;
      asm volatile("" : "+v"(mm));
      const u32 one2 = (mm & 1) ? 0x3f800000u : 0x3f80u;
      const u32x4 sel = {(mm >> 1) == 0 ? one2 : 0u, (mm >> 1) == 1 ? one2 : 0u, (mm >> 1) == 2 ? one2 : 0u, (mm >> 1) == 3 ? one2 : 0u};
      f32x4 rd;
      u32 tmx, cnt;
      unsigned long long m0, m1, m2, m3, mo;
      asm volatile(
          "s_mov_b32 %[cnt], 0\n\t"
          "s_cmp_ge_i32 %[doit], 0\n\t"
          "s_cbranch_scc1 4f\n\t"
          "s_sleep %[slp0]\n\t"
          "buffer_load_dwordx4 v[224:227], %[o], %[rs], 0 offen offset:0 sc1\n\t"
          "buffer_load_dwordx4 v[228:231], %[o], %[rs], 0 offen offset:1024 sc1\n\t"
          "buffer_load_dwordx4 v[232:235], %[o], %[rs], 0 offen offset:2048 sc1\n\t"
          "buffer_load_dwordx4 v[236:239], %[o], %[rs], 0 offen offset:3072 sc1\n\t"
          "s_mov_b64 %[m0], -1\n\t"
          "s_mov_b64 %[m1], -1\n\t"
          "s_mov_b64 %[m2], -1\n\t"
          "s_mov_b64 %[m3], -1\n\t"
          "s_sleep %[slp]\n"
          "1:\n\t"
          "s_mov_b64 exec, %[m0]\n\t"
          "buffer_load_dwordx4 v[224:227], %[o], %[rs], 0 offen offset:0 sc1\n\t"
          "s_mov_b64 exec, %[m1]\n\t"
          "buffer_load_dwordx4 v[228:231], %[o], %[rs], 0 offen offset:1024 sc1\n\t"
          "s_mov_b64 exec, %[m2]\n\t"
          "buffer_load_dwordx4 v[232:235], %[o], %[rs], 0 offen offset:2048 sc1\n\t"
          "s_mov_b64 exec, %[m3]\n\t"
          "buffer_load_dwordx4 v[236:239], %[o], %[rs], 0 offen offset:3072 sc1\n\t"
          "s_mov_b64 exec, -1\n\t"
          "s_waitcnt vmcnt(4)\n\t"
          "v_max3_u32 %[t], v224, v225, v226\n\t"
          "v_max_u32 %[t], %[t], v227\n\t"
          "v_cmp_eq_u32 %[m0], -1, %[t]\n\t"
          "v_max3_u32 %[t], v228, v229, v230\n\t"
          "v_max_u32 %[t], %[t], v231\n\t"
          "v_cmp_eq_u32 %[m1], -1, %[t]\n\t"
          "v_max3_u32 %[t], v232, v233, v234\n\t"
          "v_max_u32 %[t], %[t], v235\n\t"
          "v_cmp_eq_u32 %[m2], -1, %[t]\n\t"
          "v_max3_u32 %[t], v236, v237, v238\n\t"
          "v_max_u32 %[t], %[t], v239\n\t"
          "v_cmp_eq_u32 %[m3], -1, %[t]\n\t"
          "s_or_b64 %[mo], %[m0], %[m1]\n\t"
          "s_or_b64 vcc, %[m2], %[m3]\n\t"
          "s_or_b64 %[mo], %[mo], vcc\n\t"
          "s_cbranch_scc0 2f\n\t"
          "s_add_u32 %[cnt], %[cnt], 1\n\t"
          "s_cmp_lt_u32 %[cnt], 0x20000\n\t"
          "s_cbranch_scc1 1b\n"
          "2:\n\t"
          "v_mfma_f32_16x16x32_bf16 %[d], v[224:227], %[sel], 0\n\t"
          "v_mfma_f32_16x16x32_bf16 %[d], v[228:231], %[sel], %[d]\n\t"
          "v_mfma_f32_16x16x32_bf16 %[d], v[232:235], %[sel], %[d]\n\t"
          "v_mfma_f32_16x16x32_bf16 %[d], v[236:239], %[sel], %[d]\n\t"
          "4:"
          : [d] "=&v"(rd), "+{v[224:227]}"(qt[0]), "+{v[228:231]}"(qt[1]), "+{v[232:235]}"(qt[2]), "+{v[236:239]}"(qt[3]), [t] "=&v"(tmx), [cnt] "=&s"(cnt),
            [m0] "=&s"(m0), [m1] "=&s"(m1), [m2] "=&s"(m2), [m3] "=&s"(m3), [mo] "=&s"(mo)
          : [o] "v"(base), [rs] "s"(din), [slp] "i"(SLP_D1), [slp0] "i"(SLP_RI), [doit] "s"(doit), [sel] "v"(sel)
          : "vcc", "scc", "memory");
      load_kv(s != NSEG - 1 ? s + 1 : s);
      if (c >= (int)NRED && s != NSEG - 1) __builtin_amdgcn_s_sleep(SLP_NR);
      if (doit < 0) {
      if (cnt >= 0x20000u && l == 0) *err = 2;
      STAMP(5);
      asm volatile("s_nop 4" : "+v"(rd));
      {
        const f32x2 r2 = {hsum32(rd.x, rd.z), hsum32(rd.y, rd.w)};
        if (m < 8) *(f32x2*)(smem + CH_SRED + (w * 8 + 4 * (g & 1) + (m & 3)) * 32 + (m >> 2) * 16 + (g >> 1) * 8) = r2;
      }
      __syncthreads();
      if (w == 0) {
        const int l8 = l & 7;
        const f32x4* sr = (const f32x4*)(smem + CH_SRED);
        f32x4 a0 = (sr[2 * l8] + sr[16 + 2 * l8]) + (sr[32 + 2 * l8] + sr[48 + 2 * l8]);
        f32x4 a1 = (sr[2 * l8 + 1] + sr[16 + 2 * l8 + 1]) + (sr[32 + 2 * l8 + 1] + sr[48 + 2 * l8 + 1]);
        u32x4 o = {pack2(a0.x, a0.y), pack2(a0.z, a0.w), pack2(a1.x, a1.y), pack2(a1.z, a1.w)};
        __builtin_amdgcn_raw_buffer_store_b128(o, rmem, (s + 1) * MEM_STEP + (l >> 3) * MEM_REP + c * 128 + l8 * 16, 0, 16);
      }
      STAMP(6);
      __builtin_amdgcn_s_sleep(SLP_RM);
      }
    }
  }
  asm volatile("s_waitcnt vmcnt(0)" ::"{v[224:227]}"(qt[0]), "{v[228:231]}"(qt[1]), "{v[232:235]}"(qt[2]), "{v[236:239]}"(qt[3]), "{v[240:243]}"(mfo[0]), "{v[244:247]}"(mfo[1]), "{v[248:251]}"(mfo[2]), "{v252}"(qf) : "memory");
  __syncthreads();
  if (tid == 0) __hip_atomic_fetch_add(mcnt + ((NSEG - 1) * 32 + h) * 16, 1u, __ATOMIC_RELAXED, __HIP_MEMORY_SCOPE_AGENT);
}

#define WSTAMP(i)
#define WSTAMPJ(i)
__device__ __forceinline__ int otid() { int t = threadIdx.x; asm volatile("" : "+v"(t)); return t; }
#define VMCNT(n) asm volatile("s_waitcnt vmcnt(%0)" ::"i"(n) : "memory")
#define RAWBAR() do { asm volatile("s_waitcnt lgkmcnt(0)" ::: "memory"); __builtin_amdgcn_s_barrier(); asm volatile("" ::: "memory"); } while (0)

__device__ __forceinline__ void block_top8(const float* __restrict__ r, int* sidx, float* sval, float* swv, int* swi) {
  const int tid = otid(), w = tid >> 6, l = tid & 63;
  float v[8];
#pragma unroll
  for (int j = 0; j < 8; ++j) v[j] = r[tid + 256 * j];
  for (int round = 0; round < CSEL; ++round) {
    float bv = v[0]; int bi = tid;
#pragma unroll
    for (int j = 1; j < 8; ++j) if (v[j] > bv) { bv = v[j]; bi = tid + 256 * j; }
#pragma unroll
    for (int o = 32; o > 0; o >>= 1) {
      float ov = __shfl_xor(bv, o); int oi = __shfl_xor(bi, o);
      if (ov > bv || (ov == bv && oi < bi)) { bv = ov; bi = oi; }
    }
    if (l == 0) { swv[w] = bv; swi[w] = bi; }
    __syncthreads();
    float fv = swv[0]; int fi = swi[0];
#pragma unroll
    for (int k = 1; k < 4; ++k) { float ov = swv[k]; int oi = swi[k]; if (ov > fv || (ov == fv && oi < fi)) { fv = ov; fi = oi; } }
    if (tid == 0) { sidx[round] = fi; sval[round] = fv; }
#pragma unroll
    for (int j = 0; j < 8; ++j) if (tid + 256 * j == fi) v[j] = -INFINITY;
    __syncthreads();
  }
}

__device__ __forceinline__ void worker_modA(const u16* __restrict__ wg, const u16* __restrict__ wu, char* __restrict__ ws, char* smem, const int blk0, const int bstep) {
  float* xs = (float*)smem;
  float* red = (float*)(smem + 20480);
  int* sidx = (int*)(smem + 24576);
  float* sval = (float*)(smem + 24576 + 64);
  float* swv = (float*)(smem + 24576 + 128);
  int* swi = (int*)(smem + 24576 + 192);
  const int tid = otid(), w = tid >> 6, l = tid & 63;
  block_top8((const float*)(ws + OFF_R), sidx, sval, swv, swi);
  const u16* xn = (const u16*)(ws + OFF_XN);
  for (int i = tid; i < 8 * 640; i += 256) { int rr = i / 640, k = i % 640; xs[i] = bf2f(xn[(size_t)sidx[rr] * DM + k]); }
  const __amdgpu_buffer_rsrc_t rh = __builtin_amdgcn_make_buffer_rsrc(ws + OFF_HB, 0, (u32)(OFF_ERR - OFF_HB), 0x00020000);
  if (blk0 == 0 && tid < 8) {
    __builtin_amdgcn_raw_buffer_store_b32((u32)sidx[tid], rh, (u32)(OFF_SEL - OFF_HB) + tid * 4, 0, 16);
    __builtin_amdgcn_raw_buffer_store_b32(__float_as_uint(bfr(1.0f / (1.0f + __expf(-sval[tid])))), rh, (u32)(OFF_SEL - OFF_HB) + 32 + tid * 4, 0, 16);
  }
  __syncthreads();
  const int cp = tid & 7, kg = tid >> 3;
  for (int blk = blk0; blk < 108; blk += bstep) {
  const int n0 = blk * 16 + 2 * cp;
  float ag[8][2], au[8][2];
#pragma unroll
  for (int rr = 0; rr < 8; ++rr) { ag[rr][0] = ag[rr][1] = au[rr][0] = au[rr][1] = 0.f; }
  u32 g2[20], u2[20];
#pragma unroll
  for (int i = 0; i < 20; ++i) {
    g2[i] = *(const u32*)(wg + (size_t)(20 * kg + i) * FF + n0);
    u2[i] = *(const u32*)(wu + (size_t)(20 * kg + i) * FF + n0);
  }
#pragma unroll
  for (int i = 0; i < 20; ++i) {
    const float g0 = bflo(g2[i]), g1 = bfhi(g2[i]), u0 = bflo(u2[i]), u1 = bfhi(u2[i]);
#pragma unroll
    for (int rr = 0; rr < 8; ++rr) {
      const float xv = xs[rr * 640 + 20 * kg + i];
      ag[rr][0] += xv * g0; ag[rr][1] += xv * g1; au[rr][0] += xv * u0; au[rr][1] += xv * u1;
    }
  }
#pragma unroll
  for (int rr = 0; rr < 8; ++rr) {
#pragma unroll
    for (int e = 0; e < 2; ++e) {
      ag[rr][e] = xr32_add(xr16_add(xr8_add(ag[rr][e])));
      au[rr][e] = xr32_add(xr16_add(xr8_add(au[rr][e])));
    }
  }
  if (l < 8) {
#pragma unroll
    for (int rr = 0; rr < 8; ++rr) *(f32x4*)(red + (w * 8 + l) * 32 + rr * 4) = (f32x4){ag[rr][0], ag[rr][1], au[rr][0], au[rr][1]};
  }
  __syncthreads();
  if (tid < 64) {
    const int rr = tid >> 3, c2 = tid & 7;
    f32x4 a0 = *(const f32x4*)(red + (0 * 8 + c2) * 32 + rr * 4), a1 = *(const f32x4*)(red + (1 * 8 + c2) * 32 + rr * 4);
    f32x4 a2 = *(const f32x4*)(red + (2 * 8 + c2) * 32 + rr * 4), a3 = *(const f32x4*)(red + (3 * 8 + c2) * 32 + rr * 4);
    f32x4 t = (a0 + a1) + (a2 + a3);
    float hv[2];
#pragma unroll
    for (int e = 0; e < 2; ++e) {
      const float gsum = bfr(t[e]), usum = bfr(t[2 + e]);
      const float sg = bfr(gsum / (1.0f + __expf(-gsum)));
      hv[e] = sg * usum;
    }
    __builtin_amdgcn_raw_buffer_store_b32(pack2(hv[0], hv[1]), rh, (u32)((rr * FF + blk * 16 + 2 * c2) * 2), 0, 16);
  }
  asm volatile("s_waitcnt vmcnt(0)" ::: "memory");
  __syncthreads();
  if (tid == 0) __hip_atomic_fetch_add((u32*)(ws + OFF_HCNT), 1u, __ATOMIC_RELAXED, __HIP_MEMORY_SCOPE_AGENT);
  }
}

__device__ __forceinline__ void worker_modB(const u16* __restrict__ wd, char* __restrict__ ws, char* smem, const int blk) {
  float* hs = (float*)smem;
  float* red = (float*)(smem + 4096);
  const int tid = otid(), w = tid >> 6, l = tid & 63;
  const int cb = blk % 5, kb = blk / 5;
  const int n0 = cb * 128 + 2 * l;
  u32 d2[27];
#pragma unroll
  for (int i = 0; i < 27; ++i) d2[i] = *(const u32*)(wd + (size_t)(kb * 108 + 27 * w + i) * DM + n0);
  wait_count((const u32*)(ws + OFF_HCNT), 108u, (u32*)(ws + OFF_ERR));
  const __amdgpu_buffer_rsrc_t rh = __builtin_amdgcn_make_buffer_rsrc(ws + OFF_HB, 0, (u32)(OFF_ERR - OFF_HB), 0x00020000);
  for (int i = tid; i < 8 * 54; i += 256) {
    const int rr = i / 54, k2 = i % 54;
    const u32 v = __builtin_amdgcn_raw_buffer_load_b32(rh, (u32)((rr * FF + kb * 108 + 2 * k2) * 2), 0, 16);
    hs[rr * 108 + 2 * k2] = bflo(v); hs[rr * 108 + 2 * k2 + 1] = bfhi(v);
  }
  __syncthreads();
  float a[8][2];
#pragma unroll
  for (int rr = 0; rr < 8; ++rr) a[rr][0] = a[rr][1] = 0.f;
#pragma unroll
  for (int i = 0; i < 27; ++i) {
    const float d0 = bflo(d2[i]), d1 = bfhi(d2[i]);
#pragma unroll
    for (int rr = 0; rr < 8; ++rr) { const float hv = hs[rr * 108 + 27 * w + i]; a[rr][0] += hv * d0; a[rr][1] += hv * d1; }
  }
#pragma unroll
  for (int rr = 0; rr < 8; ++rr) { red[(w * 8 + rr) * 128 + 2 * l] = a[rr][0]; red[(w * 8 + rr) * 128 + 2 * l + 1] = a[rr][1]; }
  __syncthreads();
  for (int i = tid; i < 8 * 128; i += 256) {
    const int rr = i >> 7, cc = i & 127;
    const float v = (red[rr * 128 + cc] + red[(8 + rr) * 128 + cc]) + (red[(16 + rr) * 128 + cc] + red[(24 + rr) * 128 + cc]);
    __builtin_amdgcn_raw_buffer_store_b32(__float_as_uint(v), rh, (u32)(OFF_YP - OFF_HB) + (u32)(((kb * 8 + rr) * DM + cb * 128 + cc) * 4), 0, 16);
  }
  asm volatile("s_waitcnt vmcnt(0)" ::: "memory");
  __syncthreads();
  if (tid == 0) __hip_atomic_fetch_add((u32*)(ws + OFF_YCNT), 1u, __ATOMIC_RELAXED, __HIP_MEMORY_SCOPE_AGENT);
}

#define MFMA_BA(acc, ma, wb) asm volatile("v_mfma_f32_16x16x32_bf16 %0, %1, %2, %0" : "+v"(acc) : "v"(ma), "a"(wb))
#define MFMA_BA0(acc, ma, wb) asm volatile("v_mfma_f32_16x16x32_bf16 %0, %1, %2, 0" : "=&v"(acc) : "v"(ma), "a"(wb))
#define MFMA_BV(acc, ma, wb) asm volatile("v_mfma_f32_16x16x32_bf16 %0, %1, %2, %0" : "+v"(acc) : "v"(ma), "v"(wb))
__device__ __forceinline__ void worker_kv(const u16* __restrict__ Wk, const u16* __restrict__ Wv, char* __restrict__ ws, char* smem, const int h) {
  const int tid = otid(), w = __builtin_amdgcn_readfirstlane(tid >> 6), l = tid & 63;
  const int c = l & 15, g = l >> 4;
  const char* xsrc = ws + OFF_XNF + (size_t)(10 * w) * 1024 + l * 16;
  auto stage = [&](int i, int buf) {
#pragma unroll
    for (int k = 0; k < 10; ++k) glds16(xsrc + (size_t)i * 40960 + k * 1024, smem + buf * 40960 + (10 * w + k) * 1024 + l * 16);
  };
  stage(0, 0); stage(1, 1);
  u32x4 wf[4][20];
  {
    const u32x4 S0 = sel_op(g == (c >> 2), c & 3), S1 = sel_op(g == (c >> 2), 4 + (c & 3));
    const u16* p = ((w >> 1) ? Wv : Wk) + p32(0, c) * HD + h * 128 + 64 * (w & 1) + 8 * g;
    u32x4 rb[10][4];
    auto ldb = [&](int ks) {
      u32x4* r = rb[ks % 10];
      r[0] = *(const u32x4*)(p + ks * 32 * HD); r[1] = *(const u32x4*)(p + ks * 32 * HD + 4 * HD);
      r[2] = *(const u32x4*)(p + ks * 32 * HD + 32); r[3] = *(const u32x4*)(p + ks * 32 * HD + 32 + 4 * HD);
    };
#pragma unroll
    for (int ks = 0; ks < 10; ++ks) ldb(ks);
#pragma unroll
    for (int ks = 0; ks < 20; ++ks) {
      __builtin_amdgcn_sched_barrier(0);
      const u32x4* r = rb[ks % 10];
      tr_frag2(r[0], r[1], S0, S1, wf[0][ks], wf[1][ks]);
      tr_frag2(r[2], r[3], S0, S1, wf[2][ks], wf[3][ks]);
      if (ks < 16) asm volatile("" : "+a"(wf[0][ks]), "+a"(wf[1][ks]), "+a"(wf[2][ks]), "+a"(wf[3][ks]));
      else asm volatile("" : "+v"(wf[0][ks]), "+v"(wf[1][ks]), "+v"(wf[2][ks]), "+v"(wf[3][ks]));
      __builtin_amdgcn_sched_barrier(0);
      if (ks + 10 < 20) ldb(ks + 10);
    }
    __builtin_amdgcn_sched_barrier(0);
  }
  const __amdgpu_buffer_rsrc_t rkv = __builtin_amdgcn_make_buffer_rsrc(ws + OFF_K, 0, (u32)(2 * SZ_Q), 0x00020000);
  const u32 ko = (u32)((p32(0, c) * HD + h * 128 + 64 * (w & 1) + 8 * g) * 2);
  const u32 vo = (u32)(SZ_Q + ((h * 128 + 64 * (w & 1) + p32(0, c)) * 32 + 8 * g) * 2);
  u32* kvc = (u32*)(ws + OFF_KVC) + h * 16;
  VMCNT(0);
  WSTAMPJ(3);
  int buf = 0;
  for (int sg = 0; sg < NSEG; ++sg) {
    VMCNT(14);
    RAWBAR();
    if (sg == 2) WSTAMPJ(12);
    if (sg == 3) WSTAMPJ(13);
    if (sg >= 2 && tid == 0) __hip_atomic_fetch_add(kvc + (sg - 2) * (32 * 16), 1u, __ATOMIC_RELAXED, __HIP_MEMORY_SCOPE_AGENT);
    const int b2 = buf == 0 ? 2 : buf - 1;
    stage(min(sg + 2, NSEG - 1), b2);
    const char* base = smem + buf * 40960 + l * 16;
    buf = buf == 2 ? 0 : buf + 1;
    f32x4 acc[2][4];
    u32x4 xa[3][2];
    auto ldx = [&](int ks) { xa[ks % 3][0] = *(const u32x4*)(base + ks * 1024); xa[ks % 3][1] = *(const u32x4*)(base + (20 + ks) * 1024); };
    ldx(0); ldx(1);
    if (w < 2) {
#pragma unroll
      for (int ks = 0; ks < 20; ++ks) {
        if (ks + 2 < 20) ldx(ks + 2);
        __builtin_amdgcn_sched_barrier(0);
#pragma unroll
        for (int j = 0; j < 4; ++j)
#pragma unroll
          for (int t = 0; t < 2; ++t) {
            if (ks == 0) MFMA_A0(acc[t][j], wf[j][ks], xa[ks % 3][t]);
            else if (ks < 16) MFMA_A(acc[t][j], wf[j][ks], xa[ks % 3][t]);
            else MFMA_V(acc[t][j], wf[j][ks], xa[ks % 3][t]);
          }
        __builtin_amdgcn_sched_barrier(0);
      }
    } else {
#pragma unroll
      for (int ks = 0; ks < 20; ++ks) {
        if (ks + 2 < 20) ldx(ks + 2);
        __builtin_amdgcn_sched_barrier(0);
#pragma unroll
        for (int j = 0; j < 4; ++j)
#pragma unroll
          for (int t = 0; t < 2; ++t) {
            if (ks == 0) MFMA_BA0(acc[t][j], xa[ks % 3][t], wf[j][ks]);
            else if (ks < 16) MFMA_BA(acc[t][j], xa[ks % 3][t], wf[j][ks]);
            else MFMA_BV(acc[t][j], xa[ks % 3][t], wf[j][ks]);
          }
        __builtin_amdgcn_sched_barrier(0);
      }
    }
    asm volatile("s_nop 7\n\ts_nop 7" : "+v"(acc[0][0]), "+v"(acc[0][1]), "+v"(acc[0][2]), "+v"(acc[0][3]), "+v"(acc[1][0]), "+v"(acc[1][1]), "+v"(acc[1][2]), "+v"(acc[1][3]));
    if (w < 2) {
#pragma unroll
      for (int t = 0; t < 2; ++t)
#pragma unroll
        for (int u = 0; u < 2; ++u) {
          const f32x4 a0 = acc[t][2 * u], a1 = acc[t][2 * u + 1];
          const u32x4 v = {pack2(a0.x, a0.y), pack2(a0.z, a0.w), pack2(a1.x, a1.y), pack2(a1.z, a1.w)};
          __builtin_amdgcn_raw_buffer_store_b128(v, rkv, ko + (u32)((4 * t * HD + 32 * u) * 2), (u32)sg * (32 * HD * 2), 16);
        }
    } else {
#pragma unroll
      for (int j = 0; j < 4; ++j) {
        const f32x4 a0 = acc[0][j], a1 = acc[1][j];
        const u32x4 v = {pack2(a0.x, a0.y), pack2(a0.z, a0.w), pack2(a1.x, a1.y), pack2(a1.z, a1.w)};
        __builtin_amdgcn_raw_buffer_store_b128(v, rkv, vo + (u32)((32 * (j >> 1) + 4 * (j & 1)) * 64), (u32)sg * (32 * HD * 2), 16);
      }
    }
  }
  VMCNT(0);
  RAWBAR();
  if (tid < 2) __hip_atomic_fetch_add(kvc + (NSEG - 2 + tid) * (32 * 16), 1u, __ATOMIC_RELAXED, __HIP_MEMORY_SCOPE_AGENT);
}

__device__ __forceinline__ void worker_qattn(const u16* __restrict__ Wq, char* __restrict__ ws, char* smem, const int mb0, const int mbstep, const int h) {
  const int tid = otid(), w = __builtin_amdgcn_readfirstlane(tid >> 6), l = tid & 63;
  const f32x4 z4 = {0, 0, 0, 0};
  const int c = l & 15, g = l >> 4;
  const char* xsrc = ws + OFF_XNF + (size_t)(10 * w) * 1024 + l * 16;
  auto stage = [&](int sg) {
#pragma unroll
    for (int k = 0; k < 10; ++k) glds16(xsrc + (size_t)sg * 40960 + k * 1024, smem + (sg & 1) * 40960 + (10 * w + k) * 1024 + l * 16);
  };
  stage(4 * mb0);
  u32x4 wf[2][20];
  {
    const u32x4 S0 = sel_op(g == (c >> 2), c & 3), S1 = sel_op(g == (c >> 2), 4 + (c & 3));
    const u16* p = Wq + p32(0, c) * HD + h * 128 + 32 * w + 8 * g;
    u32x4 rb[20][2];
#pragma unroll
    for (int ks = 0; ks < 20; ++ks) { rb[ks][0] = *(const u32x4*)(p + ks * 32 * HD); rb[ks][1] = *(const u32x4*)(p + ks * 32 * HD + 4 * HD); }
#pragma unroll
    for (int ks = 0; ks < 20; ++ks) {
      __builtin_amdgcn_sched_barrier(0);
      tr_frag2(rb[ks][0], rb[ks][1], S0, S1, wf[0][ks], wf[1][ks]);
      asm volatile("" : "+a"(wf[0][ks]), "+a"(wf[1][ks]));
    }
    __builtin_amdgcn_sched_barrier(0);
  }
  for (int mb = mb0; mb < 16; mb += mbstep) {
#pragma unroll 1
  for (int q4 = 0; q4 < 4; ++q4) {
    VMCNT(0);
    RAWBAR();
    if (q4 < 3) stage(4 * mb + q4 + 1);
    const char* base = smem + (q4 & 1) * 40960 + l * 16;
    f32x4 acc[2][2];
    u32x4 xa[4][2];
    auto ldx = [&](int ks) { xa[ks & 3][0] = *(const u32x4*)(base + ks * 1024); xa[ks & 3][1] = *(const u32x4*)(base + (20 + ks) * 1024); };
    ldx(0); ldx(1); ldx(2);
#pragma unroll
    for (int ks = 0; ks < 20; ++ks) {
      if (ks + 3 < 20) ldx(ks + 3);
      __builtin_amdgcn_sched_barrier(0);
#pragma unroll
      for (int t = 0; t < 2; ++t)
#pragma unroll
        for (int j = 0; j < 2; ++j) {
          if (ks == 0) MFMA_A0(acc[t][j], wf[j][ks], xa[ks & 3][t]);
          else MFMA_A(acc[t][j], wf[j][ks], xa[ks & 3][t]);
        }
      __builtin_amdgcn_sched_barrier(0);
    }
    asm volatile("s_nop 7\n\ts_nop 7" : "+v"(acc[0][0]), "+v"(acc[0][1]), "+v"(acc[1][0]), "+v"(acc[1][1]));
#pragma unroll
    for (int t = 0; t < 2; ++t) {
      const f32x4 a0 = acc[t][0], a1 = acc[t][1];
      const u32x4 v = {pack2(a0.x, a0.y), pack2(a0.z, a0.w), pack2(a1.x, a1.y), pack2(a1.z, a1.w)};
      *(u32x4*)(smem + 81920 + (((2 * q4 + t) * 4 + w) * 64 + l) * 16) = v;
    }
  }
  RAWBAR();
  if (mb + mbstep < 16) stage(4 * (mb + mbstep));
  const int s = mb * 4 + w;
  wait_count((const u32*)(ws + OFF_KVC) + (s * 32 + h) * 16, 1u, (u32*)(ws + OFF_ERR));
  const __amdgpu_buffer_rsrc_t rkv = __builtin_amdgcn_make_buffer_rsrc(ws + OFF_K, 0, (u32)(2 * SZ_Q), 0x00020000);
  const __amdgpu_buffer_rsrc_t rkm = __builtin_amdgcn_make_buffer_rsrc(ws + OFF_KM, 0, (u32)(2 * SZ_KM), 0x00020000);
  const __amdgpu_buffer_rsrc_t rof = __builtin_amdgcn_make_buffer_rsrc(ws + OFF_OF, 0, (u32)SZ_Q, 0x00020000);
  const float scale = 0.08838834764831845f;
  f32x4 sM[2], sX[2][2];
#pragma unroll
  for (int T = 0; T < 2; ++T) { sM[T] = z4; sX[0][T] = z4; sX[1][T] = z4; }
  bf16x8 q[4][2];
#pragma unroll
  for (int ks = 0; ks < 4; ++ks) {
    bf16x8 kx[2];
#pragma unroll
    for (int T = 0; T < 2; ++T) {
      q[ks][T] = *(const bf16x8*)(smem + 81920 + (((2 * w + T) * 4 + ks) * 64 + l) * 16);
      kx[T] = as_bf8(__builtin_amdgcn_raw_buffer_load_b128(rkv, (u32)(((32 * s + p32(T, c)) * HD + h * 128 + 32 * ks + 8 * g) * 2), 0, 16));
    }
#pragma unroll
    for (int T = 0; T < 2; ++T) {
      sX[0][T] = mfma32(kx[0], q[ks][T], sX[0][T]);
      sX[1][T] = mfma32(kx[1], q[ks][T], sX[1][T]);
    }
  }
  bf16x8 vxa[4][2];
#pragma unroll
  for (int u = 0; u < 4; ++u)
#pragma unroll
    for (int E = 0; E < 2; ++E)
      vxa[u][E] = as_bf8(__builtin_amdgcn_raw_buffer_load_b128(rkv, (u32)(SZ_Q + (((s * 32 + h) * 128 + 32 * u + p32(E, c)) * 32 + 8 * g) * 2), 0, 16));
  wait_count((const u32*)(ws + OFF_MCNT) + (s * 32 + h) * 16, 4u, (u32*)(ws + OFF_ERR));
  u32x2 vma[4][2];
  {
    u32x4 km[4];
#pragma unroll
    for (int ks = 0; ks < 4; ++ks) km[ks] = __builtin_amdgcn_raw_buffer_load_b128(rkm, (u32)(((s * 8 + (c & 7)) * HD + h * 128 + 32 * ks + 8 * g) * 2), 0, 16);
#pragma unroll
    for (int u = 0; u < 4; ++u)
#pragma unroll
      for (int E = 0; E < 2; ++E)
        vma[u][E] = __builtin_amdgcn_raw_buffer_load_b64(rkm, (u32)(SZ_KM + (((s * 32 + h) * 128 + 32 * u + p32(E, c)) * 8 + 4 * (g & 1)) * 2), 0, 16);
#pragma unroll
    for (int ks = 0; ks < 4; ++ks)
#pragma unroll
      for (int T = 0; T < 2; ++T) sM[T] = mfma32(as_bf8(km[ks]), q[ks][T], sM[T]);
  }
  u32x4 px[2]; u32x2 pm[2];
#pragma unroll
  for (int T = 0; T < 2; ++T) {
    const int qi = p32(T, c);
    float sm_[4], sx[8];
#pragma unroll
    for (int i = 0; i < 4; ++i) {
      sm_[i] = g < 2 ? bfr(bfr(sM[T][i]) * scale) : -INFINITY;
      float a = bfr(bfr(sX[0][T][i]) * scale), b = bfr(bfr(sX[1][T][i]) * scale);
      sx[i] = (8 * g + i <= qi) ? a : -1e9f;
      sx[4 + i] = (8 * g + 4 + i <= qi) ? b : -1e9f;
    }
    float mx = fmaxf(fmaxf(fmaxf(sx[0], sx[1]), fmaxf(sx[2], sx[3])), fmaxf(fmaxf(sx[4], sx[5]), fmaxf(sx[6], sx[7])));
    mx = fmaxf(mx, fmaxf(fmaxf(sm_[0], sm_[1]), fmaxf(sm_[2], sm_[3])));
    mx = xr32_max(xr16_max(mx));
    float sum = 0.f;
#pragma unroll
    for (int i = 0; i < 8; ++i) { sx[i] = __expf(sx[i] - mx); sum += sx[i]; }
#pragma unroll
    for (int i = 0; i < 4; ++i) { sm_[i] = __expf(sm_[i] - mx); sum += sm_[i]; }
    sum = xr32_add(xr16_add(sum));
    const float inv = 1.0f / sum;
    px[T] = (u32x4){pack2(sx[0] * inv, sx[1] * inv), pack2(sx[2] * inv, sx[3] * inv), pack2(sx[4] * inv, sx[5] * inv), pack2(sx[6] * inv, sx[7] * inv)};
    pm[T] = (u32x2){pack2(sm_[0] * inv, sm_[1] * inv), pack2(sm_[2] * inv, sm_[3] * inv)};
  }
#pragma unroll
  for (int u = 0; u < 4; ++u) {
    f32x4 o[2][2];
#pragma unroll
    for (int E = 0; E < 2; ++E) {
      u32x2 vm = vma[u][E];
      if (g >= 2) vm = (u32x2){0, 0};
#pragma unroll
      for (int T = 0; T < 2; ++T) {
        o[E][T] = mfma32(vxa[u][E], as_bf8(px[T]), z4);
        o[E][T] = mfma16(as_bf4(vm), as_bf4(pm[T]), o[E][T]);
      }
    }
#pragma unroll
    for (int T = 0; T < 2; ++T) {
      u32x4 v = {pack2(o[0][T].x, o[0][T].y), pack2(o[0][T].z, o[0][T].w), pack2(o[1][T].x, o[1][T].y), pack2(o[1][T].z, o[1][T].w)};
      __builtin_amdgcn_raw_buffer_store_b128(v, rof, (u32)((((2 * s + T) * 128 + (4 * h + u)) * 64 + l) * 16), 0, 16);
    }
  }
  asm volatile("s_waitcnt vmcnt(0)" ::: "memory");
  if (l == 0) __hip_atomic_fetch_add((u32*)(ws + OFF_OSEG) + s * 16, 1u, __ATOMIC_RELAXED, __HIP_MEMORY_SCOPE_AGENT);
  if (!(mb & 1)) WSTAMPJ(4 + (mb >> 1));
  }
}

__device__ __forceinline__ void final_store2(char* __restrict__ ws, u16* __restrict__ out, const int tok, const int n0, const f32x4 a0, const f32x4 a1,
                                             const u32x4 xv, const u32x4 nv, const int slot) {
  const float* rsel = (const float*)(ws + OFF_SEL) + 8;
  const float* yp = (const float*)(ws + OFF_YP);
  float a[8] = {a0.x, a0.y, a0.z, a0.w, a1.x, a1.y, a1.z, a1.w};
  float xf[8] = {bflo(xv.x), bfhi(xv.x), bflo(xv.y), bfhi(xv.y), bflo(xv.z), bfhi(xv.z), bflo(xv.w), bfhi(xv.w)};
  float nf[8] = {bflo(nv.x), bfhi(nv.x), bflo(nv.y), bfhi(nv.y), bflo(nv.z), bfhi(nv.z), bflo(nv.w), bfhi(nv.w)};
  if (slot >= 0) {
    const float rs = rsel[slot];
#pragma unroll
    for (int j = 0; j < 8; ++j) {
      float y = 0.f;
#pragma unroll
      for (int kb = 0; kb < 16; ++kb) y += yp[((size_t)kb * 8 + slot) * DM + n0 + j];
      y = bfr(bfr(y) * rs);
      nf[j] = bfr(nf[j] + y);
    }
  }
  float o[8];
#pragma unroll
  for (int j = 0; j < 8; ++j) o[j] = bfr(xf[j] + bfr(a[j])) + nf[j];
  u32x4 ov = {pack2(o[0], o[1]), pack2(o[2], o[3]), pack2(o[4], o[5]), pack2(o[6], o[7])};
  *(u32x4*)(out + (size_t)tok * DM + n0) = ov;
}

__device__ __forceinline__ void worker_wo_fine(const u16* __restrict__ x, const u16* __restrict__ Wo, char* __restrict__ ws, u16* __restrict__ out, char* smem, const int par, const int np,
                                               const int sg0, const int sgstep) {
  const int tid = otid(), w = __builtin_amdgcn_readfirstlane(tid >> 6), l = tid & 63;
  const int c = l & 15, g = l >> 4;
  u32x4 b0[32], b1[32];
  const int rot = (np * 8) / 5;
  {
    const u32x4 S0 = sel_op(g == (c >> 2), c & 3), S1 = sel_op(g == (c >> 2), 4 + (c & 3));
    const u16* p = Wo + (1024 * w + p32(0, c)) * DM + 32 * np + 8 * g;
    u32x4 rb[16][2];
    auto ldb = [&](int i) {
      const int kk = (i + rot) & 31;
      rb[i & 15][0] = *(const u32x4*)(p + kk * 32 * DM); rb[i & 15][1] = *(const u32x4*)(p + kk * 32 * DM + 4 * DM);
    };
#pragma unroll
    for (int i = 0; i < 16; ++i) ldb(i);
#pragma unroll
    for (int i = 0; i < 32; ++i) {
      __builtin_amdgcn_sched_barrier(0);
      tr_frag2(rb[i & 15][0], rb[i & 15][1], S0, S1, b0[i], b1[i]);
      asm volatile("" : "+a"(b0[i]), "+a"(b1[i]));
      __builtin_amdgcn_sched_barrier(0);
      if (i + 16 < 32) ldb(i + 16);
    }
    __builtin_amdgcn_sched_barrier(0);
  }
  const __amdgpu_buffer_rsrc_t rof = __builtin_amdgcn_make_buffer_rsrc(ws + OFF_OF, 0, (u32)SZ_Q, 0x00020000);
  const u16* xn = (const u16*)(ws + OFF_XN);
  int sel[8];
  if (w == 0) {
    wait_count((const u32*)(ws + OFF_YCNT), 80u, (u32*)(ws + OFF_ERR));
    __builtin_amdgcn_fence(__ATOMIC_ACQUIRE, "agent");
#pragma unroll
    for (int k = 0; k < 8; ++k) sel[k] = __builtin_amdgcn_readfirstlane(((const int*)(ws + OFF_SEL))[k]);
  }
  int it = 0, ready = -1;
  for (int sg = sg0; sg < 64; sg += sgstep, ++it) {
    const int mt = 2 * sg + par;
    const int tok = 32 * sg + p32(par, c), n0 = 32 * np + 8 * g;
    u32x4 xv, nv;
    if (w == 0) { xv = *(const u32x4*)(x + (size_t)tok * DM + n0); nv = *(const u32x4*)(xn + (size_t)tok * DM + n0); }
    if (w == 1 && sg > ready) {
      if (sg < WO_FAST) wait_count<WO_SLP>((const u32*)(ws + OFF_OSEG) + sg * 16, 32u, (u32*)(ws + OFF_ERR));
      else wait_count<4>((const u32*)(ws + OFF_OSEG) + sg * 16, 32u, (u32*)(ws + OFF_ERR));
      const u32 cv = __hip_atomic_load((const u32*)(ws + OFF_OSEG) + l * 16, __ATOMIC_RELAXED, __HIP_MEMORY_SCOPE_AGENT);
      const unsigned long long nok = ~(__builtin_amdgcn_ballot_w64(cv >= 32u) >> sg) & ~1ull;
      ready = sg + (nok ? __builtin_ctzll(nok) : 64) - 1;
    }
    __syncthreads();
    f32x4 acc0, acc1;
    const u32 abase = (u32)(((mt * 128 + 32 * w) * 64 + l) * 16);
    u32x4 a[32];
#pragma unroll
    for (int i = 0; i < 32; ++i) {
      a[i] = __builtin_amdgcn_raw_buffer_load_b128(rof, abase, ((i + rot) & 31) * 1024, 16);
    }
    __builtin_amdgcn_sched_barrier(0);
    MFMA_A0(acc0, b0[0], a[0]); MFMA_A0(acc1, b1[0], a[0]);
#pragma unroll
    for (int i = 1; i < 32; ++i) { MFMA_A(acc0, b0[i], a[i]); MFMA_A(acc1, b1[i], a[i]); }
    asm volatile("s_nop 7\n\ts_nop 7" : "+v"(acc0), "+v"(acc1));
    char* sb = smem + (it & 1) * 8192;
    if (w > 0) {
      *(f32x4*)(sb + (w * 2) * 1024 + l * 16) = acc0;
      *(f32x4*)(sb + (w * 2 + 1) * 1024 + l * 16) = acc1;
    }
    __syncthreads();
    if (w == 0) {
      const f32x4* p = (const f32x4*)(sb + l * 16);
      acc0 = (acc0 + p[2 * 64]) + (p[4 * 64] + p[6 * 64]);
      acc1 = (acc1 + p[3 * 64]) + (p[5 * 64] + p[7 * 64]);
      int slot = -1;
#pragma unroll
      for (int k = 0; k < 8; ++k) if (sel[k] == tok) slot = k;
      final_store2(ws, out, tok, n0, acc0, acc1, xv, nv, slot);
    }
  }
}

template <typename T> __device__ __forceinline__ T* opq(T* p) { asm volatile("" : "+s"(p)); return p; }

__global__ __launch_bounds__(256, 1) void main_kernel(const u16* __restrict__ x, const u16* __restrict__ Wq, const u16* __restrict__ Wk,
                                                      const u16* __restrict__ Wv, const u16* __restrict__ Wo, const u16* __restrict__ wg,
                                                      const u16* __restrict__ wu, const u16* __restrict__ wd, char* __restrict__ ws,
                                                      u16* __restrict__ out) {
  extern __shared__ __attribute__((aligned(16))) char smem[];
  const int b = blockIdx.x;
  if (b < 128) {
    chain_role(Wq, Wk, Wv, Wo, ws, smem, b);
  } else {
    const int j = b - 128;
    if (j < 32) {
      worker_kv(Wk, Wv, ws, smem, HMAP == 1 ? 4 * (j & 7) + (j >> 3) : j);
    } else {
      __builtin_amdgcn_s_sleep(127); __builtin_amdgcn_s_sleep(127); __builtin_amdgcn_s_sleep(127);
      if (j < 64) worker_qattn(Wq, ws, smem, 0, 1, HMAP == 1 ? 4 * (j & 7) + ((j >> 3) & 3) : j - 32);
      else if (j < 104) worker_wo_fine(x, Wo, ws, out, smem, (j - 64) / 20, (j - 64) % 20, 0, 1);
      else {
        worker_modA(wg, wu, ws, smem, j - 104, 24);
        WSTAMP(1);
        for (int blk = j - 104; blk < 80; blk += 24) { __syncthreads(); worker_modB(opq(wd), opq(ws), smem, blk); }
        WSTAMP(2);
      }
    }
    WSTAMP(15);
  }
}

extern "C" void kernel_launch(void* const* d_in, const int* in_sizes, int n_in, void* d_out, int out_size, void* d_ws, size_t ws_size,
                              hipStream_t stream) {
  const u16* x = (const u16*)d_in[0];
  const u16* mem0 = (const u16*)d_in[1];
  const u16* Wq = (const u16*)d_in[2];
  const u16* Wk = (const u16*)d_in[3];
  const u16* Wv = (const u16*)d_in[4];
  const u16* Wo = (const u16*)d_in[5];
  const u16* w_r = (const u16*)d_in[8];
  const u16* wg = (const u16*)d_in[9];
  const u16* wu = (const u16*)d_in[10];
  const u16* wd = (const u16*)d_in[11];
  char* ws = (char*)d_ws;
  u16* out = (u16*)d_out;
  (void)hipFuncSetAttribute((const void*)main_kernel, hipFuncAttributeMaxDynamicSharedMemorySize, CH_LDS);
  prep_kernel<<<PREP_BLOCKS, 256, 0, stream>>>(x, mem0, w_r, ws);
  main_kernel<<<256, 256, CH_LDS, stream>>>(x, Wq, Wk, Wv, Wo, wg, wu, wd, ws, out);
}
```

```cpp
#include <hip/hip_runtime.h>
#include <hip/hip_bf16.h>
#include <stdint.h>
#include <type_traits>

constexpr int S_TOK = 2048, DM = 640, NH = 32, DH = 128, HD = 4096, NMEM = 8, SEG = 32, NSEG = 64;
constexpr int FF = 1728, CSEL = 8;
constexpr int KS_DM = DM / 32;
constexpr int KS_HD = HD / 32;

typedef unsigned int u32;
typedef unsigned short u16;
typedef __attribute__((ext_vector_type(4))) u32 u32x4;
typedef __attribute__((ext_vector_type(2))) u32 u32x2;
typedef __attribute__((ext_vector_type(4))) float f32x4;
typedef __attribute__((ext_vector_type(8))) short bf16x8;
typedef __attribute__((ext_vector_type(4))) short bf16x4;
typedef __attribute__((ext_vector_type(2))) __bf16 nbf16x2;

#define SENT 0xFFFFFFFFu
#define VMCNT(n) asm volatile("s_waitcnt vmcnt(%0)" ::"i"(n) : "memory")
#define RAWBAR() do { asm volatile("s_waitcnt lgkmcnt(0)" ::: "memory"); __builtin_amdgcn_s_barrier(); asm volatile("" ::: "memory"); } while (0)

#define TP0 54
#define ARM_SG 12
#define HINT0 63
#define ARM_PREP
static_assert(TP0 > ARM_SG + (64 - TP0) + 4 && TP0 > 20 && TP0 <= 62, "");
__device__ __forceinline__ float bf2f(u16 v) { return __uint_as_float(((u32)v) << 16); }
__device__ __forceinline__ float bflo(u32 v) { return __uint_as_float(v << 16); }
__device__ __forceinline__ float bfhi(u32 v) { return __uint_as_float(v & 0xffff0000u); }
__device__ __forceinline__ u32 pack2(float a, float b) {
  nbf16x2 v = {(__bf16)a, (__bf16)b};
  return __builtin_bit_cast(u32, v);
}
__device__ __forceinline__ float bfr(float a) { return (float)(__bf16)a; }
__device__ __forceinline__ bf16x8 as_bf8(u32x4 v) { return __builtin_bit_cast(bf16x8, v); }
__device__ __forceinline__ bf16x4 as_bf4(u32x2 v) { return __builtin_bit_cast(bf16x4, v); }
__device__ __forceinline__ f32x4 mfma32(bf16x8 a, bf16x8 b, f32x4 c) { return __builtin_amdgcn_mfma_f32_16x16x32_bf16(a, b, c, 0, 0, 0); }
__device__ __forceinline__ f32x4 mfma16(bf16x4 a, bf16x4 b, f32x4 c) {
  u32x2 a2 = __builtin_bit_cast(u32x2, a), b2 = __builtin_bit_cast(u32x2, b);
  u32x4 a4 = {a2.x, a2.y, 0u, 0u}, b4 = {b2.x, b2.y, 0u, 0u};
  return __builtin_amdgcn_mfma_f32_16x16x32_bf16(__builtin_bit_cast(bf16x8, a4), __builtin_bit_cast(bf16x8, b4), c, 0, 0, 0);
}
__device__ __forceinline__ float xr16_max(float v) { auto r = __builtin_amdgcn_permlane16_swap(__float_as_uint(v), __float_as_uint(v), false, false); return fmaxf(__uint_as_float(r[0]), __uint_as_float(r[1])); }
__device__ __forceinline__ float xr32_max(float v) { auto r = __builtin_amdgcn_permlane32_swap(__float_as_uint(v), __float_as_uint(v), false, false); return fmaxf(__uint_as_float(r[0]), __uint_as_float(r[1])); }
__device__ __forceinline__ float xr16_add(float v) { auto r = __builtin_amdgcn_permlane16_swap(__float_as_uint(v), __float_as_uint(v), false, false); return __uint_as_float(r[0]) + __uint_as_float(r[1]); }
__device__ __forceinline__ float xr32_add(float v) { auto r = __builtin_amdgcn_permlane32_swap(__float_as_uint(v), __float_as_uint(v), false, false); return __uint_as_float(r[0]) + __uint_as_float(r[1]); }
__device__ __forceinline__ float xr8_max(float v) { return fmaxf(v, __uint_as_float(__builtin_amdgcn_update_dpp(0u, __float_as_uint(v), 0x128  , 0xf, 0xf, false))); }
__device__ __forceinline__ float xr8_add(float v) { return v + __uint_as_float(__builtin_amdgcn_update_dpp(0u, __float_as_uint(v), 0x128  , 0xf, 0xf, false)); }
__device__ __forceinline__ float wave_sum(float v) {
  v += __uint_as_float(__builtin_amdgcn_update_dpp(0u, __float_as_uint(v), 0xB1  , 0xf, 0xf, false));
  v += __uint_as_float(__builtin_amdgcn_update_dpp(0u, __float_as_uint(v), 0x4E  , 0xf, 0xf, false));
  v += __uint_as_float(__builtin_amdgcn_update_dpp(0u, __float_as_uint(v), 0x124  , 0xf, 0xf, false));
  return xr32_add(xr16_add(xr8_add(v)));
}
#define PST_AUX 16
__device__ __forceinline__ void st16(char* base, u32 off, u32x4 v) {
  __builtin_amdgcn_raw_buffer_store_b128(v, __builtin_amdgcn_make_buffer_rsrc(base, 0, 0x7fffffffu, 0x00020000), off, 0, PST_AUX);
}
__device__ __forceinline__ int p32(int T, int r) { return 8 * (r >> 2) + 4 * T + (r & 3); }

__device__ __forceinline__ u32x4 sel_op(bool on, int pos) {
  const u32 one = on ? ((pos & 1) ? 0x3f800000u : 0x3f80u) : 0u;
  const int d = pos >> 1;
  return (u32x4){d == 0 ? one : 0u, d == 1 ? one : 0u, d == 2 ? one : 0u, d == 3 ? one : 0u};
}
__device__ __forceinline__ void tr_half2(u32x4 a, u32x4 s, u32x4 b, u32x4 t, u32x2& x, u32x2& y) {
  f32x4 d, e;
  asm volatile("s_nop 1\n\tv_mfma_f32_16x16x32_bf16 %0, %2, %3, 0\n\tv_mfma_f32_16x16x32_bf16 %1, %4, %5, 0\n\ts_nop 7\n\ts_nop 3"
               : "=&v"(d), "=&v"(e) : "v"(a), "v"(s), "v"(b), "v"(t));
  x = (u32x2){pack2(d.x, d.y), pack2(d.z, d.w)};
  y = (u32x2){pack2(e.x, e.y), pack2(e.z, e.w)};
}
__device__ __forceinline__ void tr_frag2(u32x4 a0, u32x4 a1, u32x4 s0, u32x4 s1, u32x4& f0, u32x4& f1) {
  f32x4 d0, d1, d2, d3;
  asm volatile("s_nop 1\n\tv_mfma_f32_16x16x32_bf16 %0, %4, %6, 0\n\tv_mfma_f32_16x16x32_bf16 %1, %5, %6, 0\n\t"
               "v_mfma_f32_16x16x32_bf16 %2, %4, %7, 0\n\tv_mfma_f32_16x16x32_bf16 %3, %5, %7, 0\n\ts_nop 7\n\ts_nop 3"
               : "=&v"(d0), "=&v"(d1), "=&v"(d2), "=&v"(d3) : "v"(a0), "v"(a1), "v"(s0), "v"(s1));
  f0 = (u32x4){pack2(d0.x, d0.y), pack2(d0.z, d0.w), pack2(d1.x, d1.y), pack2(d1.z, d1.w)};
  f1 = (u32x4){pack2(d2.x, d2.y), pack2(d2.z, d2.w), pack2(d3.x, d3.y), pack2(d3.z, d3.w)};
}

typedef __attribute__((ext_vector_type(4))) short s16x4;
__device__ __forceinline__ u32x2 trd(const char* p) {
  return __builtin_bit_cast(u32x2, __builtin_amdgcn_ds_read_tr16_b64_v4i16((__attribute__((address_space(3))) s16x4*)p));
}
__device__ __forceinline__ u32x4 j4(u32x2 a, u32x2 b) { return (u32x4){a.x, a.y, b.x, b.y}; }
__device__ __forceinline__ u32x4 swp(u32x4 v) { return (u32x4){v.z, v.w, v.x, v.y}; }

__host__ __device__ __forceinline__ int piece_col(int p, int t) {
  const int w = p / 20, r = p % 20;
  const int blk = r < 16 ? 2 * w + (r >> 3) : 8 + (w >> 1), sel = r < 16 ? (r >> 2) & 1 : w & 1, g = r & 3;
  return 64 * blk + 16 * g + 4 * (t >> 1) + 2 * sel + (t & 1);
}

constexpr size_t SZ_XN = (size_t)S_TOK * DM * 2;
constexpr size_t OFF_XN = 0;
constexpr size_t OFF_XNF = OFF_XN + SZ_XN;
constexpr size_t OFF_R = OFF_XNF + SZ_XN;
constexpr size_t OFF_WF = OFF_R + 8192;
constexpr size_t SZ_WF = (size_t)768 * 20 * 1024;
constexpr size_t OFF_WOF = OFF_WF + SZ_WF;
constexpr size_t SZ_WOF = (size_t)40 * 128 * 1024;
constexpr size_t OFF_Q = OFF_WOF + SZ_WOF;
constexpr size_t SZ_Q = (size_t)S_TOK * HD * 2;
constexpr size_t OFF_K = OFF_Q + SZ_Q;
constexpr size_t OFF_VT = OFF_K + SZ_Q;
constexpr size_t OFF_KM = OFF_VT + SZ_Q;
constexpr size_t SZ_KM = (size_t)NSEG * NMEM * HD * 2;
constexpr size_t OFF_VMT = OFF_KM + SZ_KM;
constexpr size_t OFF_OF = OFF_VMT + SZ_KM;
constexpr size_t OFF_INBOX = OFF_OF + SZ_Q;
constexpr u32 NPROD = 128, NRED = 80;
constexpr u32 INBOX_SLOT = NRED * NPROD * 128;
constexpr size_t OFF_MEMB = OFF_INBOX + 2 * (size_t)INBOX_SLOT;
#define MREP_N 8
constexpr u32 MREP = MREP_N;
constexpr u32 MEM_REP = NRED * 128;
constexpr u32 MEM_STEP = MREP * MEM_REP;
constexpr size_t SZ_MEMB = (size_t)65 * MEM_STEP;
constexpr size_t OFF_HB = OFF_MEMB + ((SZ_MEMB + 255) / 256) * 256;
constexpr size_t OFF_YP = OFF_HB + 32768;
constexpr size_t OFF_SEL = OFF_YP + (size_t)16 * 8 * 640 * 4;
constexpr size_t OFF_ERR = OFF_SEL + 256;
constexpr size_t OFF_MCNT = OFF_ERR + 256;
constexpr size_t OFF_OSEG = OFF_MCNT + (size_t)64 * 32 * 64;
constexpr size_t OFF_KVC = OFF_OSEG + 40 * 1024;
constexpr size_t OFF_HCNT = OFF_KVC + (size_t)64 * 32 * 64;
constexpr size_t OFF_YCNT = OFF_HCNT + 512;
constexpr size_t OFF_OCNT = OFF_YCNT + 512;
constexpr size_t OFF_GO = OFF_OCNT + 64 * 64;
constexpr size_t OFF_O0F = OFF_GO + 40 * 256;
constexpr size_t OFF_FLAG_END = OFF_O0F + 20 * 128;
constexpr size_t OFF_QM0 = OFF_Q;
constexpr size_t OFF_O0 = OFF_Q + (1 << 20);
constexpr size_t WS_NEED = OFF_FLAG_END;

#define PW_AUX 0
#define FS 0
#define NSH 1
constexpr int NSK = FS + NSH;
constexpr int NSP = FS;
#define PIN 96
#define PFK 0
#define PFM0 Wk
#define PFM1 Wq
#define PFM2 Wv
constexpr int PREP_G = FS ? 96 : 0, PREP_LN = 64, PREP_IN = PIN, PREP_BLOCKS = PREP_G + PREP_LN + PREP_IN;
constexpr int PREP_LDS = FS ? 163840 : 40960;

__device__ __forceinline__ float row_sum(float v) {
  v += __uint_as_float(__builtin_amdgcn_update_dpp(0u, __float_as_uint(v), 0xB1  , 0xf, 0xf, false));
  v += __uint_as_float(__builtin_amdgcn_update_dpp(0u, __float_as_uint(v), 0x4E  , 0xf, 0xf, false));
  v += __uint_as_float(__builtin_amdgcn_update_dpp(0u, __float_as_uint(v), 0x124  , 0xf, 0xf, false));
  return xr8_add(v);
}
__device__ __forceinline__ void ln_rows(u32x4* c) {
  float s = 0.f;
#pragma unroll
  for (int i = 0; i < 5; ++i) s += ((bflo(c[i].x) + bfhi(c[i].x)) + (bflo(c[i].y) + bfhi(c[i].y))) + ((bflo(c[i].z) + bfhi(c[i].z)) + (bflo(c[i].w) + bfhi(c[i].w)));
  const float mean = row_sum(s) * (1.0f / DM);
  float q = 0.f;
#pragma unroll
  for (int i = 0; i < 5; ++i)
#pragma unroll
    for (int e = 0; e < 4; ++e) { const float d0 = bflo(c[i][e]) - mean, d1 = bfhi(c[i][e]) - mean; q += d0 * d0; q += d1 * d1; }
  const float var_b = bfr(row_sum(q) * (1.0f / DM));
  const float mean_b = bfr(mean);
  const float den = bfr(sqrtf(bfr(var_b + 1e-05f)));
  const float rden = __builtin_amdgcn_rcpf(den);
#pragma unroll
  for (int i = 0; i < 5; ++i)
#pragma unroll
    for (int e = 0; e < 4; ++e) c[i][e] = pack2(bfr(bflo(c[i][e]) - mean_b) * rden, bfr(bfhi(c[i][e]) - mean_b) * rden);
}
__device__ __forceinline__ void st8(char* base, u32 off, u32x2 v) {
  __builtin_amdgcn_raw_buffer_store_b64(v, __builtin_amdgcn_make_buffer_rsrc(base, 0, 0x7fffffffu, 0x00020000), off, 0, PST_AUX);
}
#define PST(i)

template <int MAT>
__device__ __forceinline__ void prep_gemm(const u16* __restrict__ x, const u16* __restrict__ mem0, const u16* __restrict__ W, char* __restrict__ ws, char* lds, const int h) {
  const int tid = threadIdx.x, w = __builtin_amdgcn_readfirstlane(tid >> 6), l = tid & 63;
  const int c = l & 15, g = l >> 4;
  constexpr int NT = MAT < 2 ? 2 : 0;
  char* xl = lds + 112640;
  char* ml = xl + 40960;
  const int tl = w >> 2, row = 4 * (w & 3) + g;
  u32x4 xc[5];
  if (MAT < 2) {
#pragma unroll
    for (int i = 0; i < 5; ++i) xc[i] = *(const u32x4*)(x + (size_t)p32(tl, row) * DM + (c + 16 * i) * 8);
  }
  const u32x4 m0 = *(const u32x4*)(mem0 + (tid & 7) * DM + (tid >> 3) * 8);
  const u32x4 m1 = *(const u32x4*)(mem0 + (tid & 7) * DM + (64 + ((tid & 127) >> 3)) * 8);
  __builtin_amdgcn_sched_barrier(0);
  const int r0 = 16 * (w >> 1) + 4 * (w & 1) + g;
  const __amdgpu_buffer_rsrc_t rw = __builtin_amdgcn_make_buffer_rsrc((void*)W, 0, DM * HD * 2, 0x00020000);
  char* wr = lds + r0 * 256 + ((c ^ (g << 2)) * 16);
  u32x4 rr[20];
  auto wld = [&](int i) { rr[i] = __builtin_amdgcn_raw_buffer_load_b128(rw, (u32)(r0 * 8192 + h * 256 + c * 16), (64 * (i >> 1) + 8 * (i & 1)) * 8192, PW_AUX); };
  auto wst = [&](int i) { *(u32x4*)(wr + (i >> 1) * 16384 + (i & 1) * 2048) = (i & 1) ? swp(rr[i]) : rr[i]; };
#pragma unroll
  for (int i = 0; i < 10; ++i) wld(i);
  __builtin_amdgcn_sched_barrier(0);
  PST(1);
  if (MAT < 2) {
    ln_rows(xc);
#pragma unroll
    for (int i = 0; i < 5; ++i) *(u32x4*)(xl + tl * 20480 + (c + 16 * i) * 256 + ((row ^ c) << 4)) = xc[i];
  }
  *(u32x4*)(ml + tid * 16) = m0;
  *(u32x4*)(ml + 8192 + (tid & 127) * 16) = m1;
  __builtin_amdgcn_sched_barrier(0);
#pragma unroll
  for (int i = 0; i < 10; ++i) { wld(10 + i); wst(i); __builtin_amdgcn_sched_barrier(0); }
  RAWBAR();
  PST(3);
  const int blk = w & 3, kh = w >> 2, q_ = c >> 2, p_ = c & 3;
  bf16x8 mo[10], xo[NT ? NT : 1][10];
#pragma unroll
  for (int k = 0; k < 10; ++k) {
    const int kk = (k < 5 ? k : k + 5);
    mo[k] = *(const bf16x8*)(ml + (5 * kh + kk) * 512 + g * 128 + (c & 7) * 16);
#pragma unroll
    for (int t = 0; t < NT; ++t) xo[t][k] = *(const bf16x8*)(xl + t * 20480 + (5 * kh + kk) * 1024 + g * 256 + ((c ^ ((4 * (kh + kk) + g) & 15)) << 4));
  }
  const char* rd = lds + kh * 40960 + (8 * g + q_) * 256 + (((4 * blk + p_) ^ (q_ << 2)) * 16);
  f32x4 ax[NT ? NT : 1][2], am[2];
#pragma unroll
  for (int T = 0; T < 2; ++T) {
    am[T] = (f32x4){0, 0, 0, 0};
#pragma unroll
    for (int t = 0; t < NT; ++t) ax[t][T] = am[T];
  }
#pragma unroll
  for (int hf = 0; hf < 2; ++hf) {
    if (hf) { RAWBAR(); PST(4); }
#pragma unroll
    for (int k = 5 * hf; k < 5 * hf + 5; ++k)
#pragma unroll
      for (int T = 0; T < 2; ++T) {
        const char* a = rd + (k + 5 * hf) * 8192 + (((g & 1) ^ T) * 8);
        const bf16x8 wf = as_bf8(j4(trd(a), trd(a + 1024)));
        am[T] = MAT == 1 ? mfma32(mo[k], wf, am[T]) : mfma32(wf, mo[k], am[T]);
#pragma unroll
        for (int t = 0; t < NT; ++t) ax[t][T] = MAT == 1 ? mfma32(xo[t][k], wf, ax[t][T]) : mfma32(wf, xo[t][k], ax[t][T]);
      }
    __builtin_amdgcn_sched_barrier(0);
    if (hf == 0) {
      wst(10); wst(11);
      RAWBAR();
#pragma unroll
      for (int i = 12; i < 20; ++i) wst(i);
    }
  }
  PST(5);
  RAWBAR();
  char* pr = lds + (w ^ 4) * 3072 + l * 16;
  *(f32x4*)(pr) = kh ? am[0] : am[1];
#pragma unroll
  for (int t = 0; t < NT; ++t) *(f32x4*)(pr + (1 + t) * 1024) = kh ? ax[t][0] : ax[t][1];
  RAWBAR();
  PST(6);
  const int T = kh;
  const char* pq = lds + w * 3072 + l * 16;
  const f32x4 fm = kh ? *(const f32x4*)(pq) + am[1] : am[0] + *(const f32x4*)(pq);
  const u32x2 pm = {pack2(fm.x, fm.y), pack2(fm.z, fm.w)};
  u32x2 px[NT ? NT : 1];
#pragma unroll
  for (int t = 0; t < NT; ++t) {
    const f32x4 o = *(const f32x4*)(pq + (1 + t) * 1024);
    const f32x4 f = kh ? o + ax[t][1] : ax[t][0] + o;
    px[t] = (u32x2){pack2(f.x, f.y), pack2(f.z, f.w)};
  }
  const u32 hb = (u32)(h * 4 + blk);
  if (MAT == 1) {
    st16(ws, (u32)OFF_VT + hb * 2048 + T * 1024 + l * 16, j4(px[0], px[NT ? 1 : 0]));
    if (g < 2) st8(ws, (u32)OFF_VMT + hb * 512 + T * 256 + l * 8, pm);
  } else {
#pragma unroll
    for (int t = 0; t < NT; ++t) st8(ws, (u32)OFF_K + hb * 2048 + t * 1024 + l * 16 + T * 8, px[t]);
    if (c < 8) st8(ws, (u32)(MAT == 0 ? OFF_KM : OFF_QM0) + hb * 512 + g * 128 + c * 16 + T * 8, pm);
  }
}

__global__ __launch_bounds__(512, 1) void prep_kernel(const u16* __restrict__ x, const u16* __restrict__ mem0, const u16* __restrict__ w_r,
                                                      const u16* __restrict__ Wq, const u16* __restrict__ Wk, const u16* __restrict__ Wv,
                                                      const u16* __restrict__ Wo, char* __restrict__ ws) {
  extern __shared__ __attribute__((aligned(16))) char lds[];
  const int b = blockIdx.x, tid = threadIdx.x;
  const int w = __builtin_amdgcn_readfirstlane(tid >> 6), l = tid & 63;
  const u32x4 sv = {SENT, SENT, SENT, SENT};
  PST(0);
  if (b < PREP_G + PREP_LN) {
    const int sg = b - PREP_G, c = l & 15;
    const int tl = w >> 2, row = 4 * (w & 3) + (l >> 4), tok = 32 * sg + p32(tl, row);
    u32x4 xc[5], wc[5];
#pragma unroll
    for (int i = 0; i < 5; ++i) xc[i] = *(const u32x4*)(x + (size_t)tok * DM + (c + 16 * i) * 8);
#pragma unroll
    for (int i = 0; i < 5; ++i) wc[i] = *(const u32x4*)(w_r + (c + 16 * i) * 8);
    ln_rows(xc);
#pragma unroll
    for (int i = 0; i < 5; ++i) *(u32x4*)(lds + tl * 20480 + (c + 16 * i) * 256 + ((row ^ c) << 4)) = xc[i];
    __syncthreads();
#pragma unroll
    for (int k = 0; k < 5; ++k) {
      const int q = tid + 512 * k;
      st16(ws, (u32)(OFF_XNF + (size_t)sg * 40960) + q * 16, *(const u32x4*)(lds + (q & ~15) * 16 + (((q ^ (q >> 4)) & 15) << 4)));
    }
    float r = 0.f;
#pragma unroll
    for (int i = 0; i < 5; ++i)
#pragma unroll
      for (int e = 0; e < 4; ++e) r += bflo(xc[i][e]) * bflo(wc[i][e]) + bfhi(xc[i][e]) * bfhi(wc[i][e]);
    r = row_sum(r);
    if (c == 0) __builtin_amdgcn_raw_buffer_store_b32(__float_as_uint(bfr(r)), __builtin_amdgcn_make_buffer_rsrc(ws, 0, 0x7fffffffu, 0x00020000), (u32)OFF_R + tok * 4, 0, PST_AUX);
  } else {
    const int i = b - (PREP_G + PREP_LN);
    const u32 t0 = i * 512 + tid;
    if (t0 == 0) st16(ws, (u32)OFF_ERR, (u32x4){0, 0, 0, 0});
    for (u32 j = t0; j < INBOX_SLOT / 16; j += PREP_IN * 512) st16(ws, (u32)(OFF_INBOX + FS * INBOX_SLOT) + j * 16, sv);
    for (u32 j = (FS ? 0 : MEM_STEP / 16) + t0; j < 4 * MEM_STEP / 16; j += PREP_IN * 512) st16(ws, (u32)OFF_MEMB + j * 16, sv);
    for (u32 j = t0; j < (NSEG - TP0) * 4096; j += PREP_IN * 512) {
      st16(ws, (u32)(OFF_KM + TP0 * 65536) + j * 16, sv);
      st16(ws, (u32)(OFF_VMT + TP0 * 65536) + j * 16, sv);
    }
    for (u32 j = t0; j < (64 - HINT0) * 16384; j += PREP_IN * 512) st16(ws, (u32)(OFF_OF + 2 * HINT0 * 131072) + j * 16, sv);
    if (!FS && i >= PREP_IN - 2) {
      const int j = (i - (PREP_IN - 2)) * 512 + tid;
      if (j < 640) {
        const int p_ = j >> 3, m = j & 7;
        u32x4 v = {*(const u32*)(mem0 + m * DM + piece_col(p_, 0)), *(const u32*)(mem0 + m * DM + piece_col(p_, 2)),
                   *(const u32*)(mem0 + m * DM + piece_col(p_, 4)), *(const u32*)(mem0 + m * DM + piece_col(p_, 6))};
        if (v.x == SENT) v.x = 0xFFC0FFC0u;
        if (v.y == SENT) v.y = 0xFFC0FFC0u;
        if (v.z == SENT) v.z = 0xFFC0FFC0u;
        if (v.w == SENT) v.w = 0xFFC0FFC0u;
        for (u32 rp = 0; rp < MREP; ++rp) st16(ws, (u32)OFF_MEMB + rp * MEM_REP + j * 16, v);
      }
    }
    for (u32 j = t0; j < (u32)((OFF_FLAG_END - OFF_MCNT) / 16); j += PREP_IN * 512) {
      const u32 mc = (FS && j < 128 && !(j & 3)) ? 1u : 0u;
      const u32 jk = j - (u32)((OFF_KVC - OFF_MCNT) / 16);
      const u32 kv = (jk < 128 * NSP && !(jk & 3)) ? 1u : 0u;
      st16(ws, (u32)OFF_MCNT + j * 16, (u32x4){mc | kv, mc | kv, mc, mc});
    }
  }
  PST(7);
}

__device__ __forceinline__ void glds16(const void* g, void* l) {
  __builtin_amdgcn_global_load_lds((const __attribute__((address_space(1))) void*)g, (__attribute__((address_space(3))) void*)l, 16, 0, 0);
}

constexpr u32 SPIN_CAP = 1u << 16;
#define NO_PF
#define HMAP 1
#define KV_IMM (2 + NSK)
#define KV_OLD
#define KV_MID 10
#define S_LATE 1
#define WO_SC1
#define SLP_NR 38
#define SLP_RI 6
#define SLP_D1 12
#define SLP_D2 6
#define WO_SLP 16
#define WO_FAST 56
static_assert(WO_FAST >= 56 && WO_FAST <= 61, "at most the last eight segments are announced by per-head flags");
#define WO_SLP2 4
#define SLP_RM 12
#define WO_CHUNK 4
#define WO_PEND 58
#define PG_AUX "sc1"
#define PI_AUX "sc1"
#define WF_AUX 16
#define NR_G 2
#define NR_I 2
#define G_ROUND "buffer_load_dwordx4 v[240:243], %[o], %[rs], 0 offen sc1\n\tbuffer_load_dwordx4 v[244:247], %[o], %[rs], 0 offen offset:1024 sc1\n\t" \
                "buffer_load_dwordx4 v[248:251], %[o2], %[rs], 0 offen offset:2048 sc1\n\tbuffer_load_dword v252, off, %[rf], %[fo] sc1\n\ts_sleep %[slp]\n\t"
#define I_ROUND "buffer_load_dwordx4 v[224:227], %[o], %[rs], 0 offen offset:0 sc1\n\tbuffer_load_dwordx4 v[228:231], %[o], %[rs], 0 offen offset:1024 sc1\n\t" \
                "buffer_load_dwordx4 v[232:235], %[o], %[rs], 0 offen offset:2048 sc1\n\tbuffer_load_dwordx4 v[236:239], %[o], %[rs], 0 offen offset:3072 sc1\n\ts_sleep %[slp]\n\t"
#define G_EXTRA ""
#define I_EXTRA ""
#define HC_SLP 32
#define YC_SLP 64
#define MC_SLP 8
#define SIG_KS 8
#define QT_WAIT 295u
#define QT_WAIT2 200u
#define WO_WAIT 200u
#define QT_MARG 10u
#define QT_LAST 100u
#define QT_RT 50u
#define QT_SLP2 2
#define QT_SLP 4
#define QA_SLP 32
#define R0_SLP 100
#define QA_PEND 62
__device__ __forceinline__ bool has_sent(u32x4 v) { return (v.x == SENT) | (v.y == SENT) | (v.z == SENT) | (v.w == SENT); }
__device__ __forceinline__ u32 desent(float f) { u32 b = __float_as_uint(f); return b == SENT ? 0x7FC00000u : b; }

template <int SLP = 8>
__device__ __forceinline__ void wait_count(const u32* p, u32 want, u32* err) {
  u32 spins = 0;
  for (;;) {
    u32 v = __builtin_amdgcn_readfirstlane(__hip_atomic_load(p, __ATOMIC_RELAXED, __HIP_MEMORY_SCOPE_AGENT));
    if (v >= want) break;
    __builtin_amdgcn_s_sleep(SLP);
    if (++spins > (1u << 20)) { *err = 3; break; }
  }
}

__device__ __forceinline__ void set_flag2(char* ws, size_t off) {
  __builtin_amdgcn_raw_buffer_store_b64((u32x2){1u, 1u}, __builtin_amdgcn_make_buffer_rsrc(ws + OFF_MCNT, 0, (u32)(OFF_FLAG_END - OFF_MCNT), 0x00020000), (u32)(off - OFF_MCNT), 0, 16);
}
template <int SLP>
__device__ __forceinline__ void wait_flag2(char* ws, size_t off) {
  const __amdgpu_buffer_rsrc_t r = __builtin_amdgcn_make_buffer_rsrc(ws + OFF_MCNT, 0, (u32)(OFF_FLAG_END - OFF_MCNT), 0x00020000);
  u32 spins = 0;
  for (;;) {
    asm volatile("" ::: "memory");
    const u32x2 v = __builtin_amdgcn_raw_buffer_load_b64(r, 0, (u32)(off - OFF_MCNT), 16);
    if (__builtin_amdgcn_readfirstlane(v.x & v.y)) break;
    __builtin_amdgcn_s_sleep(SLP);
    if (++spins > (1u << 20)) { *(u32*)(ws + OFF_ERR) = 3; break; }
  }
}
__device__ __forceinline__ void set_flag(char* ws, size_t off) {
  __builtin_amdgcn_raw_buffer_store_b32(1u, __builtin_amdgcn_make_buffer_rsrc(ws + OFF_MCNT, 0, (u32)(OFF_FLAG_END - OFF_MCNT), 0x00020000), (u32)(off - OFF_MCNT), 0, 16);
}
template <int SLP>
__device__ __forceinline__ unsigned long long wait_flags(char* ws, size_t off, const int n, u32 lim) {
  const __amdgpu_buffer_rsrc_t r = __builtin_amdgcn_make_buffer_rsrc(ws + OFF_MCNT, 0, lim - (u32)OFF_MCNT, 0x00020000);
  const u32 o = (u32)(off - OFF_MCNT) + (threadIdx.x & 63) * 8;
  const unsigned long long want = n >= 64 ? ~0ull : (1ull << n) - 1;
  u32 spins = 0;
  for (;;) {
    asm volatile("" ::: "memory");
    const u32x2 v = __builtin_amdgcn_raw_buffer_load_b64(r, o, 0, WF_AUX);
    const unsigned long long ok = __builtin_amdgcn_ballot_w64((v.x != 0) & (v.y != 0));
    if ((ok & want) == want) return ok;
    __builtin_amdgcn_s_sleep(SLP);
    if (++spins > (1u << 20)) { *(u32*)(ws + OFF_ERR) = 3; return ~0ull; }
  }
}

__device__ __forceinline__ u32x4 mk_rsrc(const void* p, u32 n) {
  const unsigned long long a = (unsigned long long)p;
  return (u32x4){(u32)__builtin_amdgcn_readfirstlane((u32)a), (u32)__builtin_amdgcn_readfirstlane((u32)(a >> 32) & 0xffffu), n, 0x00020000u};
}

constexpr int CH_MEMF = 0, CH_PART = 20480, CH_WV = 40960, CH_WO = 81920, CH_SRED = 122880, CH_LDS = 163840;

#define MFMA_A(acc, wa, mb) asm volatile("v_mfma_f32_16x16x32_bf16 %0, %1, %2, %0" : "+v"(acc) : "a"(wa), "v"(mb))
#define MFMA_V(acc, wa, mb) asm volatile("v_mfma_f32_16x16x32_bf16 %0, %1, %2, %0" : "+v"(acc) : "v"(wa), "v"(mb))
#define MFMA_A0(acc, wa, mb) asm volatile("v_mfma_f32_16x16x32_bf16 %0, %1, %2, 0" : "=&v"(acc) : "a"(wa), "v"(mb))
#define MFMA_V0(acc, wa, mb) asm volatile("v_mfma_f32_16x16x32_bf16 %0, %1, %2, 0" : "=&v"(acc) : "v"(wa), "v"(mb))
#define MFMA_NV(acc, wa, mb) asm volatile("s_nop 1\n\tv_mfma_f32_16x16x32_bf16 %0, %1, %2, %0" : "+v"(acc) : "v"(wa), "v"(mb))
#define MFMA_NV0(acc, wa, mb) asm volatile("s_nop 1\n\tv_mfma_f32_16x16x32_bf16 %0, %1, %2, 0" : "=&v"(acc) : "v"(wa), "v"(mb))

#define SMF_A(acc, a, b) asm volatile("v_smfmac_f32_16x16x64_bf16 %0, %1, %2, %3" : "+v"(acc) : "v"(a), "a"(b), "v"(sidx))
#define SMF_V(acc, a, b) asm volatile("v_smfmac_f32_16x16x64_bf16 %0, %1, %2, %3" : "+v"(acc) : "v"(a), "v"(b), "v"(sidx))
#define MFMA16_N0(acc, a, b) asm volatile("s_nop 1\n\tv_mfma_f32_16x16x16_bf16 %0, %1, %2, 0" : "=&v"(acc) : "v"(a), "v"(b))
#define SMF_P(R, C, acc, a, b) asm volatile("v_smfmac_f32_16x16x64_bf16 %0, %1, %2, %3" : "+{" R "}"(acc) : "v"(a), C(b), "v"(sidx))
#define SQ0(C, a, b) SMF_P("v[208:211]", C, aq0, a, b)
#define SQ1(C, a, b) SMF_P("v[212:215]", C, aq1, a, b)
#define SK0(C, a, b) SMF_P("v[216:219]", C, ak0, a, b)
#define SK1(C, a, b) SMF_P("v[220:223]", C, ak1, a, b)
typedef __attribute__((ext_vector_type(8))) u32 u32x8;
typedef __attribute__((ext_vector_type(2))) float f32x2;
constexpr int PSTR = 4160;
__device__ __forceinline__ float hsum32(float a, float b) { auto r = __builtin_amdgcn_permlane32_swap(__float_as_uint(a), __float_as_uint(b), false, false); return __uint_as_float(r[0]) + __uint_as_float(r[1]); }
__device__ __forceinline__ u32 ror8(u32 v) { return (u32)__builtin_amdgcn_update_dpp(0u, v, 0x128  , 0xf, 0xf, false); }

#define STAMP(i)
#define CST(i)
__device__ __forceinline__ void chain_role(const u16* __restrict__ Wq, const u16* __restrict__ Wk, const u16* __restrict__ Wv, const u16* __restrict__ Wo,
                                           char* __restrict__ ws, char* smem, const int c) {
  const int tid = threadIdx.x, w = __builtin_amdgcn_readfirstlane(tid >> 6), l = tid & 63;
  const int h = 4 * (c & 7) + (c >> 5), gs = (c >> 3) & 3;
  const int m = l & 15, g = l >> 4;
  u32x8 wq[2][10], wk[2][10];
  {
    int l = tid & 63;
    asm volatile("" : "+v"(l));
    const int m = l & 15, g = l >> 4;
    const int r0 = 16 * (w >> 1) + 4 * (w & 1) + g;
    const u32 so = (u32)(r0 * 8192 + h * 256 + m * 16);
    const u32 lb = (u32)(uintptr_t)(__attribute__((address_space(3))) char*)smem + r0 * 256 + ((m ^ (g << 2)) * 16);
    const int q_ = m >> 2, p_ = m & 3;
    const char* rd0 = smem + (8 * g + q_) * 256 + (((4 * w + p_) ^ (q_ << 2)) * 16) + (g & 1) * 8;
    const char* rd1 = smem + (8 * g + q_) * 256 + (((4 * w + p_) ^ (q_ << 2)) * 16) + ((g & 1) ^ 1) * 8;
    auto j8 = [](u32x4 lo, u32x4 hi) { return (u32x8){lo.x, lo.y, lo.z, lo.w, hi.x, hi.y, hi.z, hi.w}; };
    u32x4 rr[40], rv[10], ro[12];
#pragma unroll
    for (int t = 0; t < 40; ++t) rr[t] = __builtin_amdgcn_raw_buffer_load_b128(__builtin_amdgcn_make_buffer_rsrc((void*)Wq, 0, DM * HD * 2, 0x00020000), so, (32 * (t >> 1) + 8 * (t & 1)) * 8192, 0);
#pragma unroll
    for (int mat = 0; mat < 2; ++mat) {
      __builtin_amdgcn_sched_barrier(0);
      if (mat == 1) asm volatile("" : "+v"(wq[0][8]), "+v"(wq[1][8]), "+v"(wq[0][9]), "+v"(wq[1][9]));
      const u32 ov = (u32)(((160 * w + (l >> 2)) * HD + h * 128 + 32 * gs + 8 * (l & 3)) * 2);
      const u16* po = Wo + (h * 128 + 32 * gs) * DM;
#pragma unroll
      for (int t = 0; t < 40; ++t) {
        const u32 ad = lb + (t >> 4) * 65536;
        asm volatile("ds_write_b128 %0, %1 offset:%2" ::"v"(ad), "v"((t & 1) ? swp(rr[t]) : rr[t]), "i"(((t >> 1) & 7) * 8192 + (t & 1) * 2048) : "memory");
        __builtin_amdgcn_sched_barrier(0);
        if (mat == 0) {
          rr[t] = __builtin_amdgcn_raw_buffer_load_b128(__builtin_amdgcn_make_buffer_rsrc((void*)Wk, 0, DM * HD * 2, 0x00020000), so, (32 * (t >> 1) + 8 * (t & 1)) * 8192, 0);
        } else {
          if (t < 8) ro[t] = *(const u32x4*)(po + (4 * t + g) * DM + 128 * w + 8 * m);
          else if (t < 12) ro[t] = *(const u32x4*)(po + (8 * (t - 8) + (l >> 3)) * DM + 64 * (8 + (w >> 1)) + 8 * (l & 7));
          else if (t < 22) rv[t - 12] = __builtin_amdgcn_raw_buffer_load_b128(__builtin_amdgcn_make_buffer_rsrc((void*)Wv, 0, DM * HD * 2, 0x00020000), ov, (t - 12) * (16 * 8192), 0);
        }
        __builtin_amdgcn_sched_barrier(0);
      }
      __builtin_amdgcn_sched_barrier(0);
      asm volatile("s_waitcnt lgkmcnt(0)" ::: "memory");
      __builtin_amdgcn_s_barrier();
      asm volatile("" ::: "memory");
#pragma unroll
      for (int i = 0; i < 10; ++i) {
        const int blk = i < 8 ? 2 * ((w + (i >> 1)) & 3) + (i & 1) : i;
        const char *a = rd0 + blk * 16384, *b = rd1 + blk * 16384;
        u32x8 a0 = j8(j4(trd(a), trd(a + 1024)), j4(trd(a + 8192), trd(a + 9216)));
        u32x8 a1 = j8(j4(trd(b), trd(b + 1024)), j4(trd(b + 8192), trd(b + 9216)));
        if (mat == 0) { wq[0][i] = a0; wq[1][i] = a1; } else { wk[0][i] = a0; wk[1][i] = a1; }
      }
#pragma unroll
      for (int i = 0; i < 10; ++i) {
        if (mat == 0) asm volatile("" : "+a"(wq[0][i]), "+a"(wq[1][i]));
        else if (i < 8) asm volatile("" : "+a"(wk[0][i]), "+a"(wk[1][i]));
        else asm volatile("" : "+v"(wk[0][i]), "+v"(wk[1][i]));
      }
      asm volatile("s_waitcnt lgkmcnt(0)" ::: "memory");
      __builtin_amdgcn_s_barrier();
      asm volatile("" ::: "memory");
    }
    {
      char* av = smem + w * 10240;
      char* ao = smem + 122880 + w * 8192;
      auto mix = [](u32x4 v) { return (u32x4){v.x, v.z, v.y, v.w}; };
#pragma unroll
      for (int t = 0; t < 10; ++t) *(u32x4*)(av + t * 1024 + l * 16) = rv[t];
#pragma unroll
      for (int t = 0; t < 8; ++t) *(u32x4*)(ao + (4 * t + g) * 256 + ((m ^ ((g & 1) | ((g & 2) << 2))) * 16)) = mix(ro[t]);
      const char* r = av + (8 * g + q_) * 64 + p_ * 16;
#pragma unroll
      for (int i = 0; i < 5; ++i) {
        *(u32x4*)(smem + CH_WV + (5 * w + i) * 1024 + l * 16) = j4(trd(r + i * 2048), trd(r + i * 2048 + 256));
        *(u32x4*)(smem + CH_WV + (20 + 5 * w + i) * 1024 + l * 16) = j4(trd(r + i * 2048 + 8), trd(r + i * 2048 + 264));
      }
#pragma unroll
      for (int t = 0; t < 4; ++t) *(u32x4*)(av + t * 1024 + l * 16) = mix(ro[8 + t]);
      const int fx = (q_ & 1) | ((q_ & 2) << 2);
#pragma unroll
      for (int f = 0; f < 10; ++f) {
        const int u = f >> 1, row = 16 * (f & 1) + 4 * g + q_;
        u32x2 x0, x1;
        if (u < 4) {
          const char* a = ao + row * 256 + (((8 * (u >> 1) + 2 * p_ + (u & 1)) ^ fx) * 16);
          x0 = trd(a); x1 = trd(a + 8);
        } else {
          const char* a = av + row * 128 + p_ * 16 + (w & 1) * 8;
          x0 = trd(a); x1 = trd(a + 64);
        }
        *(u32x4*)(smem + CH_WO + (10 * w + f) * 1024 + l * 16) = (u32x4){x0.x, x1.x, x0.y, x1.y};
      }
    }
    __syncthreads();
    __builtin_amdgcn_sched_barrier(0);
  }
  if (tid < 4) *(f32x4*)(smem + CH_PART + tid * PSTR + 4096) = (f32x4){-INFINITY, -INFINITY, -INFINITY, -INFINITY};
  __syncthreads();
  u32 sidx2 = (w & 1) ? 0xEEEEu : 0x4444u;
  u32 sidx = m < 8 ? 0x4444u : 0xEEEEu;
  asm volatile("" : "+v"(sidx), "+v"(sidx2));
  const u32 one_at = (m & 1) ? 0x3f800000u : 0x3f80u;
  const u32x2 idn = {(g == (m >> 2) && !(m & 2)) ? one_at : 0u, (g == (m >> 2) && (m & 2)) ? one_at : 0u};

  const __amdgpu_buffer_rsrc_t rin = __builtin_amdgcn_make_buffer_rsrc(ws + OFF_INBOX, 0, 2 * INBOX_SLOT, 0x00020000);
  const __amdgpu_buffer_rsrc_t rmem = __builtin_amdgcn_make_buffer_rsrc(ws + OFF_MEMB, 0, (u32)SZ_MEMB, 0x00020000);
  const u32x4 din = mk_rsrc(ws + OFF_INBOX, 2 * INBOX_SLOT), dmem = mk_rsrc(ws + OFF_MEMB, (u32)SZ_MEMB), dkv = mk_rsrc(ws + OFF_K, (u32)(2 * SZ_Q));
  const u32x4 dflag = mk_rsrc(ws + OFF_KVC, 64 * 32 * 64);
  const __amdgpu_buffer_rsrc_t rkm = __builtin_amdgcn_make_buffer_rsrc(ws + OFF_KM, 0, (u32)(2 * SZ_KM), 0x00020000);
  u32* err = (u32*)(ws + OFF_ERR);
  const float scale = 0.08838834764831845f;
  const char* lmem = smem + CH_MEMF + l * 16;

  u32x4 qt[4], mfo[3];
  u32 qf = 0;
#pragma unroll
  for (int i = 0; i < 4; ++i) qt[i] = (u32x4){0, 0, 0, 0};
#pragma unroll
  for (int i = 0; i < 3; ++i) mfo[i] = (u32x4){0, 0, 0, 0};
  u32x4 kv4[4];
  const u32 kvo0 = (u32)((h * 4 + w) * 2048 + l * 16), kvo2 = (u32)(SZ_Q + (h * 4 + gs) * 2048 + l * 16);
  auto load_kv = [&](int sn) {
    const u32 so = (u32)sn * (32 * HD * 2);
    asm volatile("buffer_load_dwordx4 v[192:195], %[o0], %[rs], %[so] offen sc1\n\t"
                 "buffer_load_dwordx4 v[196:199], %[o0], %[rs], %[so] offen offset:1024 sc1\n\t"
                 "buffer_load_dwordx4 v[200:203], %[o2], %[rs], %[so] offen sc1\n\t"
                 "buffer_load_dwordx4 v[204:207], %[o2], %[rs], %[so] offen offset:1024 sc1"
                 : "=&{v[192:195]}"(kv4[0]), "=&{v[196:199]}"(kv4[1]), "=&{v[200:203]}"(kv4[2]), "=&{v[204:207]}"(kv4[3])
                 : [o0] "v"(kvo0), [o2] "v"(kvo2), [rs] "s"(dkv), [so] "s"(so)
                 : "memory");
  };
  const __amdgpu_buffer_rsrc_t rmc = __builtin_amdgcn_make_buffer_rsrc(ws + OFF_MCNT, 0, 64 * 32 * 64, 0x00020000);
  const u32 mco = tid == 0 ? (u32)(h * 64 + gs * 4) : 0x80000000u;
  const u32 rso = c < (int)NRED ? (u32)(c * NPROD * 128 + w * 4096 + l * 16) : 0x80000000u;
  wait_flag2<8>(ws, OFF_KVC + (FS * 32 + h) * 64);
  load_kv(FS);
  auto step = [&](const int s, auto last, auto late) __attribute__((always_inline)) {
    constexpr bool LAST = decltype(last)::value, LATE = decltype(late)::value;
    u32x8 wv[3][2];
#pragma unroll
    for (int j = 0; j < 3; ++j)
#pragma unroll
      for (int T = 0; T < 2; ++T) {
        const char* pw = smem + CH_WV + (T * 20 + 2 * (j < 2 ? 2 * w + j : 8 + (w >> 1))) * 1024 + l * 16;
        const u32x4 lo = *(const u32x4*)(pw), hi = *(const u32x4*)(pw + 1024);
        wv[j][T] = (u32x8){lo.x, lo.y, lo.z, lo.w, hi.x, hi.y, hi.z, hi.w};
      }
    f32x4 aq0, aq1, ak0, ak1, av0, av1;
    asm volatile("v_mov_b64 v[164:165], 0\n\tv_mov_b64 v[166:167], 0\n\tv_mov_b64 v[168:169], 0\n\tv_mov_b64 v[170:171], 0\n\t"
                 "v_mov_b64 v[208:209], 0\n\tv_mov_b64 v[210:211], 0\n\tv_mov_b64 v[212:213], 0\n\tv_mov_b64 v[214:215], 0\n\t"
                 "v_mov_b64 v[216:217], 0\n\tv_mov_b64 v[218:219], 0\n\tv_mov_b64 v[220:221], 0\n\tv_mov_b64 v[222:223], 0"
                 : "={v[208:211]}"(aq0), "={v[212:215]}"(aq1), "={v[216:219]}"(ak0), "={v[220:223]}"(ak1), "={v[164:167]}"(av0), "={v[168:171]}"(av1));
    {
      const u32 o = (s & 3) * MEM_STEP + (c & (MREP - 1)) * MEM_REP + w * 2560 + (m >> 3) * 512 + g * 128 + (m & 7) * 16;
      const u32 o2 = m < 8 ? o : 0x80000000u;
      const u32 la = (u32)(uintptr_t)(__attribute__((address_space(3))) char*)smem + CH_MEMF + w * 2048 + l * 16;
      const u32 la2 = (u32)(uintptr_t)(__attribute__((address_space(3))) char*)smem + CH_MEMF + (m < 8 ? (8 + (w >> 1)) * 1024 + (g * 16 + m + 8 * (w & 1)) * 16 : 10240 + tid * 16);
      u32 tmx, cnt;
      unsigned long long m0, m1, m2, mo;
      const u32 fo = (u32)((LATE ? s : min(s + 1, NSEG - 1)) * 32 + h) * 64;
      asm volatile(
          "buffer_load_dwordx4 v[240:243], %[o], %[rs], 0 offen " PG_AUX "\n\t"
          "buffer_load_dwordx4 v[244:247], %[o], %[rs], 0 offen offset:1024 " PG_AUX "\n\t"
          "buffer_load_dwordx4 v[248:251], %[o2], %[rs], 0 offen offset:2048 " PG_AUX "\n\t"
          "buffer_load_dword v252, off, %[rf], %[fo] sc1\n\t"
          "s_mov_b32 %[cnt], 0\n\t"
          "s_mov_b64 %[m0], -1\n\t"
          "s_mov_b64 %[m1], -1\n\t"
          "s_mov_b64 %[m2], -1\n\t"
          "s_sleep %[slp]\n\t"
          G_EXTRA
          "\n1:\n\t"
          "s_mov_b64 exec, %[m0]\n\t"
          "buffer_load_dwordx4 v[240:243], %[o], %[rs], 0 offen " PG_AUX "\n\t"
          "s_mov_b64 exec, %[m1]\n\t"
          "buffer_load_dwordx4 v[244:247], %[o], %[rs], 0 offen offset:1024 " PG_AUX "\n\t"
          "s_mov_b64 exec, %[m2]\n\t"
          "buffer_load_dwordx4 v[248:251], %[o2], %[rs], 0 offen offset:2048 " PG_AUX "\n\t"
          "s_mov_b64 exec, -1\n\t"
          "buffer_load_dword v252, off, %[rf], %[fo] sc1\n\t"
          "s_waitcnt vmcnt(%[nw])\n\t"
          "v_max3_u32 %[t], v240, v241, v242\n\t"
          "v_max_u32 %[t], %[t], v243\n\t"
          "v_cmp_eq_u32 %[m0], -1, %[t]\n\t"
          "v_max3_u32 %[t], v244, v245, v246\n\t"
          "v_max_u32 %[t], %[t], v247\n\t"
          "v_cmp_eq_u32 %[m1], -1, %[t]\n\t"
          "v_max3_u32 %[t], v248, v249, v250\n\t"
          "v_max_u32 %[t], %[t], v251\n\t"
          "v_cmp_eq_u32 %[m2], -1, %[t]\n\t"
          "v_cmp_eq_u32 vcc, 0, v252\n\t"
          "s_or_b64 %[mo], %[m0], %[m1]\n\t"
          "s_or_b64 vcc, vcc, %[m2]\n\t"
          "s_or_b64 %[mo], %[mo], vcc\n\t"
          "s_cbranch_scc0 2f\n\t"
          "s_add_u32 %[cnt], %[cnt], 1\n\t"
          "s_cmp_lt_u32 %[cnt], 0x20000\n\t"
          "s_cbranch_scc1 1b\n"
          "2:\n\t"
          "ds_write_b128 %[la], v[240:243] offset:0\n\t"
          "ds_write_b128 %[la], v[244:247] offset:1024\n\t"
          "ds_write_b128 %[la2], v[248:251]"
          : "+{v[240:243]}"(mfo[0]), "+{v[244:247]}"(mfo[1]), "+{v[248:251]}"(mfo[2]), "+{v252}"(qf), [t] "=&v"(tmx), [cnt] "=&s"(cnt),
            [m0] "=&s"(m0), [m1] "=&s"(m1), [m2] "=&s"(m2), [mo] "=&s"(mo),
            "+{v[192:195]}"(kv4[0]), "+{v[196:199]}"(kv4[1]), "+{v[200:203]}"(kv4[2]), "+{v[204:207]}"(kv4[3])
          : [o] "v"(o), [o2] "v"(o2), [rs] "s"(dmem), [la] "v"(la), [la2] "v"(la2), [slp] "i"(SLP_D2), [rf] "s"(dflag), [fo] "s"(fo), [nw] "i"(4 * (NR_G - 1))
          : "vcc", "scc", "memory");
      if (cnt >= 0x20000u && l == 0) *err = 1;
    }
    STAMP(0);
    u32x8 wo[5];
    u32x4 kp;
    u32x2 vm0, vm1;
    auto ldwo = [&](int u) {
      const u32x4 lo = *(const u32x4*)(smem + CH_WO + (10 * w + 2 * u) * 1024 + l * 16), hi = *(const u32x4*)(smem + CH_WO + (10 * w + 2 * u + 1) * 1024 + l * 16);
      wo[u] = (u32x8){lo.x, lo.y, lo.z, lo.w, hi.x, hi.y, hi.z, hi.w};
    };
    {
      u32x4 mf[8];
      SMF_P("v[164:167]", "v", av0, mfo[0], wv[0][0]); SMF_P("v[168:171]", "v", av1, mfo[0], wv[0][1]);
      SMF_P("v[164:167]", "v", av0, mfo[1], wv[1][0]); SMF_P("v[168:171]", "v", av1, mfo[1], wv[1][1]);
      { const u32 sidx = sidx2; SMF_P("v[164:167]", "v", av0, mfo[2], wv[2][0]); SMF_P("v[168:171]", "v", av1, mfo[2], wv[2][1]); }
      asm volatile("s_waitcnt lgkmcnt(0)" ::: "memory");
      __syncthreads();
      STAMP(1);
      CST(0);
#pragma unroll
      for (int i = 0; i < 3; ++i) {
        const char* lc = lmem + ((w + 1 + i) & 3) * 2048;
        mf[2 * i] = *(const u32x4*)(lc); mf[2 * i + 1] = *(const u32x4*)(lc + 1024);
      }
      mf[6] = *(const u32x4*)(lmem + 8 * 1024);
      mf[7] = *(const u32x4*)(lmem + 9 * 1024);
      __builtin_amdgcn_sched_barrier(0);
      SQ0("a", mfo[0], wq[0][0]); SQ1("a", mfo[0], wq[1][0]); SK0("a", mfo[0], wk[0][0]); SK1("a", mfo[0], wk[1][0]);
      const u32 pp = (u32)(uintptr_t)(__attribute__((address_space(3))) char*)smem + CH_PART + w * PSTR + l * 16;
      asm volatile(
          "v_smfmac_f32_16x16x64_bf16 v[208:211], %[m], %[b0], %[ix]\n\t"
          "v_permlane32_swap_b32 v164, v168\n\t"
          "v_permlane32_swap_b32 v165, v169\n\t"
          "v_smfmac_f32_16x16x64_bf16 v[212:215], %[m], %[b1], %[ix]\n\t"
          "v_permlane32_swap_b32 v166, v170\n\t"
          "v_permlane32_swap_b32 v167, v171\n\t"
          "v_smfmac_f32_16x16x64_bf16 v[216:219], %[m], %[b2], %[ix]\n\t"
          "v_add_f32 v164, v164, v168\n\t"
          "v_add_f32 v165, v165, v169\n\t"
          "v_smfmac_f32_16x16x64_bf16 v[220:223], %[m], %[b3], %[ix]\n\t"
          "v_add_f32 v166, v166, v170\n\t"
          "v_add_f32 v167, v167, v171\n\t"
          "ds_write_b128 %[pp], v[164:167] offset:3072"
          : "+{v[208:211]}"(aq0), "+{v[212:215]}"(aq1), "+{v[216:219]}"(ak0), "+{v[220:223]}"(ak1), "+{v[164:167]}"(av0), "+{v[168:171]}"(av1)
          : [m] "v"(mfo[1]), [b0] "a"(wq[0][1]), [b1] "a"(wq[1][1]), [b2] "a"(wk[0][1]), [b3] "a"(wk[1][1]), [ix] "v"(sidx), [pp] "v"(pp)
          : "memory");
      __builtin_amdgcn_raw_buffer_store_b32(1u, rmc, s > 0 ? mco : 0x80000000u, (u32)(s - 1) * (32 * 64), 16);
      __builtin_amdgcn_sched_barrier(0);
      {
        u32x4 sv = {SENT, SENT, SENT, SENT};
        asm volatile("" : "+v"(sv));
        const u32 ro = rso;
        const u32 rb = ((s - 1) & 1) * INBOX_SLOT;
#pragma unroll
        for (int i = 0; i < 4; ++i) {
          SQ0("a", mf[i], wq[0][2 + i]); SQ1("a", mf[i], wq[1][2 + i]); SK0("a", mf[i], wk[0][2 + i]); SK1("a", mf[i], wk[1][2 + i]);
          __builtin_amdgcn_raw_buffer_store_b128(sv, rin, ro, rb + i * 1024, 16);
          if (i < 3) ldwo(i);
          __builtin_amdgcn_sched_barrier(0);
        }
      }
      CST(1);
      SQ0("a", mf[4], wq[0][6]); SQ1("a", mf[4], wq[1][6]); SQ0("a", mf[5], wq[0][7]); SQ1("a", mf[5], wq[1][7]);
      SQ0("v", mf[6], wq[0][8]); SQ1("v", mf[6], wq[1][8]); SQ0("v", mf[7], wq[0][9]); SQ1("v", mf[7], wq[1][9]);
      SK0("a", mf[4], wk[0][6]); SK1("a", mf[4], wk[1][6]);
      const u32 px = m < 8 ? pp + 1024 : (u32)(uintptr_t)(__attribute__((address_space(3))) char*)smem + CH_MEMF + 10240 + tid * 16;
      asm volatile(
          "v_smfmac_f32_16x16x64_bf16 v[216:219], %[m5], %[k05], %[ix]\n\t"
          "v_permlane32_swap_b32 v208, v212\n\t"
          "v_permlane32_swap_b32 v209, v213\n\t"
          "v_smfmac_f32_16x16x64_bf16 v[220:223], %[m5], %[k15], %[ix]\n\t"
          "v_permlane32_swap_b32 v210, v214\n\t"
          "v_permlane32_swap_b32 v211, v215\n\t"
          "v_smfmac_f32_16x16x64_bf16 v[216:219], %[m6], %[k06], %[ix]\n\t"
          "v_add_f32 v208, v208, v212\n\t"
          "v_add_f32 v209, v209, v213\n\t"
          "v_smfmac_f32_16x16x64_bf16 v[220:223], %[m6], %[k16], %[ix]\n\t"
          "v_add_f32 v210, v210, v214\n\t"
          "v_add_f32 v211, v211, v215\n\t"
          "v_smfmac_f32_16x16x64_bf16 v[216:219], %[m7], %[k07], %[ix]\n\t"
          "v_cvt_pk_bf16_f32 v208, v208, v209\n\t"
          "v_cvt_pk_bf16_f32 v209, v210, v211\n\t"
          "v_smfmac_f32_16x16x64_bf16 v[220:223], %[m7], %[k17], %[ix]\n\t"
          "s_nop 0\n\t"
          "v_mfma_f32_16x16x16_bf16 v[212:215], v[208:209], %[idn], 0\n\t"
          "s_nop 5\n\t"
          "v_permlane32_swap_b32 v216, v220\n\t"
          "v_permlane32_swap_b32 v217, v221\n\t"
          "v_permlane32_swap_b32 v218, v222\n\t"
          "v_permlane32_swap_b32 v219, v223\n\t"
          "v_cvt_pk_bf16_f32 v184, v212, v213\n\t"
          "v_cvt_pk_bf16_f32 v185, v214, v215\n\t"
          "v_add_f32 v216, v216, v220\n\t"
          "v_add_f32 v217, v217, v221\n\t"
          "v_mov_b32_dpp v186, v184 row_ror:8 row_mask:0xf bank_mask:0xf\n\t"
          "v_mov_b32_dpp v187, v185 row_ror:8 row_mask:0xf bank_mask:0xf\n\t"
          "v_add_f32 v218, v218, v222\n\t"
          "v_add_f32 v219, v219, v223\n\t"
          "v_mfma_f32_16x16x32_bf16 v[172:175], v[192:195], v[184:187], 0\n\t"
          "v_cvt_pk_bf16_f32 v216, v216, v217\n\t"
          "v_cvt_pk_bf16_f32 v217, v218, v219\n\t"
          "v_mfma_f32_16x16x32_bf16 v[176:179], v[196:199], v[184:187], 0\n\t"
          "s_nop 0\n\t"
          "v_mfma_f32_16x16x16_bf16 v[220:223], v[216:217], %[idn], 0\n\t"
          "s_nop 2\n\t"
          "ds_write_b128 %[px], v[172:175]\n\t"
          "s_nop 1\n\t"
          "ds_write_b128 %[px], v[176:179] offset:128\n\t"
          "s_nop 0\n\t"
          "v_cvt_pk_bf16_f32 v188, v220, v221\n\t"
          "v_cvt_pk_bf16_f32 v189, v222, v223\n\t"
          "s_nop 1\n\t"
          "v_mov_b32_dpp v190, v188 row_ror:8 row_mask:0xf bank_mask:0xf\n\t"
          "v_mov_b32_dpp v191, v189 row_ror:8 row_mask:0xf bank_mask:0xf\n\t"
          "s_nop 1\n\t"
          "v_mfma_f32_16x16x32_bf16 v[180:183], v[188:191], v[184:187], 0\n\t"
          "s_nop 7\n\t"
          "ds_write_b128 %[pp], v[180:183]\n\t"
          "s_waitcnt lgkmcnt(0)"
          : "+{v[208:211]}"(aq0), "+{v[212:215]}"(aq1), "+{v[216:219]}"(ak0), "+{v[220:223]}"(ak1), "=&{v[188:191]}"(kp)
          : [m5] "v"(mf[5]), [m6] "v"(mf[6]), [m7] "v"(mf[7]), [k05] "a"(wk[0][7]), [k15] "a"(wk[1][7]), [k06] "v"(wk[0][8]), [k16] "v"(wk[1][8]),
            [k07] "v"(wk[0][9]), [k17] "v"(wk[1][9]), [ix] "v"(sidx), [idn] "v"(idn), "{v[192:195]}"(kv4[0]), "{v[196:199]}"(kv4[1]),
            [pp] "v"(pp), [px] "v"(px)
          : "v172", "v173", "v174", "v175", "v176", "v177", "v178", "v179", "v180", "v181", "v182", "v183",
            "v184", "v185", "v186", "v187", "memory");
    }
    CST(2);
    STAMP(2);
    if (LAST && w == gs && m < 8) __builtin_amdgcn_raw_buffer_store_b128(kp, rkm, (u32)((h * 4 + gs) * 512 + g * 128 + m * 16), (u32)s * 65536, 16);
    asm volatile("" ::: "memory");
    __builtin_amdgcn_s_barrier();
    asm volatile("" ::: "memory");
    STAMP(3);
    CST(3);
    if constexpr (LAST) {
      const char* pb = smem + CH_PART + 3072 + l * 16;
      const f32x4 t = (*(const f32x4*)(pb) + *(const f32x4*)(pb + PSTR)) + (*(const f32x4*)(pb + 2 * PSTR) + *(const f32x4*)(pb + 3 * PSTR));
      if (w == ((gs + 1) & 3)) __builtin_amdgcn_raw_buffer_store_b64((u32x2){pack2(t.x, t.y), pack2(t.z, t.w)}, rkm, (u32)(SZ_KM + (h * 4 + gs) * 512 + l * 8), (u32)s * 65536, 16);
      return;
    }
    {
      const char* pa = smem + CH_PART + 1024 + l * 16;
      const char* pc = smem + CH_PART + (((m < 8) & (g < 2)) ? l * 16 : 4096);
      const char* pb = smem + CH_PART + 3072 + l * 16;
      const f32x4 a0 = *(const f32x4*)(pa), a1 = *(const f32x4*)(pa + PSTR), a2 = *(const f32x4*)(pa + 2 * PSTR), a3 = *(const f32x4*)(pa + 3 * PSTR);
      const f32x4 c0 = *(const f32x4*)(pc), c1 = *(const f32x4*)(pc + PSTR), c2 = *(const f32x4*)(pc + 2 * PSTR), c3 = *(const f32x4*)(pc + 3 * PSTR);
      const f32x4 b0 = *(const f32x4*)(pb), b1 = *(const f32x4*)(pb + PSTR);
      __builtin_amdgcn_sched_barrier(0);
      const f32x4 ta = (a0 + a1) + (a2 + a3);
      const f32x4 b2 = *(const f32x4*)(pb + 2 * PSTR), b3 = *(const f32x4*)(pb + 3 * PSTR);
      __builtin_amdgcn_sched_barrier(0);
      const f32x4 tc = (c0 + c1) + (c2 + c3);
      ldwo(3); ldwo(4);
      __builtin_amdgcn_sched_barrier(0);
      CST(4);
      float s0 = ta.x, s1 = ta.y, s2 = ta.z, s3 = ta.w, s4 = tc.x, s5 = tc.y, s6 = tc.z, s7 = tc.w;
      float mxl, tu;
      f32x2 tl = b0.xy, th = b0.zw, ul = b2.xy, uh = b2.zw;
      asm volatile(
          "v_max3_f32 %[t], %[s0], %[s1], %[s2]\n\t"
          "v_max3_f32 %[u], %[s3], %[s4], %[s5]\n\t"
          "v_max3_f32 %[t], %[t], %[s6], %[s7]\n\t"
          "v_max_f32 %[t], %[t], %[u]\n\t"
          "v_pk_add_f32 %[tl], %[tl], %[b1l]\n\t"
          "v_pk_add_f32 %[th], %[th], %[b1h]\n\t"
          "v_max_f32_dpp %[t], %[t], %[t] row_ror:8 row_mask:0xf bank_mask:0xf\n\t"
          "v_mov_b32 %[u], %[t]\n\t"
          "v_pk_add_f32 %[ul], %[ul], %[b3l]\n\t"
          "v_pk_add_f32 %[uh], %[uh], %[b3h]\n\t"
          "v_permlane16_swap_b32 %[t], %[u]\n\t"
          "v_max_f32 %[t], %[t], %[u]\n\t"
          "v_mov_b32 %[u], %[t]\n\t"
          "v_pk_add_f32 %[tl], %[tl], %[ul]\n\t"
          "v_pk_add_f32 %[th], %[th], %[uh]\n\t"
          "v_permlane32_swap_b32 %[t], %[u]\n\t"
          "v_max_f32 %[t], %[t], %[u]\n\t"
          "v_mul_f32 %[t], 0xbe0293ee, %[t]"
          : [t] "=&v"(mxl), [u] "=&v"(tu), [tl] "+v"(tl), [th] "+v"(th), [ul] "+v"(ul), [uh] "+v"(uh)
          : [s0] "v"(s0), [s1] "v"(s1), [s2] "v"(s2), [s3] "v"(s3), [s4] "v"(s4), [s5] "v"(s5), [s6] "v"(s6), [s7] "v"(s7),
            [b1l] "v"(b1.xy), [b1h] "v"(b1.zw), [b3l] "v"(b3.xy), [b3h] "v"(b3.zw));
      CST(5);
      const bool pub = true;
      const u32 ib = (u32)(s & 1) * INBOX_SLOT + (u32)c * 128 + (4 * (g & 1) + (m & 3)) * 16;
      const u32 ob = pub ? ib + (u32)(20 * w + 4 * (g >> 1) + (m >> 2)) * (NPROD * 128) : 0x80000000u;
      const u32 ob2 = pub ? ib + (u32)(20 * w + 16 + 2 * (g >> 1) + (m >> 3)) * (NPROD * 128) + ((m >> 2) & 1) * 8 : 0x80000000u;
      asm volatile(
          "v_fmamk_f32 %[s0], %[s0], 0x3e0293ee, %[mx]\n\t"
          "v_fmamk_f32 %[s1], %[s1], 0x3e0293ee, %[mx]\n\t"
          "v_exp_f32 %[s0], %[s0]\n\t"
          "v_fmamk_f32 %[s2], %[s2], 0x3e0293ee, %[mx]\n\t"
          "v_exp_f32 %[s1], %[s1]\n\t"
          "v_fmamk_f32 %[s3], %[s3], 0x3e0293ee, %[mx]\n\t"
          "v_exp_f32 %[s2], %[s2]\n\t"
          "v_exp_f32 %[s3], %[s3]\n\t"
          "v_fmamk_f32 %[s4], %[s4], 0x3e0293ee, %[mx]\n\t"
          "v_cvt_pk_bf16_f32 v172, %[s0], %[s1]\n\t"
          "v_cvt_pk_bf16_f32 v173, %[s2], %[s3]\n\t"
          "v_fmamk_f32 %[s5], %[s5], 0x3e0293ee, %[mx]\n\t"
          "v_fmamk_f32 %[s6], %[s6], 0x3e0293ee, %[mx]\n\t"
          "v_mov_b32_dpp v174, v172 row_ror:8 row_mask:0xf bank_mask:0xf\n\t"
          "v_mov_b32_dpp v175, v173 row_ror:8 row_mask:0xf bank_mask:0xf\n\t"
          "v_fmamk_f32 %[s7], %[s7], 0x3e0293ee, %[mx]\n\t"
          "v_exp_f32 %[s4], %[s4]\n\t"
          "v_mfma_f32_16x16x32_bf16 v[208:211], v[200:203], v[172:175], 0\n\t"
          "v_exp_f32 %[s5], %[s5]\n\t"
          "v_mfma_f32_16x16x32_bf16 v[212:215], v[204:207], v[172:175], 0\n\t"
          "v_exp_f32 %[s6], %[s6]\n\t"
          "v_exp_f32 %[s7], %[s7]\n\t"
          "v_cvt_pk_bf16_f32 v178, %[t0], %[t1]\n\t"
          "v_cvt_pk_bf16_f32 v179, %[t2], %[t3]\n\t"
          "v_cvt_pk_bf16_f32 v176, %[s4], %[s5]\n\t"
          "v_cvt_pk_bf16_f32 v177, %[s6], %[s7]\n\t"
          "v_add_f32 v182, %[s0], %[s1]\n\t"
          "v_permlane32_swap_b32 v178, v172\n\t"
          "v_permlane32_swap_b32 v179, v173\n\t"
          "v_add_f32 v183, %[s2], %[s3]\n\t"
          "v_add_f32 v182, v182, v183\n\t"
          "v_mfma_f32_16x16x16_bf16 v[208:211], v[178:179], v[176:177], v[208:211]\n\t"
          "v_add_f32 v183, %[s4], %[s5]\n\t"
          "v_add_f32 %[s6], %[s6], %[s7]\n\t"
          "v_mfma_f32_16x16x16_bf16 v[212:215], v[172:173], v[176:177], v[212:215]\n\t"
          "v_add_f32 v183, v183, %[s6]\n\t"
          "v_add_f32 v182, v182, v183\n\t"
          "v_mov_b64 v[184:185], 0\n\t"
          "v_mov_b64 v[186:187], 0\n\t"
          "v_add_f32_dpp v182, v182, v182 row_ror:8 row_mask:0xf bank_mask:0xf\n\t"
          "v_mov_b32 v183, v182\n\t"
          "v_mov_b64 v[164:165], 0\n\t"
          "v_mov_b64 v[166:167], 0\n\t"
          "v_permlane16_swap_b32 v182, v183\n\t"
          "v_add_f32 v182, v182, v183\n\t"
          "v_mov_b32 v183, v182\n\t"
          "v_mov_b64 v[220:221], 0\n\t"
          "v_mov_b64 v[222:223], 0\n\t"
          "v_permlane32_swap_b32 v182, v183\n\t"
          "v_add_f32 v182, v182, v183\n\t"
          "v_rcp_f32 v182, v182\n\t"
          "v_mov_b64 v[192:193], 0\n\t"
          "v_mul_f32 v208, v208, v182\n\t"
          "v_mul_f32 v209, v209, v182\n\t"
          "v_mul_f32 v210, v210, v182\n\t"
          "v_mul_f32 v211, v211, v182\n\t"
          "v_cvt_pk_bf16_f32 v216, v208, v209\n\t"
          "v_cvt_pk_bf16_f32 v217, v210, v211\n\t"
          "v_mul_f32 v212, v212, v182\n\t"
          "v_mul_f32 v213, v213, v182\n\t"
          "v_mul_f32 v214, v214, v182\n\t"
          "v_mul_f32 v215, v215, v182\n\t"
          "v_cvt_pk_bf16_f32 v218, v212, v213\n\t"
          "v_cvt_pk_bf16_f32 v219, v214, v215\n\t"
          "v_mov_b32_dpp v216, v216 row_ror:8 row_mask:0xf bank_mask:0xc\n\t"
          "v_mov_b32_dpp v217, v217 row_ror:8 row_mask:0xf bank_mask:0xc\n\t"
          "v_mov_b32_dpp v218, v218 row_ror:8 row_mask:0xf bank_mask:0xc\n\t"
          "v_mov_b32_dpp v219, v219 row_ror:8 row_mask:0xf bank_mask:0xc\n\t"
          "v_mov_b64 v[194:195], 0\n\t"
          "v_mov_b64 v[196:197], 0\n\t"
          "v_smfmac_f32_16x16x64_bf16 v[184:187], v[216:219], %[w0], %[ix]\n\t"
          "v_mov_b64 v[198:199], 0\n\t"
          "v_smfmac_f32_16x16x64_bf16 v[164:167], v[216:219], %[w1], %[ix]\n\t"
          "v_smfmac_f32_16x16x64_bf16 v[220:223], v[216:219], %[w2], %[ix]\n\t"
          "v_smfmac_f32_16x16x64_bf16 v[192:195], v[216:219], %[w3], %[ix]\n\t"
          "v_smfmac_f32_16x16x64_bf16 v[196:199], v[216:219], %[w4], %[ix]\n\t"
          "v_cvt_pk_bf16_f32 v208, v184, v185\n\t"
          "v_cvt_pk_bf16_f32 v209, v186, v187\n\t"
          "v_cvt_pk_bf16_f32 v210, v164, v165\n\t"
          "v_cvt_pk_bf16_f32 v211, v166, v167\n\t"
          "buffer_store_dwordx4 v[208:211], %[ob], %[ri], 0 offen sc1\n\t"
          "v_cvt_pk_bf16_f32 v212, v220, v221\n\t"
          "v_cvt_pk_bf16_f32 v213, v222, v223\n\t"
          "v_cvt_pk_bf16_f32 v214, v192, v193\n\t"
          "v_cvt_pk_bf16_f32 v215, v194, v195\n\t"
          "buffer_store_dwordx4 v[212:215], %[ob], %[ri], %[so] offen sc1\n\t"
          "v_cvt_pk_bf16_f32 v216, v196, v197\n\t"
          "v_cvt_pk_bf16_f32 v217, v198, v199\n\t"
          "buffer_store_dwordx2 v[216:217], %[ob2], %[ri], 0 offen sc1\n\t"
          "s_nop 1"
          : [s0] "+v"(s0), [s1] "+v"(s1), [s2] "+v"(s2), [s3] "+v"(s3), [s4] "+v"(s4), [s5] "+v"(s5), [s6] "+v"(s6), [s7] "+v"(s7), "=&{v[178:179]}"(vm0), "=&{v[172:173]}"(vm1)
          : [mx] "v"(mxl), [t0] "v"(tl.x), [t1] "v"(tl.y), [t2] "v"(th.x), [t3] "v"(th.y), "{v[200:203]}"(kv4[2]), "{v[204:207]}"(kv4[3]),
            [w0] "v"(wo[0]), [w1] "v"(wo[1]), [w2] "v"(wo[2]), [w3] "v"(wo[3]), [w4] "v"(wo[4]), [ix] "v"(sidx),
            [ob] "v"(ob), [ob2] "v"(ob2), [ri] "s"(din), [so] "s"(8 * NPROD * 128)
          : "v192", "v193", "v194", "v195", "v196", "v197", "v198", "v199", "v174", "v175", "v176", "v177", "v182", "v183",
            "v184", "v185", "v186", "v187", "v164", "v165", "v166", "v167", "v208", "v209", "v210", "v211", "v212", "v213", "v214", "v215",
            "v216", "v217", "v218", "v219", "v220", "v221", "v222", "v223", "memory");
    }
    const int slot = s & 1;
    CST(6);
    STAMP(4);
    if (w == gs && m < 8) __builtin_amdgcn_raw_buffer_store_b128(kp, rkm, (u32)((h * 4 + gs) * 512 + g * 128 + m * 16), (u32)s * 65536, 16);
    if (w == ((gs + 1) & 3) && g < 2) __builtin_amdgcn_raw_buffer_store_b64(vm0, rkm, (u32)(SZ_KM + (h * 4 + gs) * 512 + l * 8), (u32)s * 65536, 16);
    if (w == ((gs + 2) & 3) && g < 2) __builtin_amdgcn_raw_buffer_store_b64(vm1, rkm, (u32)(SZ_KM + (h * 4 + gs) * 512 + 256 + l * 8), (u32)s * 65536, 16);
    const int doit = c - (int)NRED;
    {
      const u32 base = slot * INBOX_SLOT + (c < (int)NRED ? c : 0) * NPROD * 128 + w * 4096 + l * 16;
      int mm = m;
      asm volatile("" : "+v"(mm));
      const u32 one2 = (mm & 1) ? 0x3f800000u : 0x3f80u;
      const u32x4 sel = {(mm >> 1) == 0 ? one2 : 0u, (mm >> 1) == 1 ? one2 : 0u, (mm >> 1) == 2 ? one2 : 0u, (mm >> 1) == 3 ? one2 : 0u};
      f32x4 rd;
      u32 tmx, cnt;
      unsigned long long m0, m1, m2, m3, mo;
      asm volatile(
          "s_mov_b32 %[cnt], 0\n\t"
          "s_cmp_ge_i32 %[doit], 0\n\t"
          "s_cbranch_scc1 4f\n\t"
          "s_sleep %[slp0]\n\t"
          "buffer_load_dwordx4 v[224:227], %[o], %[rs], 0 offen offset:0 " PI_AUX "\n\t"
          "buffer_load_dwordx4 v[228:231], %[o], %[rs], 0 offen offset:1024 " PI_AUX "\n\t"
          "buffer_load_dwordx4 v[232:235], %[o], %[rs], 0 offen offset:2048 " PI_AUX "\n\t"
          "buffer_load_dwordx4 v[236:239], %[o], %[rs], 0 offen offset:3072 " PI_AUX "\n\t"
          "s_mov_b64 %[m0], -1\n\t"
          "s_mov_b64 %[m1], -1\n\t"
          "s_mov_b64 %[m2], -1\n\t"
          "s_mov_b64 %[m3], -1\n\t"
          "s_sleep %[slp]\n\t"
          I_EXTRA
          "\n1:\n\t"
          "s_mov_b64 exec, %[m0]\n\t"
          "buffer_load_dwordx4 v[224:227], %[o], %[rs], 0 offen offset:0 " PI_AUX "\n\t"
          "s_mov_b64 exec, %[m1]\n\t"
          "buffer_load_dwordx4 v[228:231], %[o], %[rs], 0 offen offset:1024 " PI_AUX "\n\t"
          "s_mov_b64 exec, %[m2]\n\t"
          "buffer_load_dwordx4 v[232:235], %[o], %[rs], 0 offen offset:2048 " PI_AUX "\n\t"
          "s_mov_b64 exec, %[m3]\n\t"
          "buffer_load_dwordx4 v[236:239], %[o], %[rs], 0 offen offset:3072 " PI_AUX "\n\t"
          "s_mov_b64 exec, -1\n\t"
          "s_waitcnt vmcnt(%[nw])\n\t"
          "v_max3_u32 %[t], v224, v225, v226\n\t"
          "v_max_u32 %[t], %[t], v227\n\t"
          "v_cmp_eq_u32 %[m0], -1, %[t]\n\t"
          "v_max3_u32 %[t], v228, v229, v230\n\t"
          "v_max_u32 %[t], %[t], v231\n\t"
          "v_cmp_eq_u32 %[m1], -1, %[t]\n\t"
          "v_max3_u32 %[t], v232, v233, v234\n\t"
          "v_max_u32 %[t], %[t], v235\n\t"
          "v_cmp_eq_u32 %[m2], -1, %[t]\n\t"
          "v_max3_u32 %[t], v236, v237, v238\n\t"
          "v_max_u32 %[t], %[t], v239\n\t"
          "v_cmp_eq_u32 %[m3], -1, %[t]\n\t"
          "s_or_b64 %[mo], %[m0], %[m1]\n\t"
          "s_or_b64 vcc, %[m2], %[m3]\n\t"
          "s_or_b64 %[mo], %[mo], vcc\n\t"
          "s_cbranch_scc0 2f\n\t"
          "s_add_u32 %[cnt], %[cnt], 1\n\t"
          "s_cmp_lt_u32 %[cnt], 0x20000\n\t"
          "s_cbranch_scc1 1b\n"
          "2:\n\t"
          "v_mfma_f32_16x16x32_bf16 %[d], v[224:227], %[sel], 0\n\t"
          "v_mfma_f32_16x16x32_bf16 %[d], v[228:231], %[sel], %[d]\n\t"
          "v_mfma_f32_16x16x32_bf16 %[d], v[232:235], %[sel], %[d]\n\t"
          "v_mfma_f32_16x16x32_bf16 %[d], v[236:239], %[sel], %[d]\n\t"
          "4:"
          : [d] "=&v"(rd), "+{v[224:227]}"(qt[0]), "+{v[228:231]}"(qt[1]), "+{v[232:235]}"(qt[2]), "+{v[236:239]}"(qt[3]), [t] "=&v"(tmx), [cnt] "=&s"(cnt),
            [m0] "=&s"(m0), [m1] "=&s"(m1), [m2] "=&s"(m2), [m3] "=&s"(m3), [mo] "=&s"(mo)
          : [o] "v"(base), [rs] "s"(din), [slp] "i"(SLP_D1), [slp0] "i"(SLP_RI), [doit] "s"(doit), [sel] "v"(sel), [nw] "i"(4 * (NR_I - 1))
          : "vcc", "scc", "memory");
      if (!LATE) load_kv(s + 1);
      if (c >= (int)NRED) __builtin_amdgcn_s_sleep(SLP_NR);
      if (doit < 0) {
      if (cnt >= 0x20000u && l == 0) *err = 2;
      STAMP(5);
      asm volatile("s_nop 4" : "+v"(rd));
      {
        const f32x2 r2 = {hsum32(rd.x, rd.z), hsum32(rd.y, rd.w)};
        if (m < 8) *(f32x2*)(smem + CH_SRED + (w * 8 + 4 * (g & 1) + (m & 3)) * 32 + (m >> 2) * 16 + (g >> 1) * 8) = r2;
      }
      __syncthreads();
      if (w == 0) {
        const int l8 = l & 7;
        const f32x4* sr = (const f32x4*)(smem + CH_SRED);
        f32x4 a0 = (sr[2 * l8] + sr[16 + 2 * l8]) + (sr[32 + 2 * l8] + sr[48 + 2 * l8]);
        f32x4 a1 = (sr[2 * l8 + 1] + sr[16 + 2 * l8 + 1]) + (sr[32 + 2 * l8 + 1] + sr[48 + 2 * l8 + 1]);
        u32x4 o = {pack2(a0.x, a0.y), pack2(a0.z, a0.w), pack2(a1.x, a1.y), pack2(a1.z, a1.w)};
#pragma unroll
        for (u32 rp = 0; rp < MREP; rp += 8)
          __builtin_amdgcn_raw_buffer_store_b128(o, rmem, (l >> 3) * MEM_REP + c * 128 + l8 * 16, ((s + 1) & 3) * MEM_STEP + rp * MEM_REP, 16);
        u32x4 sv = {SENT, SENT, SENT, SENT};
        asm volatile("" : "+v"(sv));
#pragma unroll
        for (u32 rp = 0; rp < MREP; rp += 8)
          __builtin_amdgcn_raw_buffer_store_b128(sv, rmem, (l >> 3) * MEM_REP + c * 128 + l8 * 16, (s & 3) * MEM_STEP + rp * MEM_REP, 16);
      }
      STAMP(6);
      if (!LATE) __builtin_amdgcn_s_sleep(SLP_RM);
      }
      if (LATE) {
        wait_flag2<1>(ws, OFF_KVC + ((s + 1) * 32 + h) * 64);
        load_kv(s + 1);
      }
    }
  };
  step(FS, std::false_type{}, std::true_type{});
  for (int s = FS + 1; s < NSEG - 1; ++s) step(s, std::false_type{}, std::false_type{});
  step(NSEG - 1, std::true_type{}, std::false_type{});
  asm volatile("s_waitcnt vmcnt(0)" ::"{v[224:227]}"(qt[0]), "{v[228:231]}"(qt[1]), "{v[232:235]}"(qt[2]), "{v[236:239]}"(qt[3]), "{v[240:243]}"(mfo[0]), "{v[244:247]}"(mfo[1]), "{v[248:251]}"(mfo[2]), "{v252}"(qf) : "memory");
  __syncthreads();
  __builtin_amdgcn_raw_buffer_store_b32(1u, rmc, mco, (u32)(NSEG - 1) * (32 * 64), 16);
}

#define WSTAMP(i)
#define WSTAMPJ(i)
#define LST(cond, i)
#define QST(i)
__device__ __forceinline__ int otid() { int t = threadIdx.x; asm volatile("" : "+v"(t)); return t; }

__device__ __forceinline__ void block_top8(const float* __restrict__ r, int* sidx, float* sval, float* swv, int* swi) {
  const int tid = otid(), w = tid >> 6, l = tid & 63;
  float v[8];
#pragma unroll
  for (int j = 0; j < 8; ++j) v[j] = r[tid + 256 * j];
  for (int round = 0; round < CSEL; ++round) {
    float bv = v[0]; int bi = tid;
#pragma unroll
    for (int j = 1; j < 8; ++j) if (v[j] > bv) { bv = v[j]; bi = tid + 256 * j; }
#pragma unroll
    for (int o = 32; o > 0; o >>= 1) {
      float ov = __shfl_xor(bv, o); int oi = __shfl_xor(bi, o);
      if (ov > bv || (ov == bv && oi < bi)) { bv = ov; bi = oi; }
    }
    if (l == 0) { swv[w] = bv; swi[w] = bi; }
    __syncthreads();
    float fv = swv[0]; int fi = swi[0];
#pragma unroll
    for (int k = 1; k < 4; ++k) { float ov = swv[k]; int oi = swi[k]; if (ov > fv || (ov == fv && oi < fi)) { fv = ov; fi = oi; } }
    if (tid == 0) { sidx[round] = fi; sval[round] = fv; }
#pragma unroll
    for (int j = 0; j < 8; ++j) if (tid + 256 * j == fi) v[j] = -INFINITY;
    __syncthreads();
  }
}

__device__ __forceinline__ void worker_modA(const u16* __restrict__ wg, const u16* __restrict__ wu, char* __restrict__ ws, char* smem, const int blk0, const int bstep) {
  float* xs = (float*)smem;
  float* red = (float*)(smem + 20480);
  int* sidx = (int*)(smem + 24576);
  float* sval = (float*)(smem + 24576 + 64);
  float* swv = (float*)(smem + 24576 + 128);
  int* swi = (int*)(smem + 24576 + 192);
  const int tid = otid(), w = tid >> 6, l = tid & 63;
  block_top8((const float*)(ws + OFF_R), sidx, sval, swv, swi);
  const u16* xnf = (const u16*)(ws + OFF_XNF);
  for (int i = tid; i < 8 * 640; i += 256) {
    const int rr = i / 640, k = i % 640, tok = sidx[rr], tt = tok & 31;
    const int mt = 2 * (tok >> 5) + ((tt >> 2) & 1), r = 4 * (tt >> 3) + (tt & 3);
    xs[i] = bf2f(xnf[((mt * 20 + (k >> 5)) * 64 + ((k >> 3) & 3) * 16 + r) * 8 + (k & 7)]);
  }
  const __amdgpu_buffer_rsrc_t rh = __builtin_amdgcn_make_buffer_rsrc(ws + OFF_HB, 0, (u32)(OFF_ERR - OFF_HB), 0x00020000);
  if (blk0 == 0 && tid < 8) {
    __builtin_amdgcn_raw_buffer_store_b32((u32)sidx[tid], rh, (u32)(OFF_SEL - OFF_HB) + tid * 4, 0, 16);
    __builtin_amdgcn_raw_buffer_store_b32(__float_as_uint(bfr(1.0f / (1.0f + __expf(-sval[tid])))), rh, (u32)(OFF_SEL - OFF_HB) + 32 + tid * 4, 0, 16);
  }
  __syncthreads();
  const int cp = tid & 7, kg = tid >> 3;
  for (int blk = blk0; blk < 108; blk += bstep) {
  const int n0 = blk * 16 + 2 * cp;
  float ag[8][2], au[8][2];
#pragma unroll
  for (int rr = 0; rr < 8; ++rr) { ag[rr][0] = ag[rr][1] = au[rr][0] = au[rr][1] = 0.f; }
  u32 g2[20], u2[20];
#pragma unroll
  for (int i = 0; i < 20; ++i) {
    g2[i] = *(const u32*)(wg + (size_t)(20 * kg + i) * FF + n0);
    u2[i] = *(const u32*)(wu + (size_t)(20 * kg + i) * FF + n0);
  }
#pragma unroll
  for (int i = 0; i < 20; ++i) {
    const float g0 = bflo(g2[i]), g1 = bfhi(g2[i]), u0 = bflo(u2[i]), u1 = bfhi(u2[i]);
#pragma unroll
    for (int rr = 0; rr < 8; ++rr) {
      const float xv = xs[rr * 640 + 20 * kg + i];
      ag[rr][0] += xv * g0; ag[rr][1] += xv * g1; au[rr][0] += xv * u0; au[rr][1] += xv * u1;
    }
  }
#pragma unroll
  for (int rr = 0; rr < 8; ++rr) {
#pragma unroll
    for (int e = 0; e < 2; ++e) {
      ag[rr][e] = xr32_add(xr16_add(xr8_add(ag[rr][e])));
      au[rr][e] = xr32_add(xr16_add(xr8_add(au[rr][e])));
    }
  }
  if (l < 8) {
#pragma unroll
    for (int rr = 0; rr < 8; ++rr) *(f32x4*)(red + (w * 8 + l) * 32 + rr * 4) = (f32x4){ag[rr][0], ag[rr][1], au[rr][0], au[rr][1]};
  }
  __syncthreads();
  if (tid < 64) {
    const int rr = tid >> 3, c2 = tid & 7;
    f32x4 a0 = *(const f32x4*)(red + (0 * 8 + c2) * 32 + rr * 4), a1 = *(const f32x4*)(red + (1 * 8 + c2) * 32 + rr * 4);
    f32x4 a2 = *(const f32x4*)(red + (2 * 8 + c2) * 32 + rr * 4), a3 = *(const f32x4*)(red + (3 * 8 + c2) * 32 + rr * 4);
    f32x4 t = (a0 + a1) + (a2 + a3);
    float hv[2];
#pragma unroll
    for (int e = 0; e < 2; ++e) {
      const float gsum = bfr(t[e]), usum = bfr(t[2 + e]);
      const float sg = bfr(gsum / (1.0f + __expf(-gsum)));
      hv[e] = sg * usum;
    }
    __builtin_amdgcn_raw_buffer_store_b32(pack2(hv[0], hv[1]), rh, (u32)((rr * FF + blk * 16 + 2 * c2) * 2), 0, 16);
  }
  asm volatile("s_waitcnt vmcnt(0)" ::: "memory");
  __syncthreads();
  if (tid == 0) set_flag(ws, OFF_HCNT + blk * 4);
  }
}

__device__ __forceinline__ void worker_modB(const u16* __restrict__ wd, char* __restrict__ ws, char* smem, const int blk) {
  float* hs = (float*)smem;
  float* red = (float*)(smem + 4096);
  const int tid = otid(), w = tid >> 6, l = tid & 63;
  const int cb = blk % 5, kb = blk / 5;
  const int n0 = cb * 128 + 2 * l;
  u32 d2[27];
#pragma unroll
  for (int i = 0; i < 27; ++i) d2[i] = *(const u32*)(wd + (size_t)(kb * 108 + 27 * w + i) * DM + n0);
  wait_flags<HC_SLP>(ws, OFF_HCNT, 54, (u32)OFF_FLAG_END);
  const __amdgpu_buffer_rsrc_t rh = __builtin_amdgcn_make_buffer_rsrc(ws + OFF_HB, 0, (u32)(OFF_ERR - OFF_HB), 0x00020000);
  for (int i = tid; i < 8 * 54; i += 256) {
    const int rr = i / 54, k2 = i % 54;
    const u32 v = __builtin_amdgcn_raw_buffer_load_b32(rh, (u32)((rr * FF + kb * 108 + 2 * k2) * 2), 0, 16);
    hs[rr * 108 + 2 * k2] = bflo(v); hs[rr * 108 + 2 * k2 + 1] = bfhi(v);
  }
  __syncthreads();
  float a[8][2];
#pragma unroll
  for (int rr = 0; rr < 8; ++rr) a[rr][0] = a[rr][1] = 0.f;
#pragma unroll
  for (int i = 0; i < 27; ++i) {
    const float d0 = bflo(d2[i]), d1 = bfhi(d2[i]);
#pragma unroll
    for (int rr = 0; rr < 8; ++rr) { const float hv = hs[rr * 108 + 27 * w + i]; a[rr][0] += hv * d0; a[rr][1] += hv * d1; }
  }
#pragma unroll
  for (int rr = 0; rr < 8; ++rr) { red[(w * 8 + rr) * 128 + 2 * l] = a[rr][0]; red[(w * 8 + rr) * 128 + 2 * l + 1] = a[rr][1]; }
  __syncthreads();
  for (int i = tid; i < 8 * 128; i += 256) {
    const int rr = i >> 7, cc = i & 127;
    const float v = (red[rr * 128 + cc] + red[(8 + rr) * 128 + cc]) + (red[(16 + rr) * 128 + cc] + red[(24 + rr) * 128 + cc]);
    __builtin_amdgcn_raw_buffer_store_b32(__float_as_uint(v), rh, (u32)(OFF_YP - OFF_HB) + (u32)(((kb * 8 + rr) * DM + cb * 128 + cc) * 4), 0, 16);
  }
  asm volatile("s_waitcnt vmcnt(0)" ::: "memory");
  __syncthreads();
  if (tid == 0) set_flag(ws, OFF_YCNT + blk * 4);
}

#define MFMA_BA(acc, ma, wb) asm volatile("v_mfma_f32_16x16x32_bf16 %0, %1, %2, %0" : "+v"(acc) : "v"(ma), "a"(wb))
#define MFMA_BA0(acc, ma, wb) asm volatile("v_mfma_f32_16x16x32_bf16 %0, %1, %2, 0" : "=&v"(acc) : "v"(ma), "a"(wb))
#define MFMA_BV(acc, ma, wb) asm volatile("v_mfma_f32_16x16x32_bf16 %0, %1, %2, %0" : "+v"(acc) : "v"(ma), "v"(wb))
__device__ __forceinline__ void worker_kv(const u16* __restrict__ Wk, const u16* __restrict__ Wv, char* __restrict__ ws, char* smem, const int h) {
  const int tid = otid(), w = __builtin_amdgcn_readfirstlane(tid >> 6), l = tid & 63;
  const int c = l & 15, g = l >> 4;
  const char* xsrc = ws + OFF_XNF + (size_t)(10 * w) * 1024 + l * 16;
  auto stage = [&](int i, int buf) {
    const int xo = buf == 0 ? 98304 : (buf - 1) * 40960;
#pragma unroll
    for (int k = 0; k < 10; ++k) glds16(xsrc + (size_t)i * 40960 + k * 1024, smem + xo + (10 * w + k) * 1024 + l * 16);
  };
  stage(NSK, 0);
  u32x4 wf[4][20];
  {
    const __amdgpu_buffer_rsrc_t rsw = __builtin_amdgcn_make_buffer_rsrc((void*)((w >> 1) ? Wv : Wk), 0, DM * HD * 2, 0x00020000);
    const u32 src = (u32)((32 * (w & 1) + g) * 8192 + h * 256 + c * 16);
    char* wr = smem + 8 * w * 1024 + g * 256 + ((c ^ (g << 2)) * 16);
    u32x4 rb[4][8];
    auto wld = [&](int ph) {
#pragma unroll
      for (int t = 0; t < 8; ++t) rb[ph & 3][t] = __builtin_amdgcn_raw_buffer_load_b128(rsw, src, (64 * ph + 4 * t) * 8192, 0);
    };
    const int q_ = c >> 2, p_ = c & 3;
    const char* rd = smem + (w >> 1) * 16384 + (8 * g + q_) * 256;
    const u32 ch0 = (u32)(((8 * (w & 1) + p_) ^ (q_ << 2)) * 16 + (g & 1) * 8);
    wld(0); wld(1); wld(2); wld(3);
#pragma unroll
    for (int ph = 0; ph < 10; ++ph) {
      __builtin_amdgcn_sched_barrier(0);
#pragma unroll
      for (int t = 0; t < 8; ++t) *(u32x4*)(wr + (ph & 1) * 32768 + t * 1024) = (t & 2) ? swp(rb[ph & 3][t]) : rb[ph & 3][t];
      __builtin_amdgcn_sched_barrier(0);
      if (ph + 4 < 10) wld(ph + 4);
      __builtin_amdgcn_sched_barrier(0);
      RAWBAR();
#pragma unroll
      for (int kk = 0; kk < 2; ++kk) {
        const int ks = 2 * ph + kk;
        const char* r = rd + (ph & 1) * 32768 + kk * 8192;
#pragma unroll
        for (int j = 0; j < 4; ++j) {
          const char* a = r + (ch0 ^ (u32)((j >> 1) * 64 + (j & 1) * 8));
          wf[j][ks] = j4(trd(a), trd(a + 1024));
        }
      }
#pragma unroll
      for (int kk = 0; kk < 2; ++kk) {
        const int ks = 2 * ph + kk;
        if (ks < 16) asm volatile("" : "+a"(wf[0][ks]), "+a"(wf[1][ks]), "+a"(wf[2][ks]), "+a"(wf[3][ks]));
        else asm volatile("" : "+v"(wf[0][ks]), "+v"(wf[1][ks]), "+v"(wf[2][ks]), "+v"(wf[3][ks]));
      }
      if (ph == 0) WSTAMPJ(0);
      if (ph == 4) WSTAMPJ(1);
    }
    RAWBAR();
    stage(NSK + 1, 1);
  }
  const __amdgpu_buffer_rsrc_t rkv = __builtin_amdgcn_make_buffer_rsrc(ws + OFF_K, 0, (u32)(2 * SZ_Q), 0x00020000);
  const u32 ko = (u32)((h * 4 + 2 * (w & 1)) * 2048 + l * 16), vo = (u32)SZ_Q + ko;
  const size_t kvc = OFF_KVC + h * 64;
  WSTAMPJ(3);
  int buf = 0;
  for (int sg = NSK; sg < NSEG; ++sg) {
    VMCNT(14);
    RAWBAR();
    if (sg == 2) WSTAMPJ(12);
    if (sg == 3) WSTAMPJ(13);
    if (sg >= 2 + KV_IMM && tid == 0) set_flag2(ws, kvc + (sg - 2) * (32 * 64));
    const int b2 = buf == 0 ? 2 : buf - 1;
    stage(min(sg + 2, NSEG - 1), b2);
    const char* base = smem + (buf == 0 ? 98304 : (buf - 1) * 40960) + l * 16;
    buf = buf == 2 ? 0 : buf + 1;
    f32x4 acc[2][4];
    u32x4 xa[3][2];
    auto ldx = [&](int ks) { xa[ks % 3][0] = *(const u32x4*)(base + ks * 1024); xa[ks % 3][1] = *(const u32x4*)(base + (20 + ks) * 1024); };
    ldx(0); ldx(1);
    auto mid = [&]() {
    };
    if (w < 2) {
#pragma unroll
      for (int ks = 0; ks < 20; ++ks) {
        if (ks + 2 < 20) ldx(ks + 2);
        if (ks == KV_MID) mid();
        __builtin_amdgcn_sched_barrier(0);
#pragma unroll
        for (int j = 0; j < 4; ++j)
#pragma unroll
          for (int t = 0; t < 2; ++t) {
            if (ks == 0) MFMA_A0(acc[t][j], wf[j][ks], xa[ks % 3][t]);
            else if (ks < 16) MFMA_A(acc[t][j], wf[j][ks], xa[ks % 3][t]);
            else MFMA_V(acc[t][j], wf[j][ks], xa[ks % 3][t]);
          }
        __builtin_amdgcn_sched_barrier(0);
      }
    } else {
#pragma unroll
      for (int ks = 0; ks < 20; ++ks) {
        if (ks + 2 < 20) ldx(ks + 2);
        if (ks == KV_MID) mid();
        __builtin_amdgcn_sched_barrier(0);
#pragma unroll
        for (int j = 0; j < 4; ++j)
#pragma unroll
          for (int t = 0; t < 2; ++t) {
            if (ks == 0) MFMA_BA0(acc[t][j], xa[ks % 3][t], wf[j][ks]);
            else if (ks < 16) MFMA_BA(acc[t][j], xa[ks % 3][t], wf[j][ks]);
            else MFMA_BV(acc[t][j], xa[ks % 3][t], wf[j][ks]);
          }
        __builtin_amdgcn_sched_barrier(0);
      }
    }
    asm volatile("s_nop 7\n\ts_nop 7" : "+v"(acc[0][0]), "+v"(acc[0][1]), "+v"(acc[0][2]), "+v"(acc[0][3]), "+v"(acc[1][0]), "+v"(acc[1][1]), "+v"(acc[1][2]), "+v"(acc[1][3]));
    if (w < 2) {
#pragma unroll
      for (int t = 0; t < 2; ++t)
#pragma unroll
        for (int u = 0; u < 2; ++u) {
          const f32x4 a0 = acc[t][2 * u], a1 = acc[t][2 * u + 1];
          const u32x4 v = {pack2(a0.x, a0.y), pack2(a0.z, a0.w), pack2(a1.x, a1.y), pack2(a1.z, a1.w)};
          __builtin_amdgcn_raw_buffer_store_b128(v, rkv, ko + (u32)(u * 2048 + t * 1024), (u32)sg * (32 * HD * 2), 16);
        }
    } else {
#pragma unroll
      for (int j = 0; j < 4; ++j) {
        const f32x4 a0 = acc[0][j], a1 = acc[1][j];
        const u32x4 v = {pack2(a0.x, a0.y), pack2(a0.z, a0.w), pack2(a1.x, a1.y), pack2(a1.z, a1.w)};
        __builtin_amdgcn_raw_buffer_store_b128(v, rkv, vo + (u32)(j * 1024), (u32)sg * (32 * HD * 2), 16);
      }
    }
    if (sg < KV_IMM) {
      VMCNT(0);
      RAWBAR();
      if (tid == 0) set_flag2(ws, kvc + sg * (32 * 64));
    }
  }
  VMCNT(0);
  RAWBAR();
  if (tid < 2) set_flag2(ws, kvc + (NSEG - 2 + tid) * (32 * 64));
}

template <int MAT>
__device__ __forceinline__ void kv_helper(const u16* __restrict__ W, char* __restrict__ ws, char* smem, const int h) {
  const int tid = otid(), w = __builtin_amdgcn_readfirstlane(tid >> 6), l = tid & 63;
  const int c = l & 15, g = l >> 4;
#pragma unroll
  for (int k = 0; k < 10 * NSH; ++k) glds16(ws + OFF_XNF + FS * 40960 + (size_t)(10 * NSH * w + k) * 1024 + l * 16, smem + 81920 + (10 * NSH * w + k) * 1024 + l * 16);
  const int r0 = 16 * (w >> 1) + 4 * (w & 1) + g;
  u32x4 rr[40];
#pragma unroll
  for (int t = 0; t < 40; ++t)
    rr[t] = __builtin_amdgcn_raw_buffer_load_b128(__builtin_amdgcn_make_buffer_rsrc((void*)W, 0, DM * HD * 2, 0x00020000), (u32)(r0 * 8192 + h * 256 + c * 16), (32 * (t >> 1) + 8 * (t & 1)) * 8192, 0);
  __builtin_amdgcn_sched_barrier(0);
  const int q_ = c >> 2, p_ = c & 3;
  char* wr = smem + r0 * 256 + ((c ^ (g << 2)) * 16);
  const char* rd = smem + (8 * g + q_) * 256 + (((4 * w + p_) ^ (q_ << 2)) * 16);
  f32x4 acc[2 * NSH][2];
#pragma unroll
  for (int t = 0; t < 2 * NSH; ++t) acc[t][0] = acc[t][1] = (f32x4){0, 0, 0, 0};
#pragma unroll
  for (int qt = 0; qt < 4; ++qt) {
#pragma unroll
    for (int t = 0; t < 10; ++t) *(u32x4*)(wr + (qt & 1) * 40960 + (t >> 1) * 8192 + (t & 1) * 2048) = (t & 1) ? swp(rr[10 * qt + t]) : rr[10 * qt + t];
    RAWBAR();
#pragma unroll
    for (int k = 0; k < 5; ++k) {
      bf16x8 wf[2];
#pragma unroll
      for (int T = 0; T < 2; ++T) {
        const char* a = rd + (qt & 1) * 40960 + k * 8192 + (((g & 1) ^ T) * 8);
        wf[T] = as_bf8(j4(trd(a), trd(a + 1024)));
      }
#pragma unroll
      for (int t = 0; t < 2 * NSH; ++t) {
        const bf16x8 xo = *(const bf16x8*)(smem + 81920 + t * 20480 + (5 * qt + k) * 1024 + l * 16);
#pragma unroll
        for (int T = 0; T < 2; ++T) acc[t][T] = MAT ? mfma32(xo, wf[T], acc[t][T]) : mfma32(wf[T], xo, acc[t][T]);
      }
    }
    __builtin_amdgcn_sched_barrier(0);
  }
  const __amdgpu_buffer_rsrc_t rkv = __builtin_amdgcn_make_buffer_rsrc(ws + OFF_K, 0, (u32)(2 * SZ_Q), 0x00020000);
#pragma unroll
  for (int sg = 0; sg < NSH; ++sg)
#pragma unroll
    for (int u = 0; u < 2; ++u) {
      const f32x4 a0 = MAT ? acc[2 * sg][u] : acc[2 * sg + u][0], a1 = MAT ? acc[2 * sg + 1][u] : acc[2 * sg + u][1];
      const u32x4 v = {pack2(a0.x, a0.y), pack2(a0.z, a0.w), pack2(a1.x, a1.y), pack2(a1.z, a1.w)};
      __builtin_amdgcn_raw_buffer_store_b128(v, rkv, (u32)(MAT * SZ_Q + (h * 4 + w) * 2048 + u * 1024 + l * 16), (u32)(FS + sg) * (32 * HD * 2), 16);
    }
  VMCNT(0);
  RAWBAR();
  if (tid < NSH) set_flag(ws, OFF_KVC + ((FS + tid) * 32 + h) * 64 + MAT * 4);
  WSTAMPJ(3);
}

__device__ __forceinline__ void worker_qattn(const u16* __restrict__ Wq, const u16* __restrict__ Wo, char* __restrict__ ws, char* smem, const int h) {
  const int tid = otid(), w = __builtin_amdgcn_readfirstlane(tid >> 6), l = tid & 63;
  const f32x4 z4 = {0, 0, 0, 0};
  const int c = l & 15, g = l >> 4;
  constexpr int LQ = 81920, LKV = 98304;
  __builtin_amdgcn_s_sleep(127); __builtin_amdgcn_s_sleep(127); __builtin_amdgcn_s_sleep(127);
  if (FS) {
    const float scale = 0.08838834764831845f;
    bf16x8 q[4], km[4], kx[8], vx[2];
    u32x2 vm[2];
#pragma unroll
    for (int ks = 0; ks < 4; ++ks) {
      q[ks] = *(const bf16x8*)(ws + OFF_QM0 + h * 2048 + ks * 512 + g * 128 + (c & 7) * 16);
      km[ks] = *(const bf16x8*)(ws + OFF_KM + h * 2048 + ks * 512 + g * 128 + (c & 7) * 16);
      kx[2 * ks] = *(const bf16x8*)(ws + OFF_K + h * 8192 + (2 * ks) * 1024 + l * 16);
      kx[2 * ks + 1] = *(const bf16x8*)(ws + OFF_K + h * 8192 + (2 * ks + 1) * 1024 + l * 16);
    }
#pragma unroll
    for (int E = 0; E < 2; ++E) {
      vx[E] = *(const bf16x8*)(ws + OFF_VT + h * 8192 + (2 * w + E) * 1024 + l * 16);
      vm[E] = *(const u32x2*)(ws + OFF_VMT + (h * 4 + w) * 512 + E * 256 + (l & 31) * 8);
      if (g >= 2) vm[E] = (u32x2){0, 0};
    }
    __builtin_amdgcn_sched_barrier(0);
    const int r0 = 16 * (w >> 1) + 4 * (w & 1) + g;
    u32x4 rr[2][4][5];
#pragma unroll
    for (int hf = 0; hf < 2; ++hf)
#pragma unroll
      for (int t = 0; t < 4; ++t)
#pragma unroll
        for (int wn = 0; wn < 5; ++wn)
          rr[hf][t][wn] = __builtin_amdgcn_raw_buffer_load_b128(__builtin_amdgcn_make_buffer_rsrc((void*)Wo, 0, DM * HD * 2, 0x00020000), (u32)((h * 128 + r0) * 1280 + c * 16),
                                                                (64 * hf + 32 * (t >> 1) + 8 * (t & 1)) * 1280 + wn * 256, 0);
    __builtin_amdgcn_sched_barrier(0);
    f32x4 sM = z4, sX[2] = {z4, z4};
#pragma unroll
    for (int ks = 0; ks < 4; ++ks) {
      sX[0] = mfma32(kx[2 * ks], q[ks], sX[0]);
      sX[1] = mfma32(kx[2 * ks + 1], q[ks], sX[1]);
      sM = mfma32(km[ks], q[ks], sM);
    }
    float sm_[4], sx[8];
#pragma unroll
    for (int i = 0; i < 4; ++i) {
      sm_[i] = g < 2 ? bfr(bfr(sM[i]) * scale) : -INFINITY;
      sx[i] = bfr(bfr(sX[0][i]) * scale);
      sx[4 + i] = bfr(bfr(sX[1][i]) * scale);
    }
    float mx = fmaxf(fmaxf(fmaxf(sx[0], sx[1]), fmaxf(sx[2], sx[3])), fmaxf(fmaxf(sx[4], sx[5]), fmaxf(sx[6], sx[7])));
    mx = fmaxf(mx, fmaxf(fmaxf(sm_[0], sm_[1]), fmaxf(sm_[2], sm_[3])));
    mx = xr32_max(xr16_max(mx));
    float sum = 0.f;
#pragma unroll
    for (int i = 0; i < 8; ++i) { sx[i] = __expf(sx[i] - mx); sum += sx[i]; }
#pragma unroll
    for (int i = 0; i < 4; ++i) { sm_[i] = __expf(sm_[i] - mx); sum += sm_[i]; }
    sum = xr32_add(xr16_add(sum));
    const float inv = 1.0f / sum;
    const u32x4 px = {pack2(sx[0] * inv, sx[1] * inv), pack2(sx[2] * inv, sx[3] * inv), pack2(sx[4] * inv, sx[5] * inv), pack2(sx[6] * inv, sx[7] * inv)};
    const u32x2 pm = {pack2(sm_[0] * inv, sm_[1] * inv), pack2(sm_[2] * inv, sm_[3] * inv)};
    f32x4 o[2];
#pragma unroll
    for (int E = 0; E < 2; ++E) {
      o[E] = mfma32(vx[E], as_bf8(px), z4);
      o[E] = mfma16(as_bf4(vm[E]), as_bf4(pm), o[E]);
    }
    const u32x4 v = {pack2(o[0].x, o[0].y), pack2(o[0].z, o[0].w), pack2(o[1].x, o[1].y), pack2(o[1].z, o[1].w)};
    *(u32x4*)(smem + 81920 + w * 1024 + l * 16) = v;
    const int q_ = c >> 2, p_ = c & 3;
    char* wr = smem + r0 * 1280 + ((c ^ (g << 2)) * 16);
    f32x4 acc[5][2];
#pragma unroll
    for (int i = 0; i < 5; ++i) acc[i][0] = acc[i][1] = z4;
#pragma unroll
    for (int hf = 0; hf < 2; ++hf) {
      if (hf) RAWBAR();
#pragma unroll
      for (int t = 0; t < 4; ++t)
#pragma unroll
        for (int wn = 0; wn < 5; ++wn) *(u32x4*)(wr + (32 * (t >> 1) + 8 * (t & 1)) * 1280 + wn * 256) = (t & 1) ? swp(rr[hf][t][wn]) : rr[hf][t][wn];
      RAWBAR();
#pragma unroll
      for (int k = 0; k < 2; ++k) {
        const bf16x8 of = *(const bf16x8*)(smem + 81920 + (2 * hf + k) * 1024 + l * 16);
#pragma unroll
        for (int i = 0; i < 5; ++i) {
          const int np = 5 * w + i;
          const char* a = smem + (32 * k + 8 * g + q_) * 1280 + (np >> 2) * 256 + (((4 * (np & 3) + p_) ^ (q_ << 2)) * 16);
#pragma unroll
          for (int T = 0; T < 2; ++T) {
            const char* aa = a + (((g & 1) ^ T) * 8);
            acc[i][T] = mfma32(as_bf8(j4(trd(aa), trd(aa + 4 * 1280))), of, acc[i][T]);
          }
        }
      }
    }
#pragma unroll
    for (int i = 0; i < 5; ++i) {
      const f32x4 e0 = acc[i][0], e1 = acc[i][1];
      const bool lo = c < 8;
      const u32x4 e = {__float_as_uint(lo ? e0.x : e1.x), __float_as_uint(lo ? e0.y : e1.y), __float_as_uint(lo ? e0.z : e1.z), __float_as_uint(lo ? e0.w : e1.w)};
      __builtin_amdgcn_raw_buffer_store_b128(e, __builtin_amdgcn_make_buffer_rsrc(ws + OFF_O0, 0, 20 * 32 * 1024, 0x00020000), (u32)((((5 * w + i) * 32 + h) * 64 + l) * 16), 0, 16);
    }
    VMCNT(0);
    RAWBAR();
    if (tid < 20) set_flag(ws, OFF_O0F + tid * 128 + h * 4);
    WSTAMPJ(0);
    __builtin_amdgcn_s_sleep(127); __builtin_amdgcn_s_sleep(QA_SLP);
    __builtin_amdgcn_sched_barrier(0);
  }
  const char* xsrc = ws + OFF_XNF + (size_t)(10 * w) * 1024 + l * 16;
  auto stage = [&](int sg, int buf) {
#pragma unroll
    for (int k = 0; k < 10; ++k) glds16(xsrc + (size_t)sg * 40960 + k * 1024, smem + buf * 40960 + (10 * w + k) * 1024 + l * 16);
  };
  const char* kvsrc = ws + OFF_K + (size_t)(w >> 1) * SZ_Q + (size_t)h * 8192 + (w & 1) * 4096 + l * 16;
  auto stage_kv = [&](int sg, int buf) {
#pragma unroll
    for (int k = 0; k < 4; ++k)
      __builtin_amdgcn_global_load_lds((const __attribute__((address_space(1))) void*)(kvsrc + (size_t)sg * (32 * 8192) + k * 1024),
                                       (__attribute__((address_space(3))) void*)(smem + LKV + buf * 16384 + w * 4096 + k * 1024 + l * 16), 16, 0, 16);
  };
  stage(0, 0);
  u32x4 wf[2][20];
  {
    const u32x4 S0 = sel_op(g == (c >> 2), c & 3), S1 = sel_op(g == (c >> 2), 4 + (c & 3));
    const u16* p = Wq + p32(0, c) * HD + h * 128 + 32 * w + 8 * g;
    u32x4 rb[20][2];
#pragma unroll
    for (int ks = 0; ks < 20; ++ks) { rb[ks][0] = *(const u32x4*)(p + ks * 32 * HD); rb[ks][1] = *(const u32x4*)(p + ks * 32 * HD + 4 * HD); }
#pragma unroll
    for (int ks = 0; ks < 20; ++ks) {
      __builtin_amdgcn_sched_barrier(0);
      tr_frag2(rb[ks][0], rb[ks][1], S0, S1, wf[0][ks], wf[1][ks]);
      asm volatile("" : "+a"(wf[0][ks]), "+a"(wf[1][ks]));
    }
    __builtin_amdgcn_sched_barrier(0);
  }
  const __amdgpu_buffer_rsrc_t rfl = __builtin_amdgcn_make_buffer_rsrc(ws + OFF_MCNT, 0, (u32)(OFF_FLAG_END - OFF_MCNT), 0x00020000);
  const __amdgpu_buffer_rsrc_t rmc = __builtin_amdgcn_make_buffer_rsrc(ws + OFF_MCNT, 0, 64 * 32 * 64, 0x00020000);
  const __amdgpu_buffer_rsrc_t rkm = __builtin_amdgcn_make_buffer_rsrc(ws + OFF_KM, 0, (u32)(2 * SZ_KM), 0x00020000);
  const __amdgpu_buffer_rsrc_t rof = __builtin_amdgcn_make_buffer_rsrc(ws + OFF_OF, 0, (u32)SZ_Q, 0x00020000);
  const float scale = 0.08838834764831845f;
  const int T = w & 1, uh = w >> 1;
  const int qi = p32(T, c);
  u32 tarv = 0, perv = 0, mlo = l < 8 ? (u32)(l * 32 + h) * 64 : 0x80000000u;
  asm volatile("" : "+v"(tarv), "+v"(perv), "+v"(mlo));
  int kvr = 0, mcr = 0;
  int* smc = (int*)(smem + LKV + 32768);
  const u32x4 dfl = mk_rsrc(ws + OFF_MCNT, (u32)(OFF_FLAG_END - OFF_MCNT));
  asm volatile("v_mov_b32 v250, 0" ::: "v250");
  auto go = [&](int sg) {
    u32 n;
    asm volatile("v_readfirstlane_b32 %0, v250\n\tv_mov_b32 v250, 0" : "=s"(n)::"v250");
    if (n == 31u) __builtin_amdgcn_raw_buffer_store_b32(1u, rfl, l < 40 ? (u32)(OFF_GO - OFF_MCNT) + l * 256 + sg * 4 : 0x80000000u, 0, 16);
  };
  char* lm = smem + LKV + 33024 + w * 3072;
  const char* msrc = ws + OFF_KM + (size_t)h * 2048 + l * 16;
#pragma unroll 1
  for (int s = -1; s < NSEG; ++s) {
    const bool dp = s >= TP0;
    char* lms = smem + LKV + 33024 + (s & 1) * 4096;
    const u32 dof = (u32)((l < 32 ? 0 : SZ_KM - 512) + h * 2048 + w * 512 + l * 16);
    auto ldm = [&]() {
      if (dp) {
        __builtin_amdgcn_global_load_lds((const __attribute__((address_space(1))) void*)(ws + OFF_KM + (size_t)s * 65536 + dof),
                                         (__attribute__((address_space(3))) void*)(lms + w * 1024 + l * 16), 16, 0, 16);
        return;
      }
#pragma unroll
      for (int k = 0; k < 3; ++k)
        __builtin_amdgcn_global_load_lds((const __attribute__((address_space(1))) void*)(msrc + (size_t)s * 65536 + (k < 2 ? k * 1024 : SZ_KM + uh * 1024)),
                                         (__attribute__((address_space(3))) void*)(lm + k * 1024 + l * 16), 16, 0, 16);
    };
    const bool early = dp ? __builtin_amdgcn_readfirstlane(tarv) == 0 : mcr > s && s >= 0;
    auto sig = [&]() {
      if (s > 0 && w == 0) {
        if (s > WO_FAST) __builtin_amdgcn_raw_buffer_store_b32(1u, rfl, l < 40 ? (u32)(OFF_OSEG - OFF_MCNT) + l * 1024 + h * 4 : 0x80000000u, (u32)(s - 1 - 56) * 128, 16);
        else asm volatile("v_mov_b32 v250, 1\n\ts_nop 0\n\tbuffer_atomic_add v250, %0, %1, 0 offen sc0 sc1" ::"v"(l == 0 ? (u32)(OFF_OCNT - OFF_MCNT) + (u32)(s - 1) * 64 : 0x80000000u), "s"(dfl) : "v250", "memory");
      }
    };
    QST(0);
    if (s == TP0 - 17 || s == TP0 - 1) {
      const u32 t = (u32)__builtin_amdgcn_s_memrealtime();
      perv = s == TP0 - 1 ? min(max((t - perv) >> 4, 250u), 400u) : t;
      asm volatile("" : "+v"(perv));
    }
    if (s < NSEG - 1) {
      if (s <= 0) VMCNT(0); else VMCNT(2);
      RAWBAR();
      if (w == 0) go(s - 2);
      if (early) ldm();
      __builtin_amdgcn_sched_barrier(0);
      if (kvr <= s + 1) {
        u32 spins = 0;
        for (;;) {
          asm volatile("" ::: "memory");
          const u32x2 v = __builtin_amdgcn_raw_buffer_load_b64(rfl, (u32)(OFF_KVC - OFF_MCNT) + (u32)(l * 32 + h) * 64, 0, 16);
          const unsigned long long nok = ~__builtin_amdgcn_ballot_w64((v.x & v.y) != 0);
          kvr = nok ? __builtin_ctzll(nok) : 64;
          if (kvr > s + 1) break;
          __builtin_amdgcn_s_sleep(16);
          if (++spins > (1u << 20)) { *(u32*)(ws + OFF_ERR) = 4; kvr = 64; break; }
        }
      }
      const char* xs_ = xsrc + (size_t)min(s + 2, NSEG - 1) * 40960;
      const char* ks_ = kvsrc + (size_t)(s + 1) * (32 * 8192);
      const char* base = smem + ((s + 1) & 1) * 40960 + l * 16;
      f32x4 acc[2][2];
      u32x4 xa[4][2];
      auto ldx = [&](int ks) { xa[ks & 3][0] = *(const u32x4*)(base + ks * 1024); xa[ks & 3][1] = *(const u32x4*)(base + (20 + ks) * 1024); };
      ldx(0); ldx(1); ldx(2);
#pragma unroll
      for (int ks = 0; ks < 20; ++ks) {
        if (ks + 3 < 20) ldx(ks + 3);
        __builtin_amdgcn_sched_barrier(0);
        if (ks < 4) __builtin_amdgcn_global_load_lds((const __attribute__((address_space(1))) void*)(ks_ + ks * 1024),
                                                     (__attribute__((address_space(3))) void*)(smem + LKV + ((s + 1) & 1) * 16384 + w * 4096 + ks * 1024 + l * 16), 16, 0, 16);
        else if (ks < 14) glds16(xs_ + (ks - 4) * 1024, smem + (s & 1) * 40960 + (10 * w + ks - 4) * 1024 + l * 16);
        __builtin_amdgcn_sched_barrier(0);
        constexpr int NY = SIG_KS + 1;
        if (ks == SIG_KS) {
          if (!early) VMCNT(NY); else if (dp) VMCNT(NY + 1); else VMCNT(NY + 3);
          __builtin_amdgcn_s_barrier();
          asm volatile("" ::: "memory");
          sig();
          __builtin_amdgcn_sched_barrier(0);
        }
#pragma unroll
        for (int t = 0; t < 2; ++t)
#pragma unroll
          for (int j = 0; j < 2; ++j) {
            if (ks == 0) MFMA_A0(acc[t][j], wf[j][ks], xa[ks & 3][t]);
            else MFMA_A(acc[t][j], wf[j][ks], xa[ks & 3][t]);
          }
        __builtin_amdgcn_sched_barrier(0);
      }
      asm volatile("s_nop 7\n\ts_nop 7" : "+v"(acc[0][0]), "+v"(acc[0][1]), "+v"(acc[1][0]), "+v"(acc[1][1]));
#pragma unroll
      for (int t = 0; t < 2; ++t) {
        const f32x4 a0 = acc[t][0], a1 = acc[t][1];
        const u32x4 v = {pack2(a0.x, a0.y), pack2(a0.z, a0.w), pack2(a1.x, a1.y), pack2(a1.z, a1.w)};
        *(u32x4*)(smem + LQ + ((s + 1) & 1) * 8192 + (t * 4 + w) * 1024 + l * 16) = v;
      }
      VMCNT(14);
    } else {
      if (early) ldm();
      if (!early) VMCNT(0); else if (dp) VMCNT(1); else VMCNT(3);
      RAWBAR();
      if (HINT0 == 63) sig();
      if (w == 0 && HINT0 == 63) VMCNT(1); else VMCNT(0);
    }
    if (s < 0) continue;
    QST(1);
    const char* lq = smem + LQ + (s & 1) * 8192 + T * 4096 + l * 16;
    const char* lk = smem + LKV + (s & 1) * 16384 + l * 16;
    f32x4 sM = z4, sX[2] = {z4, z4};
    bf16x8 q[4];
#pragma unroll
    for (int ks = 0; ks < 4; ++ks) {
      q[ks] = *(const bf16x8*)(lq + ks * 1024);
      sX[0] = mfma32(*(const bf16x8*)(lk + (2 * ks) * 1024), q[ks], sX[0]);
      sX[1] = mfma32(*(const bf16x8*)(lk + (2 * ks + 1) * 1024), q[ks], sX[1]);
    }
    if (dp) {
      u32x4 d = *(const u32x4*)(lms + w * 1024 + l * 16);
      if (!early || __builtin_amdgcn_ballot_w64(has_sent(d))) {
        u32 spins = 0;
        const u32 tarr = __builtin_amdgcn_readfirstlane(tarv);
        const u32 per = __builtin_amdgcn_readfirstlane(perv);
        if (!early) while ((u32)__builtin_amdgcn_s_memrealtime() - tarr < (s < NSEG - 1 ? per + QT_MARG : per - QT_LAST)) __builtin_amdgcn_s_sleep(1);
        for (;;) {
          asm volatile("" ::: "memory");
          d = __builtin_amdgcn_raw_buffer_load_b128(rkm, dof, (u32)s * 65536, 16);
          const bool bad = __builtin_amdgcn_ballot_w64(has_sent(d)) != 0;
          if (w == 0 && spins == 0) { VMCNT(0); go(s - 1); }
          if (!bad) break;
          if (s < NSEG - 1) __builtin_amdgcn_s_sleep(QT_SLP); else __builtin_amdgcn_s_sleep(QT_SLP2);
          if (++spins > (1u << 18)) { *(u32*)(ws + OFF_ERR) = 7; break; }
        }
        tarv = ((!early && !spins) ? tarr + per : (u32)__builtin_amdgcn_s_memrealtime() - QT_RT) | 1u;
        asm volatile("" : "+v"(tarv));
        *(u32x4*)(lms + w * 1024 + l * 16) = d;
      }
      RAWBAR();
    } else
    if (!early) {
      if (w == 0) {
        u32 spins = 0;
        for (;;) {
          asm volatile("" ::: "memory");
          const u32x4 v = __builtin_amdgcn_raw_buffer_load_b128(rmc, mlo + (u32)s * 2048, 0, 16);
          const unsigned long long nok = ~__builtin_amdgcn_ballot_w64((v.x != 0) & (v.y != 0) & (v.z != 0) & (v.w != 0));
          mcr = s + __builtin_ctzll(nok);
          if (mcr > s) break;
          __builtin_amdgcn_s_sleep(MC_SLP);
          if (++spins > (1u << 20)) { *(u32*)(ws + OFF_ERR) = 6; mcr = 64; break; }
        }
        if (l == 0) *smc = mcr;
        VMCNT(0);
        go(s - 1);
      }
      RAWBAR();
      ldm();
      mcr = __builtin_amdgcn_readfirstlane(*smc);
      VMCNT(0);
    }
    QST(2);
    const int mst = dp ? 1024 : 512;
    const char* lkm = dp ? lms : lm;
    const char* lvm = dp ? lms + uh * 2048 + 512 : lm + 2048;
#pragma unroll
    for (int ks = 0; ks < 4; ++ks) sM = mfma32(*(const bf16x8*)(lkm + ks * mst + g * 128 + (c & 7) * 16), q[ks], sM);
    float sm_[4], sx[8];
#pragma unroll
    for (int i = 0; i < 4; ++i) {
      sm_[i] = g < 2 ? bfr(bfr(sM[i]) * scale) : -INFINITY;
      float a = bfr(bfr(sX[0][i]) * scale), b = bfr(bfr(sX[1][i]) * scale);
      sx[i] = (8 * g + i <= qi) ? a : -1e9f;
      sx[4 + i] = (8 * g + 4 + i <= qi) ? b : -1e9f;
    }
    float mx = fmaxf(fmaxf(fmaxf(sx[0], sx[1]), fmaxf(sx[2], sx[3])), fmaxf(fmaxf(sx[4], sx[5]), fmaxf(sx[6], sx[7])));
    mx = fmaxf(mx, fmaxf(fmaxf(sm_[0], sm_[1]), fmaxf(sm_[2], sm_[3])));
    mx = xr32_max(xr16_max(mx));
    float sum = 0.f;
#pragma unroll
    for (int i = 0; i < 8; ++i) { sx[i] = __expf(sx[i] - mx); sum += sx[i]; }
#pragma unroll
    for (int i = 0; i < 4; ++i) { sm_[i] = __expf(sm_[i] - mx); sum += sm_[i]; }
    sum = xr32_add(xr16_add(sum));
    const float inv = 1.0f / sum;
    const u32x4 px = {pack2(sx[0] * inv, sx[1] * inv), pack2(sx[2] * inv, sx[3] * inv), pack2(sx[4] * inv, sx[5] * inv), pack2(sx[6] * inv, sx[7] * inv)};
    const u32x2 pm = {pack2(sm_[0] * inv, sm_[1] * inv), pack2(sm_[2] * inv, sm_[3] * inv)};
#pragma unroll
    for (int u = 0; u < 2; ++u) {
      f32x4 o[2];
#pragma unroll
      for (int E = 0; E < 2; ++E) {
        u32x2 vm = *(const u32x2*)(lvm + u * mst + E * 256 + (g & 1) * 128 + c * 8);
        if (g >= 2) vm = (u32x2){0, 0};
        o[E] = mfma32(*(const bf16x8*)(lk + 8192 + ((2 * uh + u) * 2 + E) * 1024), as_bf8(px), z4);
        o[E] = mfma16(as_bf4(vm), as_bf4(pm), o[E]);
      }
      const u32x4 v = {pack2(o[0].x, o[0].y), pack2(o[0].z, o[0].w), pack2(o[1].x, o[1].y), pack2(o[1].z, o[1].w)};
      __builtin_amdgcn_raw_buffer_store_b128(v, rof, (u32)((((2 * s + T) * 128 + (4 * h + 2 * uh + u)) * 64 + l) * 16), 0, 16);
    }
    if (!(s & 7)) WSTAMPJ(4 + (s >> 3));
    QST(3);
    if (HINT0 == 62 && s == 62) {
      RAWBAR();
      if (w == 0) __builtin_amdgcn_raw_buffer_store_b32(1u, rfl, l < 40 ? (u32)(OFF_OSEG - OFF_MCNT) + l * 1024 + 768 + h * 4 : 0x80000000u, 0, 16);
    }
  }
  RAWBAR();
  if (w == 0) __builtin_amdgcn_raw_buffer_store_b32(1u, rfl, l < 40 ? (u32)(OFF_OSEG - OFF_MCNT) + l * 1024 + 896 + h * 4 : 0x80000000u, 0, 16);
}

__device__ __forceinline__ void final_store2(const float* ys, u16* __restrict__ out, const int tok, const int n0, const f32x4 a0, const f32x4 a1,
                                             const u32x4 xv, const u32x4 nv, const int slot) {
  float a[8] = {a0.x, a0.y, a0.z, a0.w, a1.x, a1.y, a1.z, a1.w};
  float xf[8] = {bflo(xv.x), bfhi(xv.x), bflo(xv.y), bfhi(xv.y), bflo(xv.z), bfhi(xv.z), bflo(xv.w), bfhi(xv.w)};
  float nf[8] = {bflo(nv.x), bfhi(nv.x), bflo(nv.y), bfhi(nv.y), bflo(nv.z), bfhi(nv.z), bflo(nv.w), bfhi(nv.w)};
  if (slot >= 0) {
#pragma unroll
    for (int j = 0; j < 8; ++j) nf[j] = bfr(nf[j] + ys[slot * 32 + j]);
  }
  float o[8];
#pragma unroll
  for (int j = 0; j < 8; ++j) o[j] = bfr(xf[j] + bfr(a[j])) + nf[j];
  u32x4 ov = {pack2(o[0], o[1]), pack2(o[2], o[3]), pack2(o[4], o[5]), pack2(o[6], o[7])};
  *(u32x4*)(out + (size_t)tok * DM + n0) = ov;
}

__device__ __forceinline__ void worker_wo_fine(const u16* __restrict__ x, const u16* __restrict__ Wo, char* __restrict__ ws, u16* __restrict__ out, char* smem, const int par, const int np,
                                               const int sg0, const int sgstep) {
  const int tid = otid(), w = __builtin_amdgcn_readfirstlane(tid >> 6), l = tid & 63;
  const int c = l & 15, g = l >> 4;
  u32x4 b0[32], b1[32];
  const int rot = (np * 8) / 5;
  {
    const u32x4 S0 = sel_op(g == (c >> 2), c & 3), S1 = sel_op(g == (c >> 2), 4 + (c & 3));
    const u16* p = Wo + (1024 * w + p32(0, c)) * DM + 32 * np + 8 * g;
    u32x4 rb[16][2];
    auto ldb = [&](int i) {
      const int kk = (i + rot) & 31;
      rb[i & 15][0] = *(const u32x4*)(p + kk * 32 * DM); rb[i & 15][1] = *(const u32x4*)(p + kk * 32 * DM + 4 * DM);
    };
#pragma unroll
    for (int i = 0; i < 16; ++i) ldb(i);
#pragma unroll
    for (int i = 0; i < 32; ++i) {
      __builtin_amdgcn_sched_barrier(0);
      tr_frag2(rb[i & 15][0], rb[i & 15][1], S0, S1, b0[i], b1[i]);
      asm volatile("" : "+a"(b0[i]), "+a"(b1[i]));
      __builtin_amdgcn_sched_barrier(0);
      if (i + 16 < 32) ldb(i + 16);
    }
    __builtin_amdgcn_sched_barrier(0);
  }
  const __amdgpu_buffer_rsrc_t rof = __builtin_amdgcn_make_buffer_rsrc(ws + OFF_OF, 0, (u32)SZ_Q, 0x00020000);
  const char* xnf = ws + OFF_XNF + np * 1024 + l * 16;
  const __amdgpu_buffer_rsrc_t rfl = __builtin_amdgcn_make_buffer_rsrc(ws + OFF_MCNT, 0, (u32)(OFF_FLAG_END - OFF_MCNT), 0x00020000);
  int sel[8];
  if (w == 0) {
    wait_flags<YC_SLP>(ws, OFF_YCNT, 40, (u32)OFF_FLAG_END);
    __builtin_amdgcn_fence(__ATOMIC_ACQUIRE, "agent");
#pragma unroll
    for (int k = 0; k < 8; ++k) sel[k] = __builtin_amdgcn_readfirstlane(((const int*)(ws + OFF_SEL))[k]);
    const float rs = ((const float*)(ws + OFF_SEL))[8 + (l >> 3)];
    const float* yp = (const float*)(ws + OFF_YP) + (l >> 3) * DM + 32 * np + 4 * (l & 7);
    f32x4 yv[16];
#pragma unroll
    for (int kb = 0; kb < 16; ++kb) yv[kb] = *(const f32x4*)(yp + (size_t)kb * 8 * DM);
    f32x4 y = {0.f, 0.f, 0.f, 0.f};
#pragma unroll
    for (int kb = 0; kb < 16; ++kb) y += yv[kb];
    *(f32x4*)(smem + 16384 + l * 16) = (f32x4){bfr(bfr(y.x) * rs), bfr(bfr(y.y) * rs), bfr(bfr(y.z) * rs), bfr(bfr(y.w) * rs)};
  }
  int ready = -1;
  u32 tarr = 0;
  const int n0 = 32 * np + 8 * g;
  f32x4 acc0, acc1;
  u32x4 xv, nv;
  auto fin = [&](int sg) {
    const f32x4* p = (const f32x4*)(smem + (sg & 1) * 8192 + l * 16);
    const int tok = 32 * sg + p32(par, c);
    const f32x4 s0 = (acc0 + p[2 * 64]) + (p[4 * 64] + p[6 * 64]);
    const f32x4 s1 = (acc1 + p[3 * 64]) + (p[5 * 64] + p[7 * 64]);
    int slot = -1;
#pragma unroll
    for (int k = 0; k < 8; ++k) if (sel[k] == tok) slot = k;
    final_store2((const float*)(smem + 16384) + 8 * g, out, tok, n0, s0, s1, xv, nv, slot);
  };
  auto ld_go = [&]() { return __builtin_amdgcn_raw_buffer_load_b32(rfl, (u32)(OFF_GO - OFF_MCNT) + (u32)(par * 20 + np) * 256 + l * 4, 0, 16); };
  auto upd_go = [&](u32 v) {
    const unsigned long long nok = ~(__builtin_amdgcn_ballot_w64(v != 0) >> (ready + 1));
    ready = min(ready + __builtin_ctzll(nok), WO_FAST - 1);
  };
  auto ld_os = [&]() { return __builtin_amdgcn_raw_buffer_load_b128(rfl, (u32)(OFF_OSEG - OFF_MCNT) + (u32)(par * 20 + np) * 1024 + l * 16, 0, 16); };
  auto upd_os = [&](u32x4 v) {
    const unsigned long long nok = ~(__builtin_amdgcn_ballot_w64((v.x != 0) & (v.y != 0) & (v.z != 0) & (v.w != 0)) >> (8 * (WO_FAST - 56)));
    ready = WO_FAST - 1 + (nok ? __builtin_ctzll(nok) >> 3 : 8);
  };
  for (int sg = 0; sg < 64; ++sg) {
    const int mt = 2 * sg + par;
    if (w == 1 && sg > ready) {
      u32 spins = 0;
      if (sg < 63) while ((u32)__builtin_amdgcn_s_memrealtime() - tarr < WO_WAIT) __builtin_amdgcn_s_sleep(2);
      for (;;) {
        asm volatile("" ::: "memory");
        if (sg < WO_FAST) upd_go(ld_go()); else upd_os(ld_os());
        if (sg <= ready) break;
        if (sg < WO_FAST) __builtin_amdgcn_s_sleep(WO_SLP); else __builtin_amdgcn_s_sleep(WO_SLP2);
        if (++spins > (1u << 20)) { *(u32*)(ws + OFF_ERR) = 5; break; }
      }
      if (spins) tarr = (u32)__builtin_amdgcn_s_memrealtime();
    }
    LST(sg >= 56 && w == 1, 2 * (sg - 56));
    RAWBAR();
    const u32 abase = (u32)(((mt * 128 + 32 * w) * 64 + l) * 16);
    u32x4 a[32];
#pragma unroll
    for (int i = 0; i < 32; ++i) a[i] = __builtin_amdgcn_raw_buffer_load_b128(rof, abase, ((i + rot) & 31) * 1024, 16);
    __builtin_amdgcn_sched_barrier(0);
    const bool pf = w == 1 && sg == ready && sg < 63;
    u32x4 fv = {0, 0, 0, 0};
    if (pf) { if (sg + 1 < WO_FAST) fv.x = ld_go(); else fv = ld_os(); }
    __builtin_amdgcn_sched_barrier(0);
    if (w == 0) {
      if (sg > 0) fin(sg - 1);
      xv = *(const u32x4*)(x + (size_t)(32 * sg + p32(par, c)) * DM + n0); nv = *(const u32x4*)(xnf + (size_t)mt * 20480);
    }
    __builtin_amdgcn_sched_barrier(0);
    for (u32 spins = 0;; ++spins) {
      u32 mxv = 0;
      MFMA_A0(acc0, b0[0], a[0]); MFMA_A0(acc1, b1[0], a[0]);
      mxv = max(max(a[0].x, a[0].y), max(a[0].z, a[0].w));
#pragma unroll
      for (int i = 1; i < 32; ++i) {
        MFMA_A(acc0, b0[i], a[i]); MFMA_A(acc1, b1[i], a[i]);
        mxv = max(max(mxv, max(a[i].x, a[i].y)), max(a[i].z, a[i].w));
      }
      if (sg < HINT0 || !__builtin_amdgcn_ballot_w64(mxv == SENT)) break;
      if (spins > (1u << 16)) { *(u32*)(ws + OFF_ERR) = 8; break; }
      asm volatile("" ::: "memory");
#pragma unroll
      for (int i = 0; i < 32; ++i) a[i] = __builtin_amdgcn_raw_buffer_load_b128(rof, abase, ((i + rot) & 31) * 1024, 16);
      __builtin_amdgcn_sched_barrier(0);
    }
    asm volatile("s_nop 7\n\ts_nop 7" : "+v"(acc0), "+v"(acc1));
    LST(sg >= 56 && w == 1, 2 * (sg - 56) + 1);
    __builtin_amdgcn_sched_barrier(0);
    if (pf) { if (sg + 1 < WO_FAST) upd_go(fv.x); else upd_os(fv); }
    char* sb = smem + (sg & 1) * 8192;
    if (w > 0) {
      *(f32x4*)(sb + (w * 2) * 1024 + l * 16) = acc0;
      *(f32x4*)(sb + (w * 2 + 1) * 1024 + l * 16) = acc1;
    }
  }
  RAWBAR();
  if (w == 0) fin(63);
}

__device__ __forceinline__ void reduce0(char* __restrict__ ws, char* smem, const int np) {
  const int l = threadIdx.x & 63, c = l & 15, g = l >> 4;
  wait_flags<2>(ws, OFF_O0F + np * 128, 16, (u32)(OFF_O0F + np * 128 + 128));
  WSTAMPJ(0);
  f32x4 a[32];
#pragma unroll
  for (int i = 0; i < 32; ++i)
    a[i] = __builtin_bit_cast(f32x4, __builtin_amdgcn_raw_buffer_load_b128(__builtin_amdgcn_make_buffer_rsrc(ws + OFF_O0, 0, 20 * 32 * 1024, 0x00020000), (u32)(np * 32768 + l * 16), i * 1024, 16));
#pragma unroll
  for (int d = 1; d < 32; d *= 2)
#pragma unroll
    for (int i = 0; i < 32; i += 2 * d) a[i] += a[i + d];
  char* pl = smem + (g >> 1) * 256 + (c & 7) * 16;
  *(u32*)(pl + (2 * (g & 1) + (c >> 3)) * 4) = pack2(a[0].x, a[0].y);
  *(u32*)(pl + 128 + (2 * (g & 1) + (c >> 3)) * 4) = pack2(a[0].z, a[0].w);
  asm volatile("s_waitcnt lgkmcnt(0)" ::: "memory");
  const u32x4 v = *(const u32x4*)(pl + (g & 1) * 128);
  const int blk = np >> 1, gq = 2 * (np & 1) + (g >> 1), sl = g & 1;
  const int pc = blk < 8 ? 20 * (blk >> 1) + 8 * (blk & 1) + 4 * sl + gq : 20 * (2 * (blk - 8) + sl) + 16 + gq;
  const __amdgpu_buffer_rsrc_t rmem = __builtin_amdgcn_make_buffer_rsrc(ws + OFF_MEMB, 0, (u32)SZ_MEMB, 0x00020000);
#pragma unroll
  for (u32 rp = 0; rp < MREP; rp += 2)
    __builtin_amdgcn_raw_buffer_store_b128(v, rmem, (u32)((c >> 3) * MEM_REP + pc * 128 + (c & 7) * 16), MEM_STEP + rp * MEM_REP, 16);
  WSTAMPJ(1);
}

template <typename T> __device__ __forceinline__ T* opq(T* p) { asm volatile("" : "+s"(p)); return p; }

__global__ __launch_bounds__(256, 1) void main_kernel(const u16* __restrict__ x, const u16* __restrict__ Wq, const u16* __restrict__ Wk,
                                                      const u16* __restrict__ Wv, const u16* __restrict__ Wo, const u16* __restrict__ wg,
                                                      const u16* __restrict__ wu, const u16* __restrict__ wd, char* __restrict__ ws,
                                                      u16* __restrict__ out) {
  extern __shared__ __attribute__((aligned(16))) char smem[];
  const int b = blockIdx.x;
  if (b < 128) {
    chain_role(Wq, Wk, Wv, Wo, ws, smem, b);
  } else {
    const int j = b - 128;
    if (j < 32) {
      worker_kv(Wk, Wv, ws, smem, HMAP == 1 ? 4 * (j & 7) + (j >> 3) : (HMAP == 3 ? 8 * ((j & 7) >> 1) + 2 * (j >> 3) + (j & 1) : j));
    } else {
      if (j >= 64) {
        const int hh = HMAP == 3 ? 8 * ((j & 7) >> 1) + 2 * ((j >> 3) & 3) + (j & 1) : 4 * (j & 7) + ((j >> 3) & 3);
        if (j < 96) kv_helper<0>(Wk, ws, smem, hh); else kv_helper<1>(Wv, ws, smem, hh);
        if (FS && j >= 84 && j < 104 && threadIdx.x < 64) reduce0(ws, smem, j - 84);
        __builtin_amdgcn_s_sleep(R0_SLP);
        __syncthreads();
      }
      if (j < 64) worker_qattn(Wq, Wo, ws, smem, HMAP == 1 ? 4 * (j & 7) + ((j >> 3) & 3) : (HMAP == 3 ? 8 * ((j & 7) >> 1) + 2 * ((j >> 3) & 3) + (j & 1) : j - 32));
      else if (j < 104) worker_wo_fine(x, Wo, ws, out, smem, (j - 64) / 20, (j - 64) % 20, 0, 1);
      else {
        worker_modA(wg, wu, ws, smem, j - 104, 24);
        for (int blk = j - 104; blk < 80; blk += 24) { __syncthreads(); worker_modB(opq(wd), opq(ws), smem, blk); }
        WSTAMP(2);
      }
    }
    WSTAMP(15);
  }
}

extern "C" void kernel_launch(void* const* d_in, const int* in_sizes, int n_in, void* d_out, int out_size, void* d_ws, size_t ws_size,
                              hipStream_t stream) {
  const u16* x = (const u16*)d_in[0];
  const u16* mem0 = (const u16*)d_in[1];
  const u16* Wq = (const u16*)d_in[2];
  const u16* Wk = (const u16*)d_in[3];
  const u16* Wv = (const u16*)d_in[4];
  const u16* Wo = (const u16*)d_in[5];
  const u16* w_r = (const u16*)d_in[8];
  const u16* wg = (const u16*)d_in[9];
  const u16* wu = (const u16*)d_in[10];
  const u16* wd = (const u16*)d_in[11];
  char* ws = (char*)d_ws;
  u16* out = (u16*)d_out;
  (void)hipFuncSetAttribute((const void*)main_kernel, hipFuncAttributeMaxDynamicSharedMemorySize, CH_LDS);
  (void)hipFuncSetAttribute((const void*)prep_kernel, hipFuncAttributeMaxDynamicSharedMemorySize, PREP_LDS);
  prep_kernel<<<PREP_BLOCKS, 512, PREP_LDS, stream>>>(x, mem0, w_r, Wq, Wk, Wv, Wo, ws);
  main_kernel<<<256, 256, CH_LDS, stream>>>(x, Wq, Wk, Wv, Wo, wg, wu, wd, ws, out);
}
```
